```python
import torch

def _softmax(x, axis=-1):
    return torch.softmax(x, dim=axis)
def _gelu(x):
    return torch.nn.functional.gelu(x, approximate="tanh")
def _silu(x):
    return torch.nn.functional.silu(x)
def _sigmoid(x):
    return torch.sigmoid(x)
def _layernorm(x, axis=-1, eps=1e-5):
    m = x.mean(dim=axis, keepdim=True)
    v = x.var(dim=axis, keepdim=True, unbiased=False)
    return (x - m) / torch.sqrt(v + eps)
def _scatter_add(tgt, idx, src, axis=0):
    return tgt.clone().index_add_(axis, idx, src)
def _segment_sum(src, seg, n):
    out = torch.zeros((n,) + src.shape[1:], dtype=src.dtype)
    return out.index_add_(0, seg, src)
def _pad(x, pw):
    flat = tuple(v for pair in reversed(pw) for v in pair)
    return torch.nn.functional.pad(x, flat)

kernel_name = 'recurrent_scan__gqa_block_cached__mixer_channel_mlp__prompt'

def setup_inputs():

    _w = (lambda a: (lambda t: t.to(torch.bfloat16) if t.is_floating_point() else t)(torch.as_tensor(a)))
    def _A_setup():
        B, Sq, D = 1, 2048, 448
        Hq, Hkv, Sk, Dh = 16, 8, 2048, 256
        assert Hq % Hkv == 0, (Hq, Hkv)
        return {
            "x": _w((lambda *s: torch.randn(*s, dtype=torch.bfloat16) * 0.05)(B, Sq, D)),
            "k_cache": _w((lambda *s: torch.randn(*s, dtype=torch.bfloat16) * 1.6)(B, Hkv, Sk, Dh)),
            "v_cache": _w((lambda *s: torch.randn(*s, dtype=torch.bfloat16) * 0.8)(B, Hkv, Sk, Dh)),
            "wq": _w((lambda *s: torch.randn(*s, dtype=torch.bfloat16) * 1.6)(D, Hq * Dh) * (D ** -0.5)),
            "wo": _w((lambda *s: torch.randn(*s, dtype=torch.bfloat16) * 0.8)(Hq * Dh, D) * ((Hq * Dh) ** -0.5)),
        }
    _A_in = _A_setup()

    def _B_setup():
        B, S, D, F = 1, 2048, 448, 1792
        return {
            "x": _w((lambda *s: torch.randn(*s, dtype=torch.bfloat16) * 0.05)(B, S, D)),
            "w1": _w((lambda *s: torch.randn(*s, dtype=torch.bfloat16) * 1.6)(D, F) * (D ** -0.5)),
            "b1": _w((lambda *s: torch.randn(*s, dtype=torch.bfloat16) * 0.8)(F)),
            "w2": _w((lambda *s: torch.randn(*s, dtype=torch.bfloat16) * 0.2)(F, D) * (F ** -0.5)),
            "b2": _w((lambda *s: torch.randn(*s, dtype=torch.bfloat16) * 0.2)(D)),
        }
    _B_in = _B_setup()

    return {
        "A_x": _A_in["x"],
        "A_k_cache": _A_in["k_cache"],
        "A_v_cache": _A_in["v_cache"],
        "A_wq": _A_in["wq"],
        "A_wo": _A_in["wo"],
        "B_x": _B_in["x"],
        "B_w1": _B_in["w1"],
        "B_b1": _B_in["b1"],
        "B_w2": _B_in["w2"],
        "B_b2": _B_in["b2"],
    }

def reference(A_x, A_k_cache, A_v_cache, A_wq, A_wo, B_x, B_w1, B_b1, B_w2, B_b2):
    def _A_ref(x, k_cache, v_cache, wq, wo):
        B, Sq, D = x.shape
        Hq, Dh = 16, 256
        Hkv = k_cache.shape[1]
        rep = Hq // Hkv
        q = (lambda x, *d: x.permute(*d))((x @ wq).reshape(B, Sq, Hq, Dh), 0, 2, 1, 3)
        k = (lambda x, n, axis: torch.repeat_interleave(x, n, dim=axis))(k_cache, rep, axis=1)
        v = (lambda x, n, axis: torch.repeat_interleave(x, n, dim=axis))(v_cache, rep, axis=1)
        scores = torch.einsum("bhqd,bhkd->bhqk", q, k) * (Dh ** -0.5)
        p = _softmax(scores, axis=-1)
        o = torch.einsum("bhqk,bhkd->bhqd", p, v)
        return (lambda x, *d: x.permute(*d))(o, 0, 2, 1, 3).reshape(B, Sq, Hq * Dh) @ wo

    def _B_ref(x, w1, b1, w2, b2):
        y = _layernorm(x, axis=-1)
        h = _gelu(torch.einsum("bsd,df->bsf", y, w1) + b1)
        return torch.einsum("bsf,fd->bsd", h, w2) + b2

    x = A_x
    xn = _layernorm(x, axis=-1)
    s = _A_ref(xn, A_k_cache, A_v_cache, A_wq, A_wo)
    g = _silu(_B_ref(xn, B_w1, B_b1, B_w2, B_b2))
    return x + s * g
```

```cpp
#include <hip/hip_runtime.h>
#include <hip/hip_bf16.h>
typedef __hip_bfloat16 bf16;
static __device__ __forceinline__ float b2f(bf16 x){ return __bfloat162float(x); }

__global__ void ln_kernel(const bf16* x, float* xn){
  int row = blockIdx.x; int lane = threadIdx.x;
  float v[7]; float s=0;
  for(int i=0;i<7;i++){ v[i]=b2f(x[row*448+lane+64*i]); s+=v[i]; }
  for(int o=32;o>0;o>>=1) s+=__shfl_xor(s,o);
  float m=s/448.f; float q=0;
  for(int i=0;i<7;i++){ float d=v[i]-m; q+=d*d; }
  for(int o=32;o>0;o>>=1) q+=__shfl_xor(q,o);
  float r=1.f/sqrtf(q/448.f+1e-5f);
  for(int i=0;i<7;i++) xn[row*448+lane+64*i]=(v[i]-m)*r;
}
template<bool TB>
__global__ void gemm(const float* A,int lda,const bf16* B,int ldb,float* C,int ldc,int K,float alpha){
  __shared__ float As[32][33], Bs[32][33];
  int tx=threadIdx.x&31, ty=threadIdx.x>>5;
  int m0=blockIdx.y*32, n0=blockIdx.x*32;
  float acc[4]={0,0,0,0};
  for(int k0=0;k0<K;k0+=32){
    for(int i=0;i<4;i++){ int r=ty+8*i; As[r][tx]=A[(size_t)(m0+r)*lda+k0+tx];
      if(TB) Bs[tx][r]=b2f(B[(size_t)(n0+r)*ldb+k0+tx]); else Bs[r][tx]=b2f(B[(size_t)(k0+r)*ldb+n0+tx]); }
    __syncthreads();
    for(int k=0;k<32;k++){ float b=Bs[k][tx]; for(int i=0;i<4;i++) acc[i]+=As[ty+8*i][k]*b; }
    __syncthreads();
  }
  for(int i=0;i<4;i++) C[(size_t)(m0+ty+8*i)*ldc+n0+tx]=acc[i]*alpha;
}
__global__ void softmax_rows(float* S){
  float* p=S+(size_t)blockIdx.x*2048; int t=threadIdx.x; __shared__ float red[256];
  float m=-1e30f; for(int i=t;i<2048;i+=256) m=fmaxf(m,p[i]);
  red[t]=m; __syncthreads(); for(int o=128;o>0;o>>=1){ if(t<o) red[t]=fmaxf(red[t],red[t+o]); __syncthreads(); }
  m=red[0]; __syncthreads();
  float s=0; for(int i=t;i<2048;i+=256){ float e=__expf(p[i]-m); p[i]=e; s+=e; }
  red[t]=s; __syncthreads(); for(int o=128;o>0;o>>=1){ if(t<o) red[t]+=red[t+o]; __syncthreads(); }
  s=1.f/red[0];
  for(int i=t;i<2048;i+=256) p[i]*=s;
}
__global__ void gelu_bias(float* h,const bf16* b1){
  int i=blockIdx.x*256+threadIdx.x; float v=h[i]+b2f(b1[i%1792]);
  float u=0.7978845608028654f*(v+0.044715f*v*v*v); h[i]=0.5f*v*(1.f+tanhf(u));
}
__global__ void final_k(const bf16* x,const float* s,const float* y,const bf16* b2,bf16* out){
  int i=blockIdx.x*256+threadIdx.x; float v=y[i]+b2f(b2[i%448]); float g=v/(1.f+__expf(-v));
  out[i]=__float2bfloat16(b2f(x[i])+s[i]*g);
}
extern "C" void kernel_launch(void* const* d_in, const int* in_sizes, int n_in, void* d_out, int out_size, void* d_ws, size_t ws_size, hipStream_t stream){
  const bf16* x=(const bf16*)d_in[0]; const bf16* k=(const bf16*)d_in[1]; const bf16* v=(const bf16*)d_in[2];
  const bf16* wq=(const bf16*)d_in[3]; const bf16* wo=(const bf16*)d_in[4];
  const bf16* w1=(const bf16*)d_in[6]; const bf16* b1=(const bf16*)d_in[7]; const bf16* w2=(const bf16*)d_in[8]; const bf16* b2=(const bf16*)d_in[9];
  float* ws=(float*)d_ws;
  float* xn=ws; float* q=xn+2048*448; float* o=q+2048*4096; float* S=o+2048*4096; float* h=S+2048*2048; float* y=h+2048*1792; float* s=y+2048*448;
  ln_kernel<<<2048,64,0,stream>>>(x,xn);
  gemm<false><<<dim3(4096/32,2048/32),256,0,stream>>>(xn,448,wq,4096,q,4096,448,1.f);
  for(int hh=0;hh<16;hh++){
    gemm<true><<<dim3(64,64),256,0,stream>>>(q+hh*256,4096,k+(size_t)(hh/2)*2048*256,256,S,2048,256,0.0625f);
    softmax_rows<<<2048,256,0,stream>>>(S);
    gemm<false><<<dim3(8,64),256,0,stream>>>(S,2048,v+(size_t)(hh/2)*2048*256,256,o+hh*256,4096,2048,1.f);
  }
  gemm<false><<<dim3(14,64),256,0,stream>>>(o,4096,wo,448,s,448,4096,1.f);
  gemm<false><<<dim3(56,64),256,0,stream>>>(xn,448,w1,1792,h,1792,448,1.f);
  gelu_bias<<<2048*1792/256,256,0,stream>>>(h,b1);
  gemm<false><<<dim3(14,64),256,0,stream>>>(h,1792,w2,448,y,448,1792,1.f);
  final_k<<<2048*448/256,256,0,stream>>>(x,s,y,b2,(bf16*)d_out);
}
```

```cpp
#include <hip/hip_runtime.h>
#include <hip/hip_bf16.h>

__global__ __launch_bounds__(256, 1) void fused_main_kernel(const void* x, const void* kc, const void* vc, const void* wq, const void* wo,
    const void* w1, const void* b1, const void* w2, const void* b2, void* out, void* ws, void* dbg) {
  __shared__ __attribute__((aligned(16))) char lds[163840];
  auto ka = __builtin_amdgcn_kernarg_segment_ptr();
  unsigned wg = blockIdx.x;
  unsigned wv = __builtin_amdgcn_readfirstlane(threadIdx.x >> 6);
  asm volatile("" :: "v"((unsigned)(size_t)lds));
  asm volatile(
    "s_load_dwordx16 s[16:31], %0, 0\n"
    "s_load_dwordx4 s[32:35], %0, 64\n"
    "s_load_dwordx4 s[36:39], %0, 80\n"
    "s_mov_b32 s68, %1\n"
    "s_mov_b32 s69, %2\n"
    "v_mbcnt_lo_u32_b32 v244, -1, 0\n"
    "v_mbcnt_hi_u32_b32 v244, -1, v244\n"
    "s_and_b32 s70, s68, 6\n"
    "s_bfe_u32 s86, s68, 65539\n"
    "s_or_b32 s70, s70, s86\n"
    "s_and_b32 s71, s68, 1\n"
    "s_lshl_b32 s71, s71, 4\n"
    "s_lshr_b32 s86, s68, 4\n"
    "s_or_b32 s71, s71, s86\n"
    "s_and_b32 s72, s69, 1\n"
    "s_lshr_b32 s73, s69, 1\n"
    "s_mul_i32 s74, s69, 8448\n"
    "s_lshl_b32 s100, s69, 12\n"
    "s_lshl_b32 s90, s69, 13\n"
    "s_getpc_b64 s[86:87]\n"
    ".Lpc:\n"
    "s_lshr_b32 s88, s68, 3\n"
    "s_lshl_b32 s88, s88, 2\n"
    "s_add_u32 s88, s88, s69\n"
    "s_mul_i32 s88, s88, 384\n"
    "v_min_u32 v245, 2, v244\n"
    "v_lshl_add_u32 v245, v245, 7, s88\n"
    "s_mov_b32 s88, .Ltail-.Lpc-4\n"
    "v_min_u32 v245, s88, v245\n"
    "v_and_b32 v245, -4, v245\n"
    "global_load_dword v230, v245, s[86:87]\n"
    "s_lshl_b32 s86, s71, 6\n"
    "s_lshl_b32 s87, s69, 4\n"
    "s_add_u32 s86, s86, s87\n"
    "s_mul_i32 s86, s86, 896\n"
    "v_lshl_add_u32 v248, v244, 4, s86\n"
    "s_mul_i32 s89, s69, 14592\n"
    "s_add_u32 s89, s89, 101376\n"
    "v_bfe_u32 v245, v244, 4, 2\n"
    "v_lshlrev_b32 v245, 1, v245\n"
    "v_and_b32 v246, 7, v244\n"
    "v_xor_b32 v245, v245, v246\n"
    "v_lshlrev_b32 v250, 4, v245\n"
    "v_add_u32 v250, 3168, v250\n"
    "v_lshrrev_b32 v251, 3, v244\n"
    "s_lshl_b32 s86, s69, 3\n"
    "v_add_u32 v251, s86, v251\n"
    "v_mul_u32_u24 v216, 3584, v251\n"
    "v_add_u32 v216, v216, v250\n"
    "v_add_u32 v217, 0xfffffc80, v216\n"
    "v_add_u32 v218, 0xfffff900, v216\n"
    "v_add_u32 v219, 0xfffff580, v216\n"
    "s_mov_b32 s91, 0\n"
    "s_waitcnt lgkmcnt(0)\n"
    "s_mov_b32 s64, s16\n"
    "s_mov_b32 s65, s17\n"
    "s_and_b32 s65, s65, 0xffff\n"
    "s_mov_b32 s66, 1835008\n"
    "s_mov_b32 s67, 0x20000\n"
    "s_mov_b32 exec_hi, 0x00ffffff\n"
    "s_add_u32 m0, s89, 0\n"
    "s_mov_b32 s75, 0\n"
    "buffer_load_dwordx4 v248, s[64:67], s75 offen lds\n"
    "s_add_u32 m0, s89, 912\n"
    "s_mov_b32 s76, 896\n"
    "buffer_load_dwordx4 v248, s[64:67], s76 offen lds\n"
    "s_add_u32 m0, s89, 1824\n"
    "s_mov_b32 s75, 1792\n"
    "buffer_load_dwordx4 v248, s[64:67], s75 offen lds\n"
    "s_add_u32 m0, s89, 2736\n"
    "s_mov_b32 s76, 2688\n"
    "buffer_load_dwordx4 v248, s[64:67], s76 offen lds\n"
    "s_add_u32 m0, s89, 3648\n"
    "s_mov_b32 s75, 3584\n"
    "buffer_load_dwordx4 v248, s[64:67], s75 offen lds\n"
    "s_add_u32 m0, s89, 4560\n"
    "s_mov_b32 s76, 4480\n"
    "buffer_load_dwordx4 v248, s[64:67], s76 offen lds\n"
    "s_add_u32 m0, s89, 5472\n"
    "s_mov_b32 s75, 5376\n"
    "buffer_load_dwordx4 v248, s[64:67], s75 offen lds\n"
    "s_add_u32 m0, s89, 6384\n"
    "s_mov_b32 s76, 6272\n"
    "buffer_load_dwordx4 v248, s[64:67], s76 offen lds\n"
    "s_add_u32 m0, s89, 7296\n"
    "s_mov_b32 s75, 7168\n"
    "buffer_load_dwordx4 v248, s[64:67], s75 offen lds\n"
    "s_add_u32 m0, s89, 8208\n"
    "s_mov_b32 s76, 8064\n"
    "buffer_load_dwordx4 v248, s[64:67], s76 offen lds\n"
    "s_add_u32 m0, s89, 9120\n"
    "s_mov_b32 s75, 8960\n"
    "buffer_load_dwordx4 v248, s[64:67], s75 offen lds\n"
    "s_add_u32 m0, s89, 10032\n"
    "s_mov_b32 s76, 9856\n"
    "buffer_load_dwordx4 v248, s[64:67], s76 offen lds\n"
    "s_add_u32 m0, s89, 10944\n"
    "s_mov_b32 s75, 10752\n"
    "buffer_load_dwordx4 v248, s[64:67], s75 offen lds\n"
    "s_add_u32 m0, s89, 11856\n"
    "s_mov_b32 s76, 11648\n"
    "buffer_load_dwordx4 v248, s[64:67], s76 offen lds\n"
    "s_add_u32 m0, s89, 12768\n"
    "s_mov_b32 s75, 12544\n"
    "buffer_load_dwordx4 v248, s[64:67], s75 offen lds\n"
    "s_add_u32 m0, s89, 13680\n"
    "s_mov_b32 s76, 13440\n"
    "buffer_load_dwordx4 v248, s[64:67], s76 offen lds\n"
    "s_mov_b64 exec, -1\n"
    "s_mul_i32 s86, s70, 448\n"
    "s_and_b32 s86, s86, 0xffffff80\n"
    "s_add_u32 s40, s26, s86\n"
    "s_addc_u32 s41, s27, 0\n"
    "s_sub_u32 s40, s40, 3168\n"
    "s_subb_u32 s41, s41, 0\n"
    "s_and_b32 s41, s41, 0xffff\n"
    "s_mov_b32 s42, 1608800\n"
    "s_mov_b32 s43, 0x20000\n"
    "s_sub_u32 s42, s42, s86\n"
    "v_mov_b32 v247, 0x80000000\n"
    "s_mul_i32 s86, s69, 28\n"
    "v_add_u32 v245, s86, v244\n"
    "v_lshrrev_b32 v246, 2, v245\n"
    "v_and_b32 v245, 3, v245\n"
    "s_and_b32 s86, s71, 15\n"
    "s_mul_i32 s86, s86, 28\n"
    "v_add_u32 v246, s86, v246\n"
    "v_mul_u32_u24 v246, 3584, v246\n"
    "v_lshl_add_u32 v245, v245, 7, v246\n"
    "v_add_u32 v245, 3168, v245\n"
    "v_cmp_gt_u32 vcc, 28, v244\n"
    "s_nop 1\n"
    "v_cndmask_b32 v245, v247, v245, vcc\n"
    "buffer_load_dword v231, v245, s[40:43], 0 offen\n"
    "s_add_u32 m0, s100, 65536\n"
    "s_mov_b32 s75, 0\n"
    "buffer_load_dwordx4 v216, s[40:43], s75 offen offset:0 lds\n"
    "buffer_load_dwordx4 v217, s[40:43], s75 offen offset:1024 lds\n"
    "buffer_load_dwordx4 v218, s[40:43], s75 offen offset:2048 lds\n"
    "buffer_load_dwordx4 v219, s[40:43], s75 offen offset:3072 lds\n"
    "s_add_u32 m0, s100, 81920\n"
    "s_mov_b32 s76, 114688\n"
    "buffer_load_dwordx4 v216, s[40:43], s76 offen offset:0 lds\n"
    "buffer_load_dwordx4 v217, s[40:43], s76 offen offset:1024 lds\n"
    "buffer_load_dwordx4 v218, s[40:43], s76 offen offset:2048 lds\n"
    "buffer_load_dwordx4 v219, s[40:43], s76 offen offset:3072 lds\n"
    "v_lshrrev_b32 v246, 4, v244\n"
    "s_mul_i32 s86, s70, 448\n"
    "s_mul_i32 s87, s69, 96\n"
    "s_add_u32 s87, s86, s87\n"
    "v_lshl_add_u32 v247, v246, 3, s87\n"
    "s_lshl_b32 s87, s72, 5\n"
    "s_add_u32 s86, s86, s87\n"
    "s_add_u32 s86, s86, 384\n"
    "v_lshl_add_u32 v246, v246, 3, s86\n"
    "global_load_dwordx2 v[184:185], v247, s[28:29] offset:0\n"
    "global_load_dwordx2 v[186:187], v247, s[28:29] offset:32\n"
    "global_load_dwordx2 v[188:189], v247, s[28:29] offset:64\n"
    "global_load_dwordx2 v[190:191], v246, s[28:29]\n"
    "v_mul_u32_u24 v240, 8192, v251\n"
    "v_add_u32 v240, v240, v250\n"
    "v_add_u32 v241, 0xfffffc80, v240\n"
    "v_add_u32 v242, 0xfffff900, v240\n"
    "v_add_u32 v243, 0xfffff580, v240\n"
    "v_mul_u32_u24 v120, 896, v251\n"
    "v_add_u32 v120, v120, v250\n"
    "v_add_u32 v121, 0xfffffc80, v120\n"
    "v_add_u32 v122, 0xfffff900, v120\n"
    "v_add_u32 v123, 0xfffff580, v120\n"
    "v_bfe_u32 v245, v244, 3, 2\n"
    "v_lshrrev_b32 v246, 5, v244\n"
    "v_mul_u32_u24 v246, 4096, v246\n"
    "v_bfe_u32 v247, v244, 2, 3\n"
    "v_lshl_add_u32 v246, v247, 7, v246\n"
    "v_and_b32 v247, 3, v244\n"
    "v_lshl_add_u32 v246, v247, 3, v246\n"
    "s_mul_i32 s86, s69, 3\n"
    "s_add_u32 s86, s86, 0\n"
    "s_and_b32 s87, s70, 1\n"
    "s_lshl_b32 s87, s87, 1\n"
    "s_add_u32 s86, s86, s87\n"
    "s_and_b32 s87, s86, 3\n"
    "s_lshr_b32 s86, s86, 2\n"
    "s_lshl_b32 s86, s86, 10\n"
    "v_xor_b32 v247, s87, v245\n"
    "v_lshl_add_u32 v208, v247, 5, v246\n"
    "v_add_u32 v208, s86, v208\n"
    "v_add_u32 v212, 0x10000, v208\n"
    "s_mul_i32 s86, s69, 3\n"
    "s_add_u32 s86, s86, 1\n"
    "s_and_b32 s87, s70, 1\n"
    "s_lshl_b32 s87, s87, 1\n"
    "s_add_u32 s86, s86, s87\n"
    "s_and_b32 s87, s86, 3\n"
    "s_lshr_b32 s86, s86, 2\n"
    "s_lshl_b32 s86, s86, 10\n"
    "v_xor_b32 v247, s87, v245\n"
    "v_lshl_add_u32 v209, v247, 5, v246\n"
    "v_add_u32 v209, s86, v209\n"
    "v_add_u32 v213, 0x10000, v209\n"
    "s_mul_i32 s86, s69, 3\n"
    "s_add_u32 s86, s86, 2\n"
    "s_and_b32 s87, s70, 1\n"
    "s_lshl_b32 s87, s87, 1\n"
    "s_add_u32 s86, s86, s87\n"
    "s_and_b32 s87, s86, 3\n"
    "s_lshr_b32 s86, s86, 2\n"
    "s_lshl_b32 s86, s86, 10\n"
    "v_xor_b32 v247, s87, v245\n"
    "v_lshl_add_u32 v210, v247, 5, v246\n"
    "v_add_u32 v210, s86, v210\n"
    "v_add_u32 v214, 0x10000, v210\n"
    "s_add_u32 s86, s72, 12\n"
    "s_and_b32 s87, s70, 1\n"
    "s_lshl_b32 s87, s87, 1\n"
    "s_add_u32 s86, s86, s87\n"
    "s_and_b32 s87, s86, 3\n"
    "s_lshr_b32 s86, s86, 2\n"
    "s_lshl_b32 s86, s86, 10\n"
    "v_xor_b32 v247, s87, v245\n"
    "v_lshl_add_u32 v211, v247, 5, v246\n"
    "v_add_u32 v211, s86, v211\n"
    "v_add_u32 v215, 0x10000, v211\n"
    "v_lshrrev_b32 v246, 5, v244\n"
    "v_mul_u32_u24 v246, 8192, v246\n"
    "v_bfe_u32 v247, v244, 2, 3\n"
    "v_lshl_add_u32 v246, v247, 7, v246\n"
    "v_and_b32 v247, 1, v244\n"
    "v_lshl_add_u32 v246, v247, 4, v246\n"
    "v_bfe_u32 v247, v244, 1, 1\n"
    "v_lshl_add_u32 v246, v247, 3, v246\n"
    "s_lshl_b32 s86, s72, 4\n"
    "s_lshl_b32 s87, s73, 3\n"
    "s_add_u32 s86, s86, s87\n"
    "s_add_u32 s86, s86, 0\n"
    "s_and_b32 s87, s86, 3\n"
    "s_lshr_b32 s86, s86, 2\n"
    "s_lshl_b32 s86, s86, 10\n"
    "v_xor_b32 v247, s87, v245\n"
    "v_lshl_add_u32 v232, v247, 5, v246\n"
    "v_add_u32 v232, s86, v232\n"
    "v_add_u32 v236, 0x10000, v232\n"
    "s_lshl_b32 s86, s72, 4\n"
    "s_lshl_b32 s87, s73, 3\n"
    "s_add_u32 s86, s86, s87\n"
    "s_add_u32 s86, s86, 1\n"
    "s_and_b32 s87, s86, 3\n"
    "s_lshr_b32 s86, s86, 2\n"
    "s_lshl_b32 s86, s86, 10\n"
    "v_xor_b32 v247, s87, v245\n"
    "v_lshl_add_u32 v233, v247, 5, v246\n"
    "v_add_u32 v233, s86, v233\n"
    "v_add_u32 v237, 0x10000, v233\n"
    "s_lshl_b32 s86, s72, 4\n"
    "s_lshl_b32 s87, s73, 3\n"
    "s_add_u32 s86, s86, s87\n"
    "s_add_u32 s86, s86, 2\n"
    "s_and_b32 s87, s86, 3\n"
    "s_lshr_b32 s86, s86, 2\n"
    "s_lshl_b32 s86, s86, 10\n"
    "v_xor_b32 v247, s87, v245\n"
    "v_lshl_add_u32 v234, v247, 5, v246\n"
    "v_add_u32 v234, s86, v234\n"
    "v_add_u32 v238, 0x10000, v234\n"
    "s_lshl_b32 s86, s72, 4\n"
    "s_lshl_b32 s87, s73, 3\n"
    "s_add_u32 s86, s86, s87\n"
    "s_add_u32 s86, s86, 3\n"
    "s_and_b32 s87, s86, 3\n"
    "s_lshr_b32 s86, s86, 2\n"
    "s_lshl_b32 s86, s86, 10\n"
    "v_xor_b32 v247, s87, v245\n"
    "v_lshl_add_u32 v235, v247, 5, v246\n"
    "v_add_u32 v235, s86, v235\n"
    "v_add_u32 v239, 0x10000, v235\n"
    "v_lshrrev_b32 v246, 5, v244\n"
    "v_mul_u32_u24 v246, 8192, v246\n"
    "v_bfe_u32 v247, v244, 2, 3\n"
    "v_lshl_add_u32 v246, v247, 7, v246\n"
    "v_and_b32 v247, 3, v244\n"
    "v_lshl_add_u32 v246, v247, 3, v246\n"
    "s_mul_i32 s86, s69, 7\n"
    "s_add_u32 s86, s86, 0\n"
    "s_and_b32 s87, s86, 3\n"
    "s_lshr_b32 s86, s86, 2\n"
    "s_lshl_b32 s86, s86, 10\n"
    "v_xor_b32 v247, s87, v245\n"
    "v_lshl_add_u32 v112, v247, 5, v246\n"
    "v_add_u32 v112, s86, v112\n"
    "v_add_u32 v116, 0x10000, v112\n"
    "s_mul_i32 s86, s69, 7\n"
    "s_add_u32 s86, s86, 1\n"
    "s_and_b32 s87, s86, 3\n"
    "s_lshr_b32 s86, s86, 2\n"
    "s_lshl_b32 s86, s86, 10\n"
    "v_xor_b32 v247, s87, v245\n"
    "v_lshl_add_u32 v113, v247, 5, v246\n"
    "v_add_u32 v113, s86, v113\n"
    "v_add_u32 v117, 0x10000, v113\n"
    "s_mul_i32 s86, s69, 7\n"
    "s_add_u32 s86, s86, 2\n"
    "s_and_b32 s87, s86, 3\n"
    "s_lshr_b32 s86, s86, 2\n"
    "s_lshl_b32 s86, s86, 10\n"
    "v_xor_b32 v247, s87, v245\n"
    "v_lshl_add_u32 v114, v247, 5, v246\n"
    "v_add_u32 v114, s86, v114\n"
    "v_add_u32 v118, 0x10000, v114\n"
    "s_mul_i32 s86, s69, 7\n"
    "s_add_u32 s86, s86, 3\n"
    "s_and_b32 s87, s86, 3\n"
    "s_lshr_b32 s86, s86, 2\n"
    "s_lshl_b32 s86, s86, 10\n"
    "v_xor_b32 v247, s87, v245\n"
    "v_lshl_add_u32 v115, v247, 5, v246\n"
    "v_add_u32 v115, s86, v115\n"
    "v_add_u32 v119, 0x10000, v115\n"
    "s_lshl_b32 s86, s73, 10\n"
    "s_add_u32 s87, s86, 131072\n"
    "v_lshl_add_u32 v124, v244, 4, s87\n"
    "s_sub_u32 s87, 131072, s86\n"
    "v_lshl_add_u32 v125, v244, 4, s87\n"
    "s_xor_b32 s86, s69, 0\n"
    "s_mul_i32 s86, s86, 14592\n"
    "s_add_u32 s86, s86, 101376\n"
    "v_lshl_add_u32 v220, v244, 4, s86\n"
    "s_xor_b32 s86, s69, 1\n"
    "s_mul_i32 s86, s86, 14592\n"
    "s_add_u32 s86, s86, 101376\n"
    "v_lshl_add_u32 v221, v244, 4, s86\n"
    "s_xor_b32 s86, s69, 2\n"
    "s_mul_i32 s86, s86, 14592\n"
    "s_add_u32 s86, s86, 101376\n"
    "v_lshl_add_u32 v222, v244, 4, s86\n"
    "s_xor_b32 s86, s69, 3\n"
    "s_mul_i32 s86, s86, 14592\n"
    "s_add_u32 s86, s86, 101376\n"
    "v_lshl_add_u32 v223, v244, 4, s86\n"
    "v_and_b32 v245, 31, v244\n"
    "v_lshrrev_b32 v246, 5, v244\n"
    "s_lshl_b32 s86, s69, 12\n"
    "v_lshlrev_b32 v126, 14, v246\n"
    "v_lshl_add_u32 v126, v245, 4, v126\n"
    "v_add_u32 v126, s86, v126\n"
    "s_mov_b32 s77, 3168\n"
    "s_mov_b32 s78, 2624\n"
    "s_mov_b32 s79, 2080\n"
    "s_mov_b32 s80, 1536\n"
    "s_mov_b32 s81, 5216\n"
    "s_mov_b32 s82, 4672\n"
    "s_mov_b32 s83, 4128\n"
    "s_mov_b32 s84, 3584\n"
    "s_lshl_b32 s86, s70, 10\n"
    "s_add_u32 s44, s22, s86\n"
    "s_addc_u32 s45, s23, 0\n"
    "s_sub_u32 s44, s44, 3168\n"
    "s_subb_u32 s45, s45, 0\n"
    "s_and_b32 s45, s45, 0xffff\n"
    "s_mov_b32 s46, 3673184\n"
    "s_mov_b32 s47, 0x20000\n"
    "s_sub_u32 s46, s46, s86\n"
    "s_mul_i32 s86, s70, 200704\n"
    "s_add_u32 s48, s30, s86\n"
    "s_addc_u32 s49, s31, 0\n"
    "s_sub_u32 s48, s48, 3168\n"
    "s_subb_u32 s49, s49, 0\n"
    "s_and_b32 s49, s49, 0xffff\n"
    "s_mov_b32 s50, 1608800\n"
    "s_mov_b32 s51, 0x20000\n"
    "s_sub_u32 s50, s50, s86\n"
    "s_lshl_b32 s86, s70, 20\n"
    "s_add_u32 s52, s18, s86\n"
    "s_addc_u32 s53, s19, 0\n"
    "s_sub_u32 s52, s52, 3168\n"
    "s_subb_u32 s53, s53, 0\n"
    "s_and_b32 s53, s53, 0xffff\n"
    "s_mov_b32 s54, 8391776\n"
    "s_mov_b32 s55, 0x20000\n"
    "s_sub_u32 s54, s54, s86\n"
    "s_add_u32 s56, s20, s86\n"
    "s_addc_u32 s57, s21, 0\n"
    "s_sub_u32 s56, s56, 3168\n"
    "s_subb_u32 s57, s57, 0\n"
    "s_and_b32 s57, s57, 0xffff\n"
    "s_mov_b32 s58, 8391776\n"
    "s_mov_b32 s59, 0x20000\n"
    "s_sub_u32 s58, s58, s86\n"
    "s_mul_i32 s86, s70, 458752\n"
    "s_add_u32 s60, s24, s86\n"
    "s_addc_u32 s61, s25, 0\n"
    "s_sub_u32 s60, s60, 3168\n"
    "s_subb_u32 s61, s61, 0\n"
    "s_and_b32 s61, s61, 0xffff\n"
    "s_mov_b32 s62, 3673184\n"
    "s_mov_b32 s63, 0x20000\n"
    "s_sub_u32 s62, s62, s86\n"
    "v_mov_b32 v247, 0x80000000\n"
    "s_mul_i32 s86, s69, 56\n"
    "v_add_u32 v192, s86, v244\n"
    "v_lshrrev_b32 v246, 3, v192\n"
    "v_and_b32 v192, 7, v192\n"
    "s_and_b32 s86, s71, 15\n"
    "s_mul_i32 s86, s86, 28\n"
    "v_add_u32 v246, s86, v246\n"
    "v_lshlrev_b32 v246, 13, v246\n"
    "v_lshl_add_u32 v192, v192, 7, v246\n"
    "v_add_u32 v192, 3168, v192\n"
    "v_cmp_gt_u32 vcc, 56, v244\n"
    "s_nop 1\n"
    "v_cndmask_b32 v192, v247, v192, vcc\n"
    "s_and_b32 s86, s71, 15\n"
    "s_mul_i32 s86, s86, 98\n"
    "s_mul_i32 s87, s69, 25\n"
    "s_add_u32 s86, s86, s87\n"
    "v_add_u32 v193, s86, v244\n"
    "v_lshlrev_b32 v193, 7, v193\n"
    "v_add_u32 v193, 3168, v193\n"
    "v_cmp_gt_u32 vcc, 25, v244\n"
    "s_nop 1\n"
    "v_cndmask_b32 v193, v247, v193, vcc\n"
    "s_and_b32 s86, s71, 15\n"
    "s_mul_i32 s86, s86, 80\n"
    "s_mul_i32 s87, s69, 20\n"
    "s_add_u32 s86, s86, s87\n"
    "v_add_u32 v194, s86, v244\n"
    "v_lshlrev_b32 v194, 7, v194\n"
    "v_add_u32 v194, 3168, v194\n"
    "v_cmp_gt_u32 vcc, 20, v244\n"
    "s_nop 1\n"
    "v_cndmask_b32 v194, v247, v194, vcc\n"
    "s_waitcnt vmcnt(13)\n"
    "s_mul_i32 s86, s69, 14592\n"
    "s_add_u32 s86, s86, 101376\n"
    "v_and_b32 v245, 15, v244\n"
    "v_mul_u32_u24 v245, 912, v245\n"
    "v_lshrrev_b32 v246, 4, v244\n"
    "v_lshl_add_u32 v245, v246, 3, v245\n"
    "v_add_u32 v245, s86, v245\n"
    "ds_read2_b64 v[128:131], v245 offset0:0 offset1:4\n"
    "ds_read2_b64 v[132:135], v245 offset0:8 offset1:12\n"
    "ds_read2_b64 v[136:139], v245 offset0:16 offset1:20\n"
    "ds_read2_b64 v[140:143], v245 offset0:24 offset1:28\n"
    "ds_read2_b64 v[144:147], v245 offset0:32 offset1:36\n"
    "ds_read2_b64 v[148:151], v245 offset0:40 offset1:44\n"
    "ds_read2_b64 v[152:155], v245 offset0:48 offset1:52\n"
    "ds_read2_b64 v[156:159], v245 offset0:56 offset1:60\n"
    "ds_read2_b64 v[160:163], v245 offset0:64 offset1:68\n"
    "ds_read2_b64 v[164:167], v245 offset0:72 offset1:76\n"
    "ds_read2_b64 v[168:171], v245 offset0:80 offset1:84\n"
    "ds_read2_b64 v[172:175], v245 offset0:88 offset1:92\n"
    "ds_read2_b64 v[176:179], v245 offset0:96 offset1:100\n"
    "ds_read2_b64 v[180:183], v245 offset0:104 offset1:108\n"
    "s_waitcnt lgkmcnt(13)\n"
    "v_lshlrev_b32 v0, 16, v128\n"
    "v_and_b32 v1, 0xffff0000, v128\n"
    "v_lshlrev_b32 v2, 16, v129\n"
    "v_and_b32 v3, 0xffff0000, v129\n"
    "v_lshlrev_b32 v4, 16, v130\n"
    "v_and_b32 v5, 0xffff0000, v130\n"
    "v_lshlrev_b32 v6, 16, v131\n"
    "v_and_b32 v7, 0xffff0000, v131\n"
    "s_waitcnt lgkmcnt(12)\n"
    "v_lshlrev_b32 v8, 16, v132\n"
    "v_and_b32 v9, 0xffff0000, v132\n"
    "v_lshlrev_b32 v10, 16, v133\n"
    "v_and_b32 v11, 0xffff0000, v133\n"
    "v_lshlrev_b32 v12, 16, v134\n"
    "v_and_b32 v13, 0xffff0000, v134\n"
    "v_lshlrev_b32 v14, 16, v135\n"
    "v_and_b32 v15, 0xffff0000, v135\n"
    "s_add_u32 m0, s100, 0\n"
    "s_mov_b32 s75, 229376\n"
    "buffer_load_dwordx4 v216, s[40:43], s75 offen offset:0 lds\n"
    "s_waitcnt lgkmcnt(11)\n"
    "v_lshlrev_b32 v16, 16, v136\n"
    "v_and_b32 v17, 0xffff0000, v136\n"
    "v_lshlrev_b32 v18, 16, v137\n"
    "v_and_b32 v19, 0xffff0000, v137\n"
    "v_lshlrev_b32 v20, 16, v138\n"
    "v_and_b32 v21, 0xffff0000, v138\n"
    "v_lshlrev_b32 v22, 16, v139\n"
    "v_and_b32 v23, 0xffff0000, v139\n"
    "s_waitcnt lgkmcnt(10)\n"
    "v_lshlrev_b32 v24, 16, v140\n"
    "v_and_b32 v25, 0xffff0000, v140\n"
    "v_lshlrev_b32 v26, 16, v141\n"
    "v_and_b32 v27, 0xffff0000, v141\n"
    "v_lshlrev_b32 v28, 16, v142\n"
    "buffer_load_dwordx4 v217, s[40:43], s75 offen offset:1024 lds\n"
    "v_and_b32 v29, 0xffff0000, v142\n"
    "v_lshlrev_b32 v30, 16, v143\n"
    "v_and_b32 v31, 0xffff0000, v143\n"
    "s_waitcnt lgkmcnt(9)\n"
    "v_lshlrev_b32 v32, 16, v144\n"
    "v_and_b32 v33, 0xffff0000, v144\n"
    "v_lshlrev_b32 v34, 16, v145\n"
    "v_and_b32 v35, 0xffff0000, v145\n"
    "v_lshlrev_b32 v36, 16, v146\n"
    "v_and_b32 v37, 0xffff0000, v146\n"
    "v_lshlrev_b32 v38, 16, v147\n"
    "v_and_b32 v39, 0xffff0000, v147\n"
    "s_waitcnt lgkmcnt(8)\n"
    "v_lshlrev_b32 v40, 16, v148\n"
    "v_and_b32 v41, 0xffff0000, v148\n"
    "buffer_load_dwordx4 v218, s[40:43], s75 offen offset:2048 lds\n"
    "v_lshlrev_b32 v42, 16, v149\n"
    "v_and_b32 v43, 0xffff0000, v149\n"
    "v_lshlrev_b32 v44, 16, v150\n"
    "v_and_b32 v45, 0xffff0000, v150\n"
    "v_lshlrev_b32 v46, 16, v151\n"
    "v_and_b32 v47, 0xffff0000, v151\n"
    "s_waitcnt lgkmcnt(7)\n"
    "v_lshlrev_b32 v48, 16, v152\n"
    "v_and_b32 v49, 0xffff0000, v152\n"
    "v_lshlrev_b32 v50, 16, v153\n"
    "v_and_b32 v51, 0xffff0000, v153\n"
    "v_lshlrev_b32 v52, 16, v154\n"
    "v_and_b32 v53, 0xffff0000, v154\n"
    "v_lshlrev_b32 v54, 16, v155\n"
    "buffer_load_dwordx4 v219, s[40:43], s75 offen offset:3072 lds\n"
    "v_and_b32 v55, 0xffff0000, v155\n"
    "s_waitcnt lgkmcnt(6)\n"
    "v_lshlrev_b32 v56, 16, v156\n"
    "v_and_b32 v57, 0xffff0000, v156\n"
    "v_lshlrev_b32 v58, 16, v157\n"
    "v_and_b32 v59, 0xffff0000, v157\n"
    "v_lshlrev_b32 v60, 16, v158\n"
    "v_and_b32 v61, 0xffff0000, v158\n"
    "v_lshlrev_b32 v62, 16, v159\n"
    "v_and_b32 v63, 0xffff0000, v159\n"
    "s_waitcnt lgkmcnt(5)\n"
    "v_lshlrev_b32 v64, 16, v160\n"
    "v_and_b32 v65, 0xffff0000, v160\n"
    "v_lshlrev_b32 v66, 16, v161\n"
    "v_and_b32 v67, 0xffff0000, v161\n"
    "s_add_u32 m0, s100, 16384\n"
    "s_mov_b32 s76, 344064\n"
    "buffer_load_dwordx4 v216, s[40:43], s76 offen offset:0 lds\n"
    "v_lshlrev_b32 v68, 16, v162\n"
    "v_and_b32 v69, 0xffff0000, v162\n"
    "v_lshlrev_b32 v70, 16, v163\n"
    "v_and_b32 v71, 0xffff0000, v163\n"
    "s_waitcnt lgkmcnt(4)\n"
    "v_lshlrev_b32 v72, 16, v164\n"
    "v_and_b32 v73, 0xffff0000, v164\n"
    "v_lshlrev_b32 v74, 16, v165\n"
    "v_and_b32 v75, 0xffff0000, v165\n"
    "v_lshlrev_b32 v76, 16, v166\n"
    "v_and_b32 v77, 0xffff0000, v166\n"
    "v_lshlrev_b32 v78, 16, v167\n"
    "v_and_b32 v79, 0xffff0000, v167\n"
    "s_waitcnt lgkmcnt(3)\n"
    "v_lshlrev_b32 v80, 16, v168\n"
    "buffer_load_dwordx4 v217, s[40:43], s76 offen offset:1024 lds\n"
    "v_and_b32 v81, 0xffff0000, v168\n"
    "v_lshlrev_b32 v82, 16, v169\n"
    "v_and_b32 v83, 0xffff0000, v169\n"
    "v_lshlrev_b32 v84, 16, v170\n"
    "v_and_b32 v85, 0xffff0000, v170\n"
    "v_lshlrev_b32 v86, 16, v171\n"
    "v_and_b32 v87, 0xffff0000, v171\n"
    "s_waitcnt lgkmcnt(2)\n"
    "v_lshlrev_b32 v88, 16, v172\n"
    "v_and_b32 v89, 0xffff0000, v172\n"
    "v_lshlrev_b32 v90, 16, v173\n"
    "v_and_b32 v91, 0xffff0000, v173\n"
    "v_lshlrev_b32 v92, 16, v174\n"
    "v_and_b32 v93, 0xffff0000, v174\n"
    "buffer_load_dwordx4 v218, s[40:43], s76 offen offset:2048 lds\n"
    "v_lshlrev_b32 v94, 16, v175\n"
    "v_and_b32 v95, 0xffff0000, v175\n"
    "s_waitcnt lgkmcnt(1)\n"
    "v_lshlrev_b32 v96, 16, v176\n"
    "v_and_b32 v97, 0xffff0000, v176\n"
    "v_lshlrev_b32 v98, 16, v177\n"
    "v_and_b32 v99, 0xffff0000, v177\n"
    "v_lshlrev_b32 v100, 16, v178\n"
    "v_and_b32 v101, 0xffff0000, v178\n"
    "v_lshlrev_b32 v102, 16, v179\n"
    "v_and_b32 v103, 0xffff0000, v179\n"
    "s_waitcnt lgkmcnt(0)\n"
    "v_lshlrev_b32 v104, 16, v180\n"
    "v_and_b32 v105, 0xffff0000, v180\n"
    "v_lshlrev_b32 v106, 16, v181\n"
    "buffer_load_dwordx4 v219, s[40:43], s76 offen offset:3072 lds\n"
    "v_and_b32 v107, 0xffff0000, v181\n"
    "v_lshlrev_b32 v108, 16, v182\n"
    "v_and_b32 v109, 0xffff0000, v182\n"
    "v_lshlrev_b32 v110, 16, v183\n"
    "v_and_b32 v111, 0xffff0000, v183\n"
    "v_pk_add_f32 v[198:199], v[0:1], v[8:9]\n"
    "v_pk_add_f32 v[200:201], v[2:3], v[10:11]\n"
    "v_pk_add_f32 v[202:203], v[4:5], v[12:13]\n"
    "v_pk_add_f32 v[204:205], v[6:7], v[14:15]\n"
    "v_pk_add_f32 v[198:199], v[198:199], v[16:17]\n"
    "v_pk_add_f32 v[200:201], v[200:201], v[18:19]\n"
    "v_pk_add_f32 v[202:203], v[202:203], v[20:21]\n"
    "v_pk_add_f32 v[204:205], v[204:205], v[22:23]\n"
    "s_add_u32 m0, s100, 32768\n"
    "s_mov_b32 s75, 458752\n"
    "buffer_load_dwordx4 v216, s[40:43], s75 offen offset:0 lds\n"
    "v_pk_add_f32 v[198:199], v[198:199], v[24:25]\n"
    "v_pk_add_f32 v[200:201], v[200:201], v[26:27]\n"
    "v_pk_add_f32 v[202:203], v[202:203], v[28:29]\n"
    "v_pk_add_f32 v[204:205], v[204:205], v[30:31]\n"
    "v_pk_add_f32 v[198:199], v[198:199], v[32:33]\n"
    "v_pk_add_f32 v[200:201], v[200:201], v[34:35]\n"
    "v_pk_add_f32 v[202:203], v[202:203], v[36:37]\n"
    "v_pk_add_f32 v[204:205], v[204:205], v[38:39]\n"
    "v_pk_add_f32 v[198:199], v[198:199], v[40:41]\n"
    "v_pk_add_f32 v[200:201], v[200:201], v[42:43]\n"
    "v_pk_add_f32 v[202:203], v[202:203], v[44:45]\n"
    "v_pk_add_f32 v[204:205], v[204:205], v[46:47]\n"
    "v_pk_add_f32 v[198:199], v[198:199], v[48:49]\n"
    "buffer_load_dwordx4 v217, s[40:43], s75 offen offset:1024 lds\n"
    "v_pk_add_f32 v[200:201], v[200:201], v[50:51]\n"
    "v_pk_add_f32 v[202:203], v[202:203], v[52:53]\n"
    "v_pk_add_f32 v[204:205], v[204:205], v[54:55]\n"
    "v_pk_add_f32 v[198:199], v[198:199], v[56:57]\n"
    "v_pk_add_f32 v[200:201], v[200:201], v[58:59]\n"
    "v_pk_add_f32 v[202:203], v[202:203], v[60:61]\n"
    "v_pk_add_f32 v[204:205], v[204:205], v[62:63]\n"
    "v_pk_add_f32 v[198:199], v[198:199], v[64:65]\n"
    "v_pk_add_f32 v[200:201], v[200:201], v[66:67]\n"
    "v_pk_add_f32 v[202:203], v[202:203], v[68:69]\n"
    "v_pk_add_f32 v[204:205], v[204:205], v[70:71]\n"
    "v_pk_add_f32 v[198:199], v[198:199], v[72:73]\n"
    "v_pk_add_f32 v[200:201], v[200:201], v[74:75]\n"
    "buffer_load_dwordx4 v218, s[40:43], s75 offen offset:2048 lds\n"
    "v_pk_add_f32 v[202:203], v[202:203], v[76:77]\n"
    "v_pk_add_f32 v[204:205], v[204:205], v[78:79]\n"
    "v_pk_add_f32 v[198:199], v[198:199], v[80:81]\n"
    "v_pk_add_f32 v[200:201], v[200:201], v[82:83]\n"
    "v_pk_add_f32 v[202:203], v[202:203], v[84:85]\n"
    "v_pk_add_f32 v[204:205], v[204:205], v[86:87]\n"
    "v_pk_add_f32 v[198:199], v[198:199], v[88:89]\n"
    "v_pk_add_f32 v[200:201], v[200:201], v[90:91]\n"
    "v_pk_add_f32 v[202:203], v[202:203], v[92:93]\n"
    "v_pk_add_f32 v[204:205], v[204:205], v[94:95]\n"
    "v_pk_add_f32 v[198:199], v[198:199], v[96:97]\n"
    "v_pk_add_f32 v[200:201], v[200:201], v[98:99]\n"
    "v_pk_add_f32 v[202:203], v[202:203], v[100:101]\n"
    "buffer_load_dwordx4 v219, s[40:43], s75 offen offset:3072 lds\n"
    "v_pk_add_f32 v[204:205], v[204:205], v[102:103]\n"
    "v_pk_add_f32 v[198:199], v[198:199], v[104:105]\n"
    "v_pk_add_f32 v[200:201], v[200:201], v[106:107]\n"
    "v_pk_add_f32 v[202:203], v[202:203], v[108:109]\n"
    "v_pk_add_f32 v[204:205], v[204:205], v[110:111]\n"
    "v_pk_add_f32 v[198:199], v[198:199], v[200:201]\n"
    "v_pk_add_f32 v[202:203], v[202:203], v[204:205]\n"
    "v_pk_add_f32 v[198:199], v[198:199], v[202:203]\n"
    "v_add_f32 v198, v198, v199\n"
    "v_mov_b32 v199, v198\n"
    "s_nop 1\n"
    "v_permlane32_swap_b32 v198, v199\n"
    "s_nop 1\n"
    "v_add_f32 v198, v198, v199\n"
    "v_mov_b32 v199, v198\n"
    "s_add_u32 m0, s100, 49152\n"
    "s_mov_b32 s76, 573440\n"
    "buffer_load_dwordx4 v216, s[40:43], s76 offen offset:0 lds\n"
    "s_nop 1\n"
    "v_permlane16_swap_b32 v198, v199\n"
    "s_nop 1\n"
    "v_add_f32 v198, v198, v199\n"
    "v_mul_f32 v198, 0xbb124925, v198\n"
    "v_mov_b32 v199, v198\n"
    "v_pk_add_f32 v[0:1], v[0:1], v[198:199]\n"
    "v_pk_add_f32 v[2:3], v[2:3], v[198:199]\n"
    "v_pk_add_f32 v[4:5], v[4:5], v[198:199]\n"
    "v_pk_add_f32 v[6:7], v[6:7], v[198:199]\n"
    "v_pk_add_f32 v[8:9], v[8:9], v[198:199]\n"
    "v_pk_add_f32 v[10:11], v[10:11], v[198:199]\n"
    "v_pk_add_f32 v[12:13], v[12:13], v[198:199]\n"
    "v_pk_add_f32 v[14:15], v[14:15], v[198:199]\n"
    "v_pk_add_f32 v[16:17], v[16:17], v[198:199]\n"
    "buffer_load_dwordx4 v217, s[40:43], s76 offen offset:1024 lds\n"
    "v_pk_add_f32 v[18:19], v[18:19], v[198:199]\n"
    "v_pk_add_f32 v[20:21], v[20:21], v[198:199]\n"
    "v_pk_add_f32 v[22:23], v[22:23], v[198:199]\n"
    "v_pk_add_f32 v[24:25], v[24:25], v[198:199]\n"
    "v_pk_add_f32 v[26:27], v[26:27], v[198:199]\n"
    "v_pk_add_f32 v[28:29], v[28:29], v[198:199]\n"
    "v_pk_add_f32 v[30:31], v[30:31], v[198:199]\n"
    "v_pk_add_f32 v[32:33], v[32:33], v[198:199]\n"
    "v_pk_add_f32 v[34:35], v[34:35], v[198:199]\n"
    "v_pk_add_f32 v[36:37], v[36:37], v[198:199]\n"
    "v_pk_add_f32 v[38:39], v[38:39], v[198:199]\n"
    "v_pk_add_f32 v[40:41], v[40:41], v[198:199]\n"
    "v_pk_add_f32 v[42:43], v[42:43], v[198:199]\n"
    "buffer_load_dwordx4 v218, s[40:43], s76 offen offset:2048 lds\n"
    "v_pk_add_f32 v[44:45], v[44:45], v[198:199]\n"
    "v_pk_add_f32 v[46:47], v[46:47], v[198:199]\n"
    "v_pk_add_f32 v[48:49], v[48:49], v[198:199]\n"
    "v_pk_add_f32 v[50:51], v[50:51], v[198:199]\n"
    "v_pk_add_f32 v[52:53], v[52:53], v[198:199]\n"
    "v_pk_add_f32 v[54:55], v[54:55], v[198:199]\n"
    "v_pk_add_f32 v[56:57], v[56:57], v[198:199]\n"
    "v_pk_add_f32 v[58:59], v[58:59], v[198:199]\n"
    "v_pk_add_f32 v[60:61], v[60:61], v[198:199]\n"
    "v_pk_add_f32 v[62:63], v[62:63], v[198:199]\n"
    "v_pk_add_f32 v[64:65], v[64:65], v[198:199]\n"
    "v_pk_add_f32 v[66:67], v[66:67], v[198:199]\n"
    "v_pk_add_f32 v[68:69], v[68:69], v[198:199]\n"
    "buffer_load_dwordx4 v219, s[40:43], s76 offen offset:3072 lds\n"
    "v_pk_add_f32 v[70:71], v[70:71], v[198:199]\n"
    "v_pk_add_f32 v[72:73], v[72:73], v[198:199]\n"
    "v_pk_add_f32 v[74:75], v[74:75], v[198:199]\n"
    "v_pk_add_f32 v[76:77], v[76:77], v[198:199]\n"
    "v_pk_add_f32 v[78:79], v[78:79], v[198:199]\n"
    "v_pk_add_f32 v[80:81], v[80:81], v[198:199]\n"
    "v_pk_add_f32 v[82:83], v[82:83], v[198:199]\n"
    "v_pk_add_f32 v[84:85], v[84:85], v[198:199]\n"
    "v_pk_add_f32 v[86:87], v[86:87], v[198:199]\n"
    "v_pk_add_f32 v[88:89], v[88:89], v[198:199]\n"
    "v_pk_add_f32 v[90:91], v[90:91], v[198:199]\n"
    "v_pk_add_f32 v[92:93], v[92:93], v[198:199]\n"
    "v_pk_add_f32 v[94:95], v[94:95], v[198:199]\n"
    "buffer_load_dword v224, v192, s[44:47], 0 offen\n"
    "v_pk_add_f32 v[96:97], v[96:97], v[198:199]\n"
    "v_pk_add_f32 v[98:99], v[98:99], v[198:199]\n"
    "v_pk_add_f32 v[100:101], v[100:101], v[198:199]\n"
    "v_pk_add_f32 v[102:103], v[102:103], v[198:199]\n"
    "v_pk_add_f32 v[104:105], v[104:105], v[198:199]\n"
    "v_pk_add_f32 v[106:107], v[106:107], v[198:199]\n"
    "v_pk_add_f32 v[108:109], v[108:109], v[198:199]\n"
    "v_pk_add_f32 v[110:111], v[110:111], v[198:199]\n"
    "v_pk_mul_f32 v[200:201], v[0:1], v[0:1]\n"
    "v_pk_mul_f32 v[202:203], v[2:3], v[2:3]\n"
    "v_pk_mul_f32 v[204:205], v[4:5], v[4:5]\n"
    "v_pk_mul_f32 v[206:207], v[6:7], v[6:7]\n"
    "v_pk_fma_f32 v[200:201], v[8:9], v[8:9], v[200:201]\n"
    "buffer_load_dword v225, v193, s[48:51], 0 offen\n"
    "v_pk_fma_f32 v[202:203], v[10:11], v[10:11], v[202:203]\n"
    "v_pk_fma_f32 v[204:205], v[12:13], v[12:13], v[204:205]\n"
    "v_pk_fma_f32 v[206:207], v[14:15], v[14:15], v[206:207]\n"
    "v_pk_fma_f32 v[200:201], v[16:17], v[16:17], v[200:201]\n"
    "v_pk_fma_f32 v[202:203], v[18:19], v[18:19], v[202:203]\n"
    "v_pk_fma_f32 v[204:205], v[20:21], v[20:21], v[204:205]\n"
    "v_pk_fma_f32 v[206:207], v[22:23], v[22:23], v[206:207]\n"
    "v_pk_fma_f32 v[200:201], v[24:25], v[24:25], v[200:201]\n"
    "v_pk_fma_f32 v[202:203], v[26:27], v[26:27], v[202:203]\n"
    "v_pk_fma_f32 v[204:205], v[28:29], v[28:29], v[204:205]\n"
    "v_pk_fma_f32 v[206:207], v[30:31], v[30:31], v[206:207]\n"
    "v_pk_fma_f32 v[200:201], v[32:33], v[32:33], v[200:201]\n"
    "v_pk_fma_f32 v[202:203], v[34:35], v[34:35], v[202:203]\n"
    "buffer_load_dword v226, v194, s[52:55], 0 offen\n"
    "v_pk_fma_f32 v[204:205], v[36:37], v[36:37], v[204:205]\n"
    "v_pk_fma_f32 v[206:207], v[38:39], v[38:39], v[206:207]\n"
    "v_pk_fma_f32 v[200:201], v[40:41], v[40:41], v[200:201]\n"
    "v_pk_fma_f32 v[202:203], v[42:43], v[42:43], v[202:203]\n"
    "v_pk_fma_f32 v[204:205], v[44:45], v[44:45], v[204:205]\n"
    "v_pk_fma_f32 v[206:207], v[46:47], v[46:47], v[206:207]\n"
    "v_pk_fma_f32 v[200:201], v[48:49], v[48:49], v[200:201]\n"
    "v_pk_fma_f32 v[202:203], v[50:51], v[50:51], v[202:203]\n"
    "v_pk_fma_f32 v[204:205], v[52:53], v[52:53], v[204:205]\n"
    "v_pk_fma_f32 v[206:207], v[54:55], v[54:55], v[206:207]\n"
    "v_pk_fma_f32 v[200:201], v[56:57], v[56:57], v[200:201]\n"
    "v_pk_fma_f32 v[202:203], v[58:59], v[58:59], v[202:203]\n"
    "v_pk_fma_f32 v[204:205], v[60:61], v[60:61], v[204:205]\n"
    "buffer_load_dword v227, v194, s[56:59], 0 offen\n"
    "v_pk_fma_f32 v[206:207], v[62:63], v[62:63], v[206:207]\n"
    "v_pk_fma_f32 v[200:201], v[64:65], v[64:65], v[200:201]\n"
    "v_pk_fma_f32 v[202:203], v[66:67], v[66:67], v[202:203]\n"
    "v_pk_fma_f32 v[204:205], v[68:69], v[68:69], v[204:205]\n"
    "v_pk_fma_f32 v[206:207], v[70:71], v[70:71], v[206:207]\n"
    "v_pk_fma_f32 v[200:201], v[72:73], v[72:73], v[200:201]\n"
    "v_pk_fma_f32 v[202:203], v[74:75], v[74:75], v[202:203]\n"
    "v_pk_fma_f32 v[204:205], v[76:77], v[76:77], v[204:205]\n"
    "v_pk_fma_f32 v[206:207], v[78:79], v[78:79], v[206:207]\n"
    "v_pk_fma_f32 v[200:201], v[80:81], v[80:81], v[200:201]\n"
    "v_pk_fma_f32 v[202:203], v[82:83], v[82:83], v[202:203]\n"
    "v_pk_fma_f32 v[204:205], v[84:85], v[84:85], v[204:205]\n"
    "v_pk_fma_f32 v[206:207], v[86:87], v[86:87], v[206:207]\n"
    "v_pk_fma_f32 v[200:201], v[88:89], v[88:89], v[200:201]\n"
    "v_pk_fma_f32 v[202:203], v[90:91], v[90:91], v[202:203]\n"
    "v_pk_fma_f32 v[204:205], v[92:93], v[92:93], v[204:205]\n"
    "v_pk_fma_f32 v[206:207], v[94:95], v[94:95], v[206:207]\n"
    "v_pk_fma_f32 v[200:201], v[96:97], v[96:97], v[200:201]\n"
    "v_pk_fma_f32 v[202:203], v[98:99], v[98:99], v[202:203]\n"
    "v_pk_fma_f32 v[204:205], v[100:101], v[100:101], v[204:205]\n"
    "v_pk_fma_f32 v[206:207], v[102:103], v[102:103], v[206:207]\n"
    "v_pk_fma_f32 v[200:201], v[104:105], v[104:105], v[200:201]\n"
    "v_pk_fma_f32 v[202:203], v[106:107], v[106:107], v[202:203]\n"
    "v_pk_fma_f32 v[204:205], v[108:109], v[108:109], v[204:205]\n"
    "v_pk_fma_f32 v[206:207], v[110:111], v[110:111], v[206:207]\n"
    "v_pk_add_f32 v[200:201], v[200:201], v[202:203]\n"
    "v_pk_add_f32 v[204:205], v[204:205], v[206:207]\n"
    "v_pk_add_f32 v[200:201], v[200:201], v[204:205]\n"
    "v_add_f32 v200, v200, v201\n"
    "v_mov_b32 v201, v200\n"
    "s_nop 1\n"
    "v_permlane32_swap_b32 v200, v201\n"
    "s_nop 1\n"
    "v_add_f32 v200, v200, v201\n"
    "v_mov_b32 v201, v200\n"
    "s_nop 1\n"
    "v_permlane16_swap_b32 v200, v201\n"
    "s_nop 1\n"
    "v_add_f32 v200, v200, v201\n"
    "v_mul_f32 v200, 0x3b124925, v200\n"
    "v_add_f32 v201, 0x3727c5ac, v200\n"
    "v_rsq_f32 v202, v201\n"
    "s_nop 0\n"
    "v_mul_f32 v204, v200, v202\n"
    "v_mul_f32 v204, v204, v202\n"
    "v_add_f32 v204, 0x3727c5ac, v204\n"
    "v_rsq_f32 v205, v204\n"
    "v_mul_f32 v204, 0x3db8aa3b, v204\n"
    "v_mul_f32 v202, 0x3db8aa3b, v202\n"
    "v_mul_f32 v206, v204, v205\n"
    "v_mov_b32 v203, v202\n"
    "v_and_b32 v247, 15, v244\n"
    "s_lshl_b32 s87, s69, 6\n"
    "s_add_u32 s87, s87, 159744\n"
    "v_lshl_add_u32 v248, v247, 2, s87\n"
    "ds_write_b32 v248, v206\n"
    "v_pk_mul_f32 v[0:1], v[0:1], v[202:203]\n"
    "v_cvt_pk_bf16_f32 v128, v0, v1\n"
    "v_pk_mul_f32 v[2:3], v[2:3], v[202:203]\n"
    "v_cvt_pk_bf16_f32 v129, v2, v3\n"
    "v_pk_mul_f32 v[4:5], v[4:5], v[202:203]\n"
    "v_cvt_pk_bf16_f32 v130, v4, v5\n"
    "v_pk_mul_f32 v[6:7], v[6:7], v[202:203]\n"
    "v_cvt_pk_bf16_f32 v131, v6, v7\n"
    "v_pk_mul_f32 v[8:9], v[8:9], v[202:203]\n"
    "v_cvt_pk_bf16_f32 v132, v8, v9\n"
    "v_pk_mul_f32 v[10:11], v[10:11], v[202:203]\n"
    "v_cvt_pk_bf16_f32 v133, v10, v11\n"
    "v_pk_mul_f32 v[12:13], v[12:13], v[202:203]\n"
    "v_cvt_pk_bf16_f32 v134, v12, v13\n"
    "v_pk_mul_f32 v[14:15], v[14:15], v[202:203]\n"
    "v_cvt_pk_bf16_f32 v135, v14, v15\n"
    "ds_write_b128 v220, v[128:131] offset:0\n"
    "v_pk_mul_f32 v[16:17], v[16:17], v[202:203]\n"
    "v_cvt_pk_bf16_f32 v136, v16, v17\n"
    "v_pk_mul_f32 v[18:19], v[18:19], v[202:203]\n"
    "v_cvt_pk_bf16_f32 v137, v18, v19\n"
    "v_pk_mul_f32 v[20:21], v[20:21], v[202:203]\n"
    "v_cvt_pk_bf16_f32 v138, v20, v21\n"
    "v_pk_mul_f32 v[22:23], v[22:23], v[202:203]\n"
    "v_cvt_pk_bf16_f32 v139, v22, v23\n"
    "ds_write_b128 v220, v[132:135] offset:1024\n"
    "v_pk_mul_f32 v[24:25], v[24:25], v[202:203]\n"
    "v_cvt_pk_bf16_f32 v140, v24, v25\n"
    "v_pk_mul_f32 v[26:27], v[26:27], v[202:203]\n"
    "v_cvt_pk_bf16_f32 v141, v26, v27\n"
    "v_pk_mul_f32 v[28:29], v[28:29], v[202:203]\n"
    "v_cvt_pk_bf16_f32 v142, v28, v29\n"
    "v_pk_mul_f32 v[30:31], v[30:31], v[202:203]\n"
    "v_cvt_pk_bf16_f32 v143, v30, v31\n"
    "ds_write_b128 v220, v[136:139] offset:2048\n"
    "v_pk_mul_f32 v[32:33], v[32:33], v[202:203]\n"
    "v_cvt_pk_bf16_f32 v144, v32, v33\n"
    "v_pk_mul_f32 v[34:35], v[34:35], v[202:203]\n"
    "v_cvt_pk_bf16_f32 v145, v34, v35\n"
    "v_pk_mul_f32 v[36:37], v[36:37], v[202:203]\n"
    "v_cvt_pk_bf16_f32 v146, v36, v37\n"
    "v_pk_mul_f32 v[38:39], v[38:39], v[202:203]\n"
    "v_cvt_pk_bf16_f32 v147, v38, v39\n"
    "ds_write_b128 v220, v[140:143] offset:3072\n"
    "v_pk_mul_f32 v[40:41], v[40:41], v[202:203]\n"
    "v_cvt_pk_bf16_f32 v148, v40, v41\n"
    "v_pk_mul_f32 v[42:43], v[42:43], v[202:203]\n"
    "v_cvt_pk_bf16_f32 v149, v42, v43\n"
    "v_pk_mul_f32 v[44:45], v[44:45], v[202:203]\n"
    "v_cvt_pk_bf16_f32 v150, v44, v45\n"
    "v_pk_mul_f32 v[46:47], v[46:47], v[202:203]\n"
    "v_cvt_pk_bf16_f32 v151, v46, v47\n"
    "ds_write_b128 v220, v[144:147] offset:4096\n"
    "v_pk_mul_f32 v[48:49], v[48:49], v[202:203]\n"
    "v_cvt_pk_bf16_f32 v152, v48, v49\n"
    "v_pk_mul_f32 v[50:51], v[50:51], v[202:203]\n"
    "v_cvt_pk_bf16_f32 v153, v50, v51\n"
    "v_pk_mul_f32 v[52:53], v[52:53], v[202:203]\n"
    "v_cvt_pk_bf16_f32 v154, v52, v53\n"
    "v_pk_mul_f32 v[54:55], v[54:55], v[202:203]\n"
    "v_cvt_pk_bf16_f32 v155, v54, v55\n"
    "ds_write_b128 v220, v[148:151] offset:5120\n"
    "v_pk_mul_f32 v[56:57], v[56:57], v[202:203]\n"
    "v_cvt_pk_bf16_f32 v156, v56, v57\n"
    "v_pk_mul_f32 v[58:59], v[58:59], v[202:203]\n"
    "v_cvt_pk_bf16_f32 v157, v58, v59\n"
    "v_pk_mul_f32 v[60:61], v[60:61], v[202:203]\n"
    "v_cvt_pk_bf16_f32 v158, v60, v61\n"
    "v_pk_mul_f32 v[62:63], v[62:63], v[202:203]\n"
    "v_cvt_pk_bf16_f32 v159, v62, v63\n"
    "ds_write_b128 v220, v[152:155] offset:6144\n"
    "v_pk_mul_f32 v[64:65], v[64:65], v[202:203]\n"
    "v_cvt_pk_bf16_f32 v160, v64, v65\n"
    "v_pk_mul_f32 v[66:67], v[66:67], v[202:203]\n"
    "v_cvt_pk_bf16_f32 v161, v66, v67\n"
    "v_pk_mul_f32 v[68:69], v[68:69], v[202:203]\n"
    "v_cvt_pk_bf16_f32 v162, v68, v69\n"
    "v_pk_mul_f32 v[70:71], v[70:71], v[202:203]\n"
    "v_cvt_pk_bf16_f32 v163, v70, v71\n"
    "ds_write_b128 v220, v[156:159] offset:7168\n"
    "v_pk_mul_f32 v[72:73], v[72:73], v[202:203]\n"
    "v_cvt_pk_bf16_f32 v164, v72, v73\n"
    "v_pk_mul_f32 v[74:75], v[74:75], v[202:203]\n"
    "v_cvt_pk_bf16_f32 v165, v74, v75\n"
    "v_pk_mul_f32 v[76:77], v[76:77], v[202:203]\n"
    "v_cvt_pk_bf16_f32 v166, v76, v77\n"
    "v_pk_mul_f32 v[78:79], v[78:79], v[202:203]\n"
    "v_cvt_pk_bf16_f32 v167, v78, v79\n"
    "ds_write_b128 v220, v[160:163] offset:8192\n"
    "v_pk_mul_f32 v[80:81], v[80:81], v[202:203]\n"
    "v_cvt_pk_bf16_f32 v168, v80, v81\n"
    "v_pk_mul_f32 v[82:83], v[82:83], v[202:203]\n"
    "v_cvt_pk_bf16_f32 v169, v82, v83\n"
    "v_pk_mul_f32 v[84:85], v[84:85], v[202:203]\n"
    "v_cvt_pk_bf16_f32 v170, v84, v85\n"
    "v_pk_mul_f32 v[86:87], v[86:87], v[202:203]\n"
    "v_cvt_pk_bf16_f32 v171, v86, v87\n"
    "ds_write_b128 v220, v[164:167] offset:9216\n"
    "v_pk_mul_f32 v[88:89], v[88:89], v[202:203]\n"
    "v_cvt_pk_bf16_f32 v172, v88, v89\n"
    "v_pk_mul_f32 v[90:91], v[90:91], v[202:203]\n"
    "v_cvt_pk_bf16_f32 v173, v90, v91\n"
    "v_pk_mul_f32 v[92:93], v[92:93], v[202:203]\n"
    "v_cvt_pk_bf16_f32 v174, v92, v93\n"
    "v_pk_mul_f32 v[94:95], v[94:95], v[202:203]\n"
    "v_cvt_pk_bf16_f32 v175, v94, v95\n"
    "ds_write_b128 v220, v[168:171] offset:10240\n"
    "v_pk_mul_f32 v[96:97], v[96:97], v[202:203]\n"
    "v_cvt_pk_bf16_f32 v176, v96, v97\n"
    "v_pk_mul_f32 v[98:99], v[98:99], v[202:203]\n"
    "v_cvt_pk_bf16_f32 v177, v98, v99\n"
    "v_pk_mul_f32 v[100:101], v[100:101], v[202:203]\n"
    "v_cvt_pk_bf16_f32 v178, v100, v101\n"
    "v_pk_mul_f32 v[102:103], v[102:103], v[202:203]\n"
    "v_cvt_pk_bf16_f32 v179, v102, v103\n"
    "ds_write_b128 v220, v[172:175] offset:11264\n"
    "v_pk_mul_f32 v[104:105], v[104:105], v[202:203]\n"
    "v_cvt_pk_bf16_f32 v180, v104, v105\n"
    "v_pk_mul_f32 v[106:107], v[106:107], v[202:203]\n"
    "v_cvt_pk_bf16_f32 v181, v106, v107\n"
    "v_pk_mul_f32 v[108:109], v[108:109], v[202:203]\n"
    "v_cvt_pk_bf16_f32 v182, v108, v109\n"
    "v_pk_mul_f32 v[110:111], v[110:111], v[202:203]\n"
    "v_cvt_pk_bf16_f32 v183, v110, v111\n"
    "ds_write_b128 v220, v[176:179] offset:12288\n"
    "ds_write_b128 v220, v[180:183] offset:13312\n"
    "v_mov_b32 v56, v232\n"
    "v_mov_b32 v57, v233\n"
    "v_mov_b32 v58, v234\n"
    "v_mov_b32 v59, v235\n"
    "v_mov_b32 v60, v236\n"
    "v_mov_b32 v61, v237\n"
    "v_mov_b32 v62, v238\n"
    "v_mov_b32 v63, v239\n"
    "v_mov_b32 v64, v240\n"
    "v_mov_b32 v65, v241\n"
    "v_mov_b32 v66, v242\n"
    "v_mov_b32 v67, v243\n"
    "s_waitcnt vmcnt(23)\n"
    "v_lshlrev_b32 v84, 16, v184\n"
    "v_and_b32 v85, 0xffff0000, v184\n"
    "v_lshlrev_b32 v86, 16, v185\n"
    "v_and_b32 v87, 0xffff0000, v185\n"
    "s_waitcnt vmcnt(22)\n"
    "v_lshlrev_b32 v88, 16, v186\n"
    "v_and_b32 v89, 0xffff0000, v186\n"
    "v_lshlrev_b32 v90, 16, v187\n"
    "v_and_b32 v91, 0xffff0000, v187\n"
    "s_waitcnt vmcnt(21)\n"
    "v_lshlrev_b32 v92, 16, v188\n"
    "v_and_b32 v93, 0xffff0000, v188\n"
    "v_lshlrev_b32 v94, 16, v189\n"
    "v_and_b32 v95, 0xffff0000, v189\n"
    "s_waitcnt vmcnt(20)\n"
    "v_lshlrev_b32 v96, 16, v190\n"
    "v_and_b32 v97, 0xffff0000, v190\n"
    "v_lshlrev_b32 v98, 16, v191\n"
    "v_and_b32 v99, 0xffff0000, v191\n"
    "v_xor_b32 v247, 64, v248\n"
    "v_xor_b32 v245, 128, v248\n"
    "v_xor_b32 v246, 192, v248\n"
    "s_waitcnt lgkmcnt(0)\n"
    "s_barrier\n"
    "ds_read_b32 v76, v248\n"
    "ds_read_b32 v77, v247\n"
    "ds_read_b32 v78, v245\n"
    "ds_read_b32 v79, v246\n"
    "ds_read_b128 a[0:3], v220 offset:0\n"
    "ds_read_b128 a[56:59], v221 offset:0\n"
    "ds_read_b128 a[112:115], v222 offset:0\n"
    "ds_read_b128 a[168:171], v223 offset:0\n"
    "ds_read_b128 a[4:7], v220 offset:1024\n"
    "ds_read_b128 a[60:63], v221 offset:1024\n"
    "ds_read_b128 a[116:119], v222 offset:1024\n"
    "ds_read_b128 a[172:175], v223 offset:1024\n"
    "s_waitcnt lgkmcnt(11)\n"
    "v_mul_f32 v0, v84, v76\n"
    "v_mul_f32 v1, v85, v76\n"
    "v_mul_f32 v2, v86, v76\n"
    "v_mul_f32 v3, v87, v76\n"
    "v_mul_f32 v16, v88, v76\n"
    "v_mul_f32 v17, v89, v76\n"
    "v_mul_f32 v18, v90, v76\n"
    "v_mul_f32 v19, v91, v76\n"
    "v_mul_f32 v32, v92, v76\n"
    "v_mul_f32 v33, v93, v76\n"
    "v_mul_f32 v34, v94, v76\n"
    "v_mul_f32 v35, v95, v76\n"
    "v_mul_f32 v48, v96, v76\n"
    "v_mul_f32 v49, v97, v76\n"
    "v_mul_f32 v50, v98, v76\n"
    "v_mul_f32 v51, v99, v76\n"
    "s_waitcnt lgkmcnt(10)\n"
    "v_mul_f32 v4, v84, v77\n"
    "v_mul_f32 v5, v85, v77\n"
    "v_mul_f32 v6, v86, v77\n"
    "v_mul_f32 v7, v87, v77\n"
    "v_mul_f32 v20, v88, v77\n"
    "v_mul_f32 v21, v89, v77\n"
    "v_mul_f32 v22, v90, v77\n"
    "v_mul_f32 v23, v91, v77\n"
    "v_mul_f32 v36, v92, v77\n"
    "v_mul_f32 v37, v93, v77\n"
    "v_mul_f32 v38, v94, v77\n"
    "v_mul_f32 v39, v95, v77\n"
    "v_mul_f32 v52, v96, v77\n"
    "v_mul_f32 v53, v97, v77\n"
    "v_mul_f32 v54, v98, v77\n"
    "v_mul_f32 v55, v99, v77\n"
    "s_waitcnt lgkmcnt(9)\n"
    "v_mul_f32 v8, v84, v78\n"
    "v_mul_f32 v9, v85, v78\n"
    "v_mul_f32 v10, v86, v78\n"
    "v_mul_f32 v11, v87, v78\n"
    "v_mul_f32 v24, v88, v78\n"
    "v_mul_f32 v25, v89, v78\n"
    "v_mul_f32 v26, v90, v78\n"
    "v_mul_f32 v27, v91, v78\n"
    "v_mul_f32 v40, v92, v78\n"
    "v_mul_f32 v41, v93, v78\n"
    "v_mul_f32 v42, v94, v78\n"
    "v_mul_f32 v43, v95, v78\n"
    "s_waitcnt lgkmcnt(8)\n"
    "v_mul_f32 v12, v84, v79\n"
    "v_mul_f32 v13, v85, v79\n"
    "v_mul_f32 v14, v86, v79\n"
    "v_mul_f32 v15, v87, v79\n"
    "v_mul_f32 v28, v88, v79\n"
    "v_mul_f32 v29, v89, v79\n"
    "v_mul_f32 v30, v90, v79\n"
    "v_mul_f32 v31, v91, v79\n"
    "v_mul_f32 v44, v92, v79\n"
    "v_mul_f32 v45, v93, v79\n"
    "v_mul_f32 v46, v94, v79\n"
    "v_mul_f32 v47, v95, v79\n"
    "v_rcp_f32 v80, v76\n"
    "v_rcp_f32 v81, v77\n"
    "v_rcp_f32 v82, v78\n"
    "v_rcp_f32 v83, v79\n"
    "v_mul_f32 v100, v80, v80\n"
    "v_mul_f32 v101, v81, v81\n"
    "v_mul_f32 v102, v82, v82\n"
    "v_mul_f32 v103, v83, v83\n"
    "v_mul_f32 v100, v100, v80\n"
    "v_mul_f32 v101, v101, v81\n"
    "v_mul_f32 v102, v102, v82\n"
    "v_mul_f32 v103, v103, v83\n"
    "v_mul_f32 v100, 0xbdd2d3e8, v100\n"
    "v_mul_f32 v101, 0xbdd2d3e8, v101\n"
    "v_mul_f32 v102, 0xbdd2d3e8, v102\n"
    "v_mul_f32 v103, 0xbdd2d3e8, v103\n"
    "v_mul_f32 v80, 0xc0135761, v80\n"
    "v_mul_f32 v81, 0xc0135761, v81\n"
    "v_mul_f32 v82, 0xc0135761, v82\n"
    "v_mul_f32 v83, 0xc0135761, v83\n"
    "s_barrier\n"
    "ds_read_b64_tr_b16 a[224:225], v212 offset:0\n"
    "ds_read_b64_tr_b16 a[226:227], v212 offset:8192\n"
    "ds_read_b64_tr_b16 a[228:229], v213 offset:0\n"
    "ds_read_b64_tr_b16 a[230:231], v213 offset:8192\n"
    "ds_read_b64_tr_b16 a[232:233], v214 offset:0\n"
    "ds_read_b64_tr_b16 a[234:235], v214 offset:8192\n"
    "s_waitcnt lgkmcnt(4)\n"
    "v_mfma_f32_16x16x32_bf16 v[0:3], a[224:227], a[0:3], v[0:3]\n"
    "ds_read_b64_tr_b16 a[236:237], v215 offset:0\n"
    "ds_read_b64_tr_b16 a[238:239], v215 offset:8192\n"
    "v_mfma_f32_16x16x32_bf16 v[4:7], a[224:227], a[56:59], v[4:7]\n"
    "s_barrier\n"
    "v_mfma_f32_16x16x32_bf16 v[8:11], a[224:227], a[112:115], v[8:11]\n"
    "ds_read_b64_tr_b16 a[240:241], v212 offset:16384\n"
    "ds_read_b64_tr_b16 a[242:243], v212 offset:24576\n"
    "v_mfma_f32_16x16x32_bf16 v[12:15], a[224:227], a[168:171], v[12:15]\n"
    "s_waitcnt lgkmcnt(6)\n"
    "v_mfma_f32_16x16x32_bf16 v[16:19], a[228:231], a[0:3], v[16:19]\n"
    "ds_read_b128 a[8:11], v220 offset:2048\n"
    "v_mfma_f32_16x16x32_bf16 v[20:23], a[228:231], a[56:59], v[20:23]\n"
    "ds_read_b128 a[64:67], v221 offset:2048\n"
    "v_mfma_f32_16x16x32_bf16 v[24:27], a[228:231], a[112:115], v[24:27]\n"
    "ds_read_b64_tr_b16 a[244:245], v213 offset:16384\n"
    "ds_read_b64_tr_b16 a[246:247], v213 offset:24576\n"
    "v_mfma_f32_16x16x32_bf16 v[28:31], a[228:231], a[168:171], v[28:31]\n"
    "s_waitcnt lgkmcnt(8)\n"
    "v_mfma_f32_16x16x32_bf16 v[32:35], a[232:235], a[0:3], v[32:35]\n"
    "ds_read_b128 a[120:123], v222 offset:2048\n"
    "v_mfma_f32_16x16x32_bf16 v[36:39], a[232:235], a[56:59], v[36:39]\n"
    "ds_read_b128 a[176:179], v223 offset:2048\n"
    "v_mfma_f32_16x16x32_bf16 v[40:43], a[232:235], a[112:115], v[40:43]\n"
    "ds_read_b64_tr_b16 a[248:249], v214 offset:16384\n"
    "ds_read_b64_tr_b16 a[250:251], v214 offset:24576\n"
    "v_mfma_f32_16x16x32_bf16 v[44:47], a[232:235], a[168:171], v[44:47]\n"
    "s_waitcnt lgkmcnt(10)\n"
    "v_mfma_f32_16x16x32_bf16 v[48:51], a[236:239], a[0:3], v[48:51]\n"
    "ds_read_b128 a[12:15], v220 offset:3072\n"
    "ds_read_b128 a[68:71], v221 offset:3072\n"
    "v_mfma_f32_16x16x32_bf16 v[52:55], a[236:239], a[56:59], v[52:55]\n"
    "s_waitcnt lgkmcnt(10)\n"
    "v_mfma_f32_16x16x32_bf16 v[0:3], a[240:243], a[4:7], v[0:3]\n"
    "s_mov_b32 s75, 688128\n"
    "ds_read_b64_tr_b16 a[252:253], v215 offset:16384\n"
    "ds_read_b64_tr_b16 a[254:255], v215 offset:24576\n"
    "v_mfma_f32_16x16x32_bf16 v[4:7], a[240:243], a[60:63], v[4:7]\n"
    "s_waitcnt vmcnt(16)\n"
    "s_barrier\n"
    "v_mfma_f32_16x16x32_bf16 v[8:11], a[240:243], a[116:119], v[8:11]\n"
    "s_add_u32 m0, s100, 65536\n"
    "ds_read_b64_tr_b16 a[224:225], v208 offset:0\n"
    "ds_read_b64_tr_b16 a[226:227], v208 offset:8192\n"
    "v_mfma_f32_16x16x32_bf16 v[12:15], a[240:243], a[172:175], v[12:15]\n"
    "s_waitcnt lgkmcnt(10)\n"
    "buffer_load_dwordx4 v216, s[40:43], s75 offen offset:0 lds\n"
    "v_mfma_f32_16x16x32_bf16 v[16:19], a[244:247], a[4:7], v[16:19]\n"
    "ds_read_b128 a[124:127], v222 offset:3072\n"
    "v_mfma_f32_16x16x32_bf16 v[20:23], a[244:247], a[60:63], v[20:23]\n"
    "ds_read_b128 a[180:183], v223 offset:3072\n"
    "v_mfma_f32_16x16x32_bf16 v[24:27], a[244:247], a[116:119], v[24:27]\n"
    "ds_read_b64_tr_b16 a[228:229], v209 offset:0\n"
    "ds_read_b64_tr_b16 a[230:231], v209 offset:8192\n"
    "v_mfma_f32_16x16x32_bf16 v[28:31], a[244:247], a[172:175], v[28:31]\n"
    "s_waitcnt lgkmcnt(10)\n"
    "buffer_load_dwordx4 v217, s[40:43], s75 offen offset:1024 lds\n"
    "v_mfma_f32_16x16x32_bf16 v[32:35], a[248:251], a[4:7], v[32:35]\n"
    "ds_read_b128 a[16:19], v220 offset:4096\n"
    "v_mfma_f32_16x16x32_bf16 v[36:39], a[248:251], a[60:63], v[36:39]\n"
    "ds_read_b128 a[72:75], v221 offset:4096\n"
    "v_mfma_f32_16x16x32_bf16 v[40:43], a[248:251], a[116:119], v[40:43]\n"
    "ds_read_b64_tr_b16 a[232:233], v210 offset:0\n"
    "ds_read_b64_tr_b16 a[234:235], v210 offset:8192\n"
    "v_mfma_f32_16x16x32_bf16 v[44:47], a[248:251], a[172:175], v[44:47]\n"
    "s_waitcnt lgkmcnt(10)\n"
    "buffer_load_dwordx4 v218, s[40:43], s75 offen offset:2048 lds\n"
    "v_mfma_f32_16x16x32_bf16 v[48:51], a[252:255], a[4:7], v[48:51]\n"
    "ds_read_b128 a[128:131], v222 offset:4096\n"
    "ds_read_b128 a[184:187], v223 offset:4096\n"
    "v_mfma_f32_16x16x32_bf16 v[52:55], a[252:255], a[60:63], v[52:55]\n"
    "s_waitcnt lgkmcnt(10)\n"
    "buffer_load_dwordx4 v219, s[40:43], s75 offen offset:3072 lds\n"
    "v_mfma_f32_16x16x32_bf16 v[0:3], a[224:227], a[8:11], v[0:3]\n"
    "s_mov_b32 s76, 802816\n"
    "ds_read_b64_tr_b16 a[236:237], v211 offset:0\n"
    "ds_read_b64_tr_b16 a[238:239], v211 offset:8192\n"
    "v_mfma_f32_16x16x32_bf16 v[4:7], a[224:227], a[64:67], v[4:7]\n"
    "s_waitcnt vmcnt(16)\n"
    "s_barrier\n"
    "v_mfma_f32_16x16x32_bf16 v[8:11], a[224:227], a[120:123], v[8:11]\n"
    "s_add_u32 m0, s100, 81920\n"
    "ds_read_b64_tr_b16 a[240:241], v208 offset:16384\n"
    "ds_read_b64_tr_b16 a[242:243], v208 offset:24576\n"
    "v_mfma_f32_16x16x32_bf16 v[12:15], a[224:227], a[176:179], v[12:15]\n"
    "s_waitcnt lgkmcnt(10)\n"
    "buffer_load_dwordx4 v216, s[40:43], s76 offen offset:0 lds\n"
    "v_mfma_f32_16x16x32_bf16 v[16:19], a[228:231], a[8:11], v[16:19]\n"
    "ds_read_b128 a[20:23], v220 offset:5120\n"
    "v_mfma_f32_16x16x32_bf16 v[20:23], a[228:231], a[64:67], v[20:23]\n"
    "ds_read_b128 a[76:79], v221 offset:5120\n"
    "v_mfma_f32_16x16x32_bf16 v[24:27], a[228:231], a[120:123], v[24:27]\n"
    "ds_read_b64_tr_b16 a[244:245], v209 offset:16384\n"
    "ds_read_b64_tr_b16 a[246:247], v209 offset:24576\n"
    "v_mfma_f32_16x16x32_bf16 v[28:31], a[228:231], a[176:179], v[28:31]\n"
    "s_waitcnt lgkmcnt(10)\n"
    "buffer_load_dwordx4 v217, s[40:43], s76 offen offset:1024 lds\n"
    "v_mfma_f32_16x16x32_bf16 v[32:35], a[232:235], a[8:11], v[32:35]\n"
    "ds_read_b128 a[132:135], v222 offset:5120\n"
    "v_mfma_f32_16x16x32_bf16 v[36:39], a[232:235], a[64:67], v[36:39]\n"
    "ds_read_b128 a[188:191], v223 offset:5120\n"
    "v_mfma_f32_16x16x32_bf16 v[40:43], a[232:235], a[120:123], v[40:43]\n"
    "ds_read_b64_tr_b16 a[248:249], v210 offset:16384\n"
    "ds_read_b64_tr_b16 a[250:251], v210 offset:24576\n"
    "v_mfma_f32_16x16x32_bf16 v[44:47], a[232:235], a[176:179], v[44:47]\n"
    "s_waitcnt lgkmcnt(10)\n"
    "buffer_load_dwordx4 v218, s[40:43], s76 offen offset:2048 lds\n"
    "v_mfma_f32_16x16x32_bf16 v[48:51], a[236:239], a[8:11], v[48:51]\n"
    "ds_read_b128 a[24:27], v220 offset:6144\n"
    "ds_read_b128 a[80:83], v221 offset:6144\n"
    "v_mfma_f32_16x16x32_bf16 v[52:55], a[236:239], a[64:67], v[52:55]\n"
    "s_waitcnt lgkmcnt(10)\n"
    "buffer_load_dwordx4 v219, s[40:43], s76 offen offset:3072 lds\n"
    "v_mfma_f32_16x16x32_bf16 v[0:3], a[240:243], a[12:15], v[0:3]\n"
    "s_mov_b32 s75, 917504\n"
    "ds_read_b64_tr_b16 a[252:253], v211 offset:16384\n"
    "ds_read_b64_tr_b16 a[254:255], v211 offset:24576\n"
    "v_mfma_f32_16x16x32_bf16 v[4:7], a[240:243], a[68:71], v[4:7]\n"
    "s_waitcnt vmcnt(16)\n"
    "s_barrier\n"
    "v_mfma_f32_16x16x32_bf16 v[8:11], a[240:243], a[124:127], v[8:11]\n"
    "s_add_u32 m0, s100, 0\n"
    "ds_read_b64_tr_b16 a[224:225], v208 offset:32768\n"
    "ds_read_b64_tr_b16 a[226:227], v208 offset:40960\n"
    "v_mfma_f32_16x16x32_bf16 v[12:15], a[240:243], a[180:183], v[12:15]\n"
    "s_waitcnt lgkmcnt(10)\n"
    "buffer_load_dwordx4 v216, s[40:43], s75 offen offset:0 lds\n"
    "v_mfma_f32_16x16x32_bf16 v[16:19], a[244:247], a[12:15], v[16:19]\n"
    "ds_read_b128 a[136:139], v222 offset:6144\n"
    "v_mfma_f32_16x16x32_bf16 v[20:23], a[244:247], a[68:71], v[20:23]\n"
    "ds_read_b128 a[192:195], v223 offset:6144\n"
    "v_mfma_f32_16x16x32_bf16 v[24:27], a[244:247], a[124:127], v[24:27]\n"
    "ds_read_b64_tr_b16 a[228:229], v209 offset:32768\n"
    "ds_read_b64_tr_b16 a[230:231], v209 offset:40960\n"
    "v_mfma_f32_16x16x32_bf16 v[28:31], a[244:247], a[180:183], v[28:31]\n"
    "s_waitcnt lgkmcnt(10)\n"
    "buffer_load_dwordx4 v217, s[40:43], s75 offen offset:1024 lds\n"
    "v_mfma_f32_16x16x32_bf16 v[32:35], a[248:251], a[12:15], v[32:35]\n"
    "ds_read_b128 a[28:31], v220 offset:7168\n"
    "v_mfma_f32_16x16x32_bf16 v[36:39], a[248:251], a[68:71], v[36:39]\n"
    "ds_read_b128 a[84:87], v221 offset:7168\n"
    "v_mfma_f32_16x16x32_bf16 v[40:43], a[248:251], a[124:127], v[40:43]\n"
    "ds_read_b64_tr_b16 a[232:233], v210 offset:32768\n"
    "ds_read_b64_tr_b16 a[234:235], v210 offset:40960\n"
    "v_mfma_f32_16x16x32_bf16 v[44:47], a[248:251], a[180:183], v[44:47]\n"
    "s_waitcnt lgkmcnt(10)\n"
    "buffer_load_dwordx4 v218, s[40:43], s75 offen offset:2048 lds\n"
    "v_mfma_f32_16x16x32_bf16 v[48:51], a[252:255], a[12:15], v[48:51]\n"
    "ds_read_b128 a[140:143], v222 offset:7168\n"
    "ds_read_b128 a[196:199], v223 offset:7168\n"
    "v_mfma_f32_16x16x32_bf16 v[52:55], a[252:255], a[68:71], v[52:55]\n"
    "s_waitcnt lgkmcnt(10)\n"
    "buffer_load_dwordx4 v219, s[40:43], s75 offen offset:3072 lds\n"
    "v_mfma_f32_16x16x32_bf16 v[0:3], a[224:227], a[16:19], v[0:3]\n"
    "s_mov_b32 s76, 1032192\n"
    "ds_read_b64_tr_b16 a[236:237], v211 offset:32768\n"
    "ds_read_b64_tr_b16 a[238:239], v211 offset:40960\n"
    "v_mfma_f32_16x16x32_bf16 v[4:7], a[224:227], a[72:75], v[4:7]\n"
    "s_waitcnt vmcnt(16)\n"
    "s_barrier\n"
    "v_mfma_f32_16x16x32_bf16 v[8:11], a[224:227], a[128:131], v[8:11]\n"
    "s_add_u32 m0, s100, 16384\n"
    "ds_read_b64_tr_b16 a[240:241], v208 offset:49152\n"
    "ds_read_b64_tr_b16 a[242:243], v208 offset:57344\n"
    "v_mfma_f32_16x16x32_bf16 v[12:15], a[224:227], a[184:187], v[12:15]\n"
    "s_waitcnt lgkmcnt(10)\n"
    "buffer_load_dwordx4 v216, s[40:43], s76 offen offset:0 lds\n"
    "v_mfma_f32_16x16x32_bf16 v[16:19], a[228:231], a[16:19], v[16:19]\n"
    "ds_read_b128 a[32:35], v220 offset:8192\n"
    "v_mfma_f32_16x16x32_bf16 v[20:23], a[228:231], a[72:75], v[20:23]\n"
    "ds_read_b128 a[88:91], v221 offset:8192\n"
    "v_mfma_f32_16x16x32_bf16 v[24:27], a[228:231], a[128:131], v[24:27]\n"
    "ds_read_b64_tr_b16 a[244:245], v209 offset:49152\n"
    "ds_read_b64_tr_b16 a[246:247], v209 offset:57344\n"
    "v_mfma_f32_16x16x32_bf16 v[28:31], a[228:231], a[184:187], v[28:31]\n"
    "s_waitcnt lgkmcnt(10)\n"
    "buffer_load_dwordx4 v217, s[40:43], s76 offen offset:1024 lds\n"
    "v_mfma_f32_16x16x32_bf16 v[32:35], a[232:235], a[16:19], v[32:35]\n"
    "ds_read_b128 a[144:147], v222 offset:8192\n"
    "v_mfma_f32_16x16x32_bf16 v[36:39], a[232:235], a[72:75], v[36:39]\n"
    "ds_read_b128 a[200:203], v223 offset:8192\n"
    "v_mfma_f32_16x16x32_bf16 v[40:43], a[232:235], a[128:131], v[40:43]\n"
    "ds_read_b64_tr_b16 a[248:249], v210 offset:49152\n"
    "ds_read_b64_tr_b16 a[250:251], v210 offset:57344\n"
    "v_mfma_f32_16x16x32_bf16 v[44:47], a[232:235], a[184:187], v[44:47]\n"
    "s_waitcnt lgkmcnt(10)\n"
    "buffer_load_dwordx4 v218, s[40:43], s76 offen offset:2048 lds\n"
    "v_mfma_f32_16x16x32_bf16 v[48:51], a[236:239], a[16:19], v[48:51]\n"
    "ds_read_b128 a[36:39], v220 offset:9216\n"
    "ds_read_b128 a[92:95], v221 offset:9216\n"
    "v_mfma_f32_16x16x32_bf16 v[52:55], a[236:239], a[72:75], v[52:55]\n"
    "s_waitcnt lgkmcnt(10)\n"
    "buffer_load_dwordx4 v219, s[40:43], s76 offen offset:3072 lds\n"
    "v_mfma_f32_16x16x32_bf16 v[0:3], a[240:243], a[20:23], v[0:3]\n"
    "s_mov_b32 s75, 1146880\n"
    "ds_read_b64_tr_b16 a[252:253], v211 offset:49152\n"
    "ds_read_b64_tr_b16 a[254:255], v211 offset:57344\n"
    "v_mfma_f32_16x16x32_bf16 v[4:7], a[240:243], a[76:79], v[4:7]\n"
    "s_waitcnt vmcnt(12)\n"
    "s_barrier\n"
    "v_mfma_f32_16x16x32_bf16 v[8:11], a[240:243], a[132:135], v[8:11]\n"
    "s_add_u32 m0, s100, 32768\n"
    "ds_read_b64_tr_b16 a[224:225], v212 offset:0\n"
    "ds_read_b64_tr_b16 a[226:227], v212 offset:8192\n"
    "v_mfma_f32_16x16x32_bf16 v[12:15], a[240:243], a[188:191], v[12:15]\n"
    "s_waitcnt lgkmcnt(10)\n"
    "buffer_load_dwordx4 v216, s[40:43], s75 offen offset:0 lds\n"
    "v_mfma_f32_16x16x32_bf16 v[16:19], a[244:247], a[20:23], v[16:19]\n"
    "ds_read_b128 a[148:151], v222 offset:9216\n"
    "v_mfma_f32_16x16x32_bf16 v[20:23], a[244:247], a[76:79], v[20:23]\n"
    "ds_read_b128 a[204:207], v223 offset:9216\n"
    "v_mfma_f32_16x16x32_bf16 v[24:27], a[244:247], a[132:135], v[24:27]\n"
    "ds_read_b64_tr_b16 a[228:229], v213 offset:0\n"
    "ds_read_b64_tr_b16 a[230:231], v213 offset:8192\n"
    "v_mfma_f32_16x16x32_bf16 v[28:31], a[244:247], a[188:191], v[28:31]\n"
    "s_waitcnt lgkmcnt(10)\n"
    "buffer_load_dwordx4 v217, s[40:43], s75 offen offset:1024 lds\n"
    "v_mfma_f32_16x16x32_bf16 v[32:35], a[248:251], a[20:23], v[32:35]\n"
    "ds_read_b128 a[40:43], v220 offset:10240\n"
    "v_mfma_f32_16x16x32_bf16 v[36:39], a[248:251], a[76:79], v[36:39]\n"
    "ds_read_b128 a[96:99], v221 offset:10240\n"
    "v_mfma_f32_16x16x32_bf16 v[40:43], a[248:251], a[132:135], v[40:43]\n"
    "ds_read_b64_tr_b16 a[232:233], v214 offset:0\n"
    "ds_read_b64_tr_b16 a[234:235], v214 offset:8192\n"
    "v_mfma_f32_16x16x32_bf16 v[44:47], a[248:251], a[188:191], v[44:47]\n"
    "s_waitcnt lgkmcnt(10)\n"
    "buffer_load_dwordx4 v218, s[40:43], s75 offen offset:2048 lds\n"
    "v_mfma_f32_16x16x32_bf16 v[48:51], a[252:255], a[20:23], v[48:51]\n"
    "ds_read_b128 a[152:155], v222 offset:10240\n"
    "ds_read_b128 a[208:211], v223 offset:10240\n"
    "v_mfma_f32_16x16x32_bf16 v[52:55], a[252:255], a[76:79], v[52:55]\n"
    "s_waitcnt lgkmcnt(10)\n"
    "buffer_load_dwordx4 v219, s[40:43], s75 offen offset:3072 lds\n"
    "v_mfma_f32_16x16x32_bf16 v[0:3], a[224:227], a[24:27], v[0:3]\n"
    "s_mov_b32 s76, 1261568\n"
    "ds_read_b64_tr_b16 a[236:237], v215 offset:0\n"
    "ds_read_b64_tr_b16 a[238:239], v215 offset:8192\n"
    "v_mfma_f32_16x16x32_bf16 v[4:7], a[224:227], a[80:83], v[4:7]\n"
    "s_waitcnt vmcnt(12)\n"
    "s_barrier\n"
    "v_mfma_f32_16x16x32_bf16 v[8:11], a[224:227], a[136:139], v[8:11]\n"
    "s_add_u32 m0, s100, 49152\n"
    "ds_read_b64_tr_b16 a[240:241], v212 offset:16384\n"
    "ds_read_b64_tr_b16 a[242:243], v212 offset:24576\n"
    "v_mfma_f32_16x16x32_bf16 v[12:15], a[224:227], a[192:195], v[12:15]\n"
    "s_waitcnt lgkmcnt(10)\n"
    "buffer_load_dwordx4 v216, s[40:43], s76 offen offset:0 lds\n"
    "v_mfma_f32_16x16x32_bf16 v[16:19], a[228:231], a[24:27], v[16:19]\n"
    "ds_read_b128 a[44:47], v220 offset:11264\n"
    "v_mfma_f32_16x16x32_bf16 v[20:23], a[228:231], a[80:83], v[20:23]\n"
    "ds_read_b128 a[100:103], v221 offset:11264\n"
    "v_mfma_f32_16x16x32_bf16 v[24:27], a[228:231], a[136:139], v[24:27]\n"
    "ds_read_b64_tr_b16 a[244:245], v213 offset:16384\n"
    "ds_read_b64_tr_b16 a[246:247], v213 offset:24576\n"
    "v_mfma_f32_16x16x32_bf16 v[28:31], a[228:231], a[192:195], v[28:31]\n"
    "s_waitcnt lgkmcnt(10)\n"
    "buffer_load_dwordx4 v217, s[40:43], s76 offen offset:1024 lds\n"
    "v_mfma_f32_16x16x32_bf16 v[32:35], a[232:235], a[24:27], v[32:35]\n"
    "ds_read_b128 a[156:159], v222 offset:11264\n"
    "v_mfma_f32_16x16x32_bf16 v[36:39], a[232:235], a[80:83], v[36:39]\n"
    "ds_read_b128 a[212:215], v223 offset:11264\n"
    "v_mfma_f32_16x16x32_bf16 v[40:43], a[232:235], a[136:139], v[40:43]\n"
    "ds_read_b64_tr_b16 a[248:249], v214 offset:16384\n"
    "ds_read_b64_tr_b16 a[250:251], v214 offset:24576\n"
    "v_mfma_f32_16x16x32_bf16 v[44:47], a[232:235], a[192:195], v[44:47]\n"
    "s_waitcnt lgkmcnt(10)\n"
    "buffer_load_dwordx4 v218, s[40:43], s76 offen offset:2048 lds\n"
    "v_mfma_f32_16x16x32_bf16 v[48:51], a[236:239], a[24:27], v[48:51]\n"
    "ds_read_b128 a[48:51], v220 offset:12288\n"
    "ds_read_b128 a[104:107], v221 offset:12288\n"
    "v_mfma_f32_16x16x32_bf16 v[52:55], a[236:239], a[80:83], v[52:55]\n"
    "s_waitcnt lgkmcnt(10)\n"
    "buffer_load_dwordx4 v219, s[40:43], s76 offen offset:3072 lds\n"
    "v_mfma_f32_16x16x32_bf16 v[0:3], a[240:243], a[28:31], v[0:3]\n"
    "s_mov_b32 s75, 1376256\n"
    "ds_read_b64_tr_b16 a[252:253], v215 offset:16384\n"
    "ds_read_b64_tr_b16 a[254:255], v215 offset:24576\n"
    "v_mfma_f32_16x16x32_bf16 v[4:7], a[240:243], a[84:87], v[4:7]\n"
    "s_waitcnt vmcnt(12)\n"
    "s_barrier\n"
    "v_mfma_f32_16x16x32_bf16 v[8:11], a[240:243], a[140:143], v[8:11]\n"
    "s_add_u32 m0, s100, 65536\n"
    "ds_read_b64_tr_b16 a[224:225], v208 offset:0\n"
    "ds_read_b64_tr_b16 a[226:227], v208 offset:8192\n"
    "v_mfma_f32_16x16x32_bf16 v[12:15], a[240:243], a[196:199], v[12:15]\n"
    "s_waitcnt lgkmcnt(10)\n"
    "buffer_load_dwordx4 v216, s[40:43], s75 offen offset:0 lds\n"
    "v_mfma_f32_16x16x32_bf16 v[16:19], a[244:247], a[28:31], v[16:19]\n"
    "ds_read_b128 a[160:163], v222 offset:12288\n"
    "v_mfma_f32_16x16x32_bf16 v[20:23], a[244:247], a[84:87], v[20:23]\n"
    "ds_read_b128 a[216:219], v223 offset:12288\n"
    "v_mfma_f32_16x16x32_bf16 v[24:27], a[244:247], a[140:143], v[24:27]\n"
    "ds_read_b64_tr_b16 a[228:229], v209 offset:0\n"
    "ds_read_b64_tr_b16 a[230:231], v209 offset:8192\n"
    "v_mfma_f32_16x16x32_bf16 v[28:31], a[244:247], a[196:199], v[28:31]\n"
    "s_waitcnt lgkmcnt(10)\n"
    "buffer_load_dwordx4 v217, s[40:43], s75 offen offset:1024 lds\n"
    "v_mfma_f32_16x16x32_bf16 v[32:35], a[248:251], a[28:31], v[32:35]\n"
    "ds_read_b128 a[52:55], v220 offset:13312\n"
    "v_mfma_f32_16x16x32_bf16 v[36:39], a[248:251], a[84:87], v[36:39]\n"
    "ds_read_b128 a[108:111], v221 offset:13312\n"
    "v_mfma_f32_16x16x32_bf16 v[40:43], a[248:251], a[140:143], v[40:43]\n"
    "ds_read_b64_tr_b16 a[232:233], v210 offset:0\n"
    "ds_read_b64_tr_b16 a[234:235], v210 offset:8192\n"
    "v_mfma_f32_16x16x32_bf16 v[44:47], a[248:251], a[196:199], v[44:47]\n"
    "s_waitcnt lgkmcnt(10)\n"
    "buffer_load_dwordx4 v218, s[40:43], s75 offen offset:2048 lds\n"
    "v_mfma_f32_16x16x32_bf16 v[48:51], a[252:255], a[28:31], v[48:51]\n"
    "ds_read_b128 a[164:167], v222 offset:13312\n"
    "ds_read_b128 a[220:223], v223 offset:13312\n"
    "v_mfma_f32_16x16x32_bf16 v[52:55], a[252:255], a[84:87], v[52:55]\n"
    "s_waitcnt lgkmcnt(10)\n"
    "buffer_load_dwordx4 v219, s[40:43], s75 offen offset:3072 lds\n"
    "v_mfma_f32_16x16x32_bf16 v[0:3], a[224:227], a[32:35], v[0:3]\n"
    "s_mov_b32 s76, 1490944\n"
    "ds_read_b64_tr_b16 a[236:237], v211 offset:0\n"
    "ds_read_b64_tr_b16 a[238:239], v211 offset:8192\n"
    "v_mfma_f32_16x16x32_bf16 v[4:7], a[224:227], a[88:91], v[4:7]\n"
    "s_waitcnt vmcnt(12)\n"
    "s_barrier\n"
    "v_mfma_f32_16x16x32_bf16 v[8:11], a[224:227], a[144:147], v[8:11]\n"
    "s_add_u32 m0, s100, 81920\n"
    "ds_read_b64_tr_b16 a[240:241], v208 offset:16384\n"
    "ds_read_b64_tr_b16 a[242:243], v208 offset:24576\n"
    "v_mfma_f32_16x16x32_bf16 v[12:15], a[224:227], a[200:203], v[12:15]\n"
    "s_waitcnt lgkmcnt(10)\n"
    "buffer_load_dwordx4 v216, s[40:43], s76 offen offset:0 lds\n"
    "v_mfma_f32_16x16x32_bf16 v[16:19], a[228:231], a[32:35], v[16:19]\n"
    "v_mfma_f32_16x16x32_bf16 v[20:23], a[228:231], a[88:91], v[20:23]\n"
    "v_mfma_f32_16x16x32_bf16 v[24:27], a[228:231], a[144:147], v[24:27]\n"
    "ds_read_b64_tr_b16 a[244:245], v209 offset:16384\n"
    "ds_read_b64_tr_b16 a[246:247], v209 offset:24576\n"
    "v_mfma_f32_16x16x32_bf16 v[28:31], a[228:231], a[200:203], v[28:31]\n"
    "s_waitcnt lgkmcnt(8)\n"
    "buffer_load_dwordx4 v217, s[40:43], s76 offen offset:1024 lds\n"
    "v_mfma_f32_16x16x32_bf16 v[32:35], a[232:235], a[32:35], v[32:35]\n"
    "v_mfma_f32_16x16x32_bf16 v[36:39], a[232:235], a[88:91], v[36:39]\n"
    "v_mfma_f32_16x16x32_bf16 v[40:43], a[232:235], a[144:147], v[40:43]\n"
    "ds_read_b64_tr_b16 a[248:249], v210 offset:16384\n"
    "ds_read_b64_tr_b16 a[250:251], v210 offset:24576\n"
    "v_mfma_f32_16x16x32_bf16 v[44:47], a[232:235], a[200:203], v[44:47]\n"
    "s_waitcnt lgkmcnt(6)\n"
    "buffer_load_dwordx4 v218, s[40:43], s76 offen offset:2048 lds\n"
    "v_mfma_f32_16x16x32_bf16 v[48:51], a[236:239], a[32:35], v[48:51]\n"
    "v_mfma_f32_16x16x32_bf16 v[52:55], a[236:239], a[88:91], v[52:55]\n"
    "s_waitcnt lgkmcnt(4)\n"
    "buffer_load_dwordx4 v219, s[40:43], s76 offen offset:3072 lds\n"
    "v_mfma_f32_16x16x32_bf16 v[0:3], a[240:243], a[36:39], v[0:3]\n"
    "s_mov_b32 s75, 0\n"
    "ds_read_b64_tr_b16 a[252:253], v211 offset:16384\n"
    "ds_read_b64_tr_b16 a[254:255], v211 offset:24576\n"
    "v_mfma_f32_16x16x32_bf16 v[4:7], a[240:243], a[92:95], v[4:7]\n"
    "s_waitcnt vmcnt(12)\n"
    "s_barrier\n"
    "v_mfma_f32_16x16x32_bf16 v[8:11], a[240:243], a[148:151], v[8:11]\n"
    "s_add_u32 m0, s90, 98304\n"
    "ds_read_b64_tr_b16 a[224:225], v208 offset:32768\n"
    "ds_read_b64_tr_b16 a[226:227], v208 offset:40960\n"
    "v_mfma_f32_16x16x32_bf16 v[12:15], a[240:243], a[204:207], v[12:15]\n"
    "s_waitcnt lgkmcnt(6)\n"
    "buffer_load_dwordx4 v64, s[44:47], s75 offen offset:0 lds\n"
    "v_mfma_f32_16x16x32_bf16 v[16:19], a[244:247], a[36:39], v[16:19]\n"
    "v_mfma_f32_16x16x32_bf16 v[20:23], a[244:247], a[92:95], v[20:23]\n"
    "v_mfma_f32_16x16x32_bf16 v[24:27], a[244:247], a[148:151], v[24:27]\n"
    "s_add_u32 s76, s75, 512\n"
    "ds_read_b64_tr_b16 a[228:229], v209 offset:32768\n"
    "ds_read_b64_tr_b16 a[230:231], v209 offset:40960\n"
    "v_mfma_f32_16x16x32_bf16 v[28:31], a[244:247], a[204:207], v[28:31]\n"
    "s_waitcnt lgkmcnt(6)\n"
    "buffer_load_dwordx4 v65, s[44:47], s75 offen offset:1024 lds\n"
    "v_mfma_f32_16x16x32_bf16 v[32:35], a[248:251], a[36:39], v[32:35]\n"
    "v_mfma_f32_16x16x32_bf16 v[36:39], a[248:251], a[92:95], v[36:39]\n"
    "v_mfma_f32_16x16x32_bf16 v[40:43], a[248:251], a[148:151], v[40:43]\n"
    "ds_read_b64_tr_b16 a[232:233], v210 offset:32768\n"
    "ds_read_b64_tr_b16 a[234:235], v210 offset:40960\n"
    "v_mfma_f32_16x16x32_bf16 v[44:47], a[248:251], a[204:207], v[44:47]\n"
    "s_waitcnt lgkmcnt(6)\n"
    "buffer_load_dwordx4 v66, s[44:47], s75 offen offset:2048 lds\n"
    "v_mfma_f32_16x16x32_bf16 v[48:51], a[252:255], a[36:39], v[48:51]\n"
    "v_mfma_f32_16x16x32_bf16 v[52:55], a[252:255], a[92:95], v[52:55]\n"
    "s_waitcnt lgkmcnt(4)\n"
    "buffer_load_dwordx4 v67, s[44:47], s75 offen offset:3072 lds\n"
    "v_mfma_f32_16x16x32_bf16 v[0:3], a[224:227], a[40:43], v[0:3]\n"
    "ds_read_b64_tr_b16 a[236:237], v211 offset:32768\n"
    "ds_read_b64_tr_b16 a[238:239], v211 offset:40960\n"
    "v_mfma_f32_16x16x32_bf16 v[4:7], a[224:227], a[96:99], v[4:7]\n"
    "s_waitcnt vmcnt(12)\n"
    "s_barrier\n"
    "v_mfma_f32_16x16x32_bf16 v[8:11], a[224:227], a[152:155], v[8:11]\n"
    "s_add_u32 m0, s90, 102400\n"
    "ds_read_b64_tr_b16 a[240:241], v208 offset:49152\n"
    "ds_read_b64_tr_b16 a[242:243], v208 offset:57344\n"
    "v_mfma_f32_16x16x32_bf16 v[12:15], a[224:227], a[208:211], v[12:15]\n"
    "s_waitcnt lgkmcnt(6)\n"
    "buffer_load_dwordx4 v64, s[44:47], s76 offen offset:0 lds\n"
    "v_mfma_f32_16x16x32_bf16 v[16:19], a[228:231], a[40:43], v[16:19]\n"
    "v_mfma_f32_16x16x32_bf16 v[20:23], a[228:231], a[96:99], v[20:23]\n"
    "v_mfma_f32_16x16x32_bf16 v[24:27], a[228:231], a[152:155], v[24:27]\n"
    "ds_read_b64_tr_b16 a[244:245], v209 offset:49152\n"
    "ds_read_b64_tr_b16 a[246:247], v209 offset:57344\n"
    "v_mfma_f32_16x16x32_bf16 v[28:31], a[228:231], a[208:211], v[28:31]\n"
    "s_waitcnt lgkmcnt(6)\n"
    "buffer_load_dwordx4 v65, s[44:47], s76 offen offset:1024 lds\n"
    "v_mfma_f32_16x16x32_bf16 v[32:35], a[232:235], a[40:43], v[32:35]\n"
    "v_mfma_f32_16x16x32_bf16 v[36:39], a[232:235], a[96:99], v[36:39]\n"
    "v_mfma_f32_16x16x32_bf16 v[40:43], a[232:235], a[152:155], v[40:43]\n"
    "ds_read_b64_tr_b16 a[248:249], v210 offset:49152\n"
    "ds_read_b64_tr_b16 a[250:251], v210 offset:57344\n"
    "v_mfma_f32_16x16x32_bf16 v[44:47], a[232:235], a[208:211], v[44:47]\n"
    "s_waitcnt lgkmcnt(6)\n"
    "buffer_load_dwordx4 v66, s[44:47], s76 offen offset:2048 lds\n"
    "v_mfma_f32_16x16x32_bf16 v[48:51], a[236:239], a[40:43], v[48:51]\n"
    "v_mfma_f32_16x16x32_bf16 v[52:55], a[236:239], a[96:99], v[52:55]\n"
    "s_waitcnt lgkmcnt(4)\n"
    "buffer_load_dwordx4 v67, s[44:47], s76 offen offset:3072 lds\n"
    "v_mfma_f32_16x16x32_bf16 v[0:3], a[240:243], a[44:47], v[0:3]\n"
    "s_mov_b32 s75, 262144\n"
    "ds_read_b64_tr_b16 a[252:253], v211 offset:49152\n"
    "ds_read_b64_tr_b16 a[254:255], v211 offset:57344\n"
    "v_mfma_f32_16x16x32_bf16 v[4:7], a[240:243], a[100:103], v[4:7]\n"
    "s_waitcnt vmcnt(12)\n"
    "s_barrier\n"
    "v_mfma_f32_16x16x32_bf16 v[8:11], a[240:243], a[156:159], v[8:11]\n"
    "s_add_u32 m0, s90, 0\n"
    "ds_read_b64_tr_b16 a[224:225], v212 offset:0\n"
    "ds_read_b64_tr_b16 a[226:227], v212 offset:8192\n"
    "v_mfma_f32_16x16x32_bf16 v[12:15], a[240:243], a[212:215], v[12:15]\n"
    "s_waitcnt lgkmcnt(6)\n"
    "buffer_load_dwordx4 v64, s[44:47], s75 offen offset:0 lds\n"
    "v_mfma_f32_16x16x32_bf16 v[16:19], a[244:247], a[44:47], v[16:19]\n"
    "v_mfma_f32_16x16x32_bf16 v[20:23], a[244:247], a[100:103], v[20:23]\n"
    "v_mfma_f32_16x16x32_bf16 v[24:27], a[244:247], a[156:159], v[24:27]\n"
    "s_add_u32 s76, s75, 512\n"
    "ds_read_b64_tr_b16 a[228:229], v213 offset:0\n"
    "ds_read_b64_tr_b16 a[230:231], v213 offset:8192\n"
    "v_mfma_f32_16x16x32_bf16 v[28:31], a[244:247], a[212:215], v[28:31]\n"
    "s_waitcnt lgkmcnt(6)\n"
    "buffer_load_dwordx4 v65, s[44:47], s75 offen offset:1024 lds\n"
    "v_mfma_f32_16x16x32_bf16 v[32:35], a[248:251], a[44:47], v[32:35]\n"
    "v_mfma_f32_16x16x32_bf16 v[36:39], a[248:251], a[100:103], v[36:39]\n"
    "v_mfma_f32_16x16x32_bf16 v[40:43], a[248:251], a[156:159], v[40:43]\n"
    "ds_read_b64_tr_b16 a[232:233], v214 offset:0\n"
    "ds_read_b64_tr_b16 a[234:235], v214 offset:8192\n"
    "v_mfma_f32_16x16x32_bf16 v[44:47], a[248:251], a[212:215], v[44:47]\n"
    "s_waitcnt lgkmcnt(6)\n"
    "buffer_load_dwordx4 v66, s[44:47], s75 offen offset:2048 lds\n"
    "v_mfma_f32_16x16x32_bf16 v[48:51], a[252:255], a[44:47], v[48:51]\n"
    "v_mfma_f32_16x16x32_bf16 v[52:55], a[252:255], a[100:103], v[52:55]\n"
    "s_waitcnt lgkmcnt(4)\n"
    "buffer_load_dwordx4 v67, s[44:47], s75 offen offset:3072 lds\n"
    "v_mfma_f32_16x16x32_bf16 v[0:3], a[224:227], a[48:51], v[0:3]\n"
    "ds_read_b64_tr_b16 a[236:237], v215 offset:0\n"
    "ds_read_b64_tr_b16 a[238:239], v215 offset:8192\n"
    "v_mfma_f32_16x16x32_bf16 v[4:7], a[224:227], a[104:107], v[4:7]\n"
    "s_waitcnt vmcnt(12)\n"
    "s_barrier\n"
    "v_mfma_f32_16x16x32_bf16 v[8:11], a[224:227], a[160:163], v[8:11]\n"
    "s_add_u32 m0, s90, 4096\n"
    "ds_read_b64_tr_b16 a[240:241], v212 offset:16384\n"
    "ds_read_b64_tr_b16 a[242:243], v212 offset:24576\n"
    "v_mfma_f32_16x16x32_bf16 v[12:15], a[224:227], a[216:219], v[12:15]\n"
    "s_waitcnt lgkmcnt(6)\n"
    "buffer_load_dwordx4 v64, s[44:47], s76 offen offset:0 lds\n"
    "v_mfma_f32_16x16x32_bf16 v[16:19], a[228:231], a[48:51], v[16:19]\n"
    "v_mfma_f32_16x16x32_bf16 v[20:23], a[228:231], a[104:107], v[20:23]\n"
    "v_mfma_f32_16x16x32_bf16 v[24:27], a[228:231], a[160:163], v[24:27]\n"
    "ds_read_b64_tr_b16 a[244:245], v213 offset:16384\n"
    "ds_read_b64_tr_b16 a[246:247], v213 offset:24576\n"
    "v_mfma_f32_16x16x32_bf16 v[28:31], a[228:231], a[216:219], v[28:31]\n"
    "s_waitcnt lgkmcnt(6)\n"
    "buffer_load_dwordx4 v65, s[44:47], s76 offen offset:1024 lds\n"
    "v_mfma_f32_16x16x32_bf16 v[32:35], a[232:235], a[48:51], v[32:35]\n"
    "v_mfma_f32_16x16x32_bf16 v[36:39], a[232:235], a[104:107], v[36:39]\n"
    "v_mfma_f32_16x16x32_bf16 v[40:43], a[232:235], a[160:163], v[40:43]\n"
    "ds_read_b64_tr_b16 a[248:249], v214 offset:16384\n"
    "ds_read_b64_tr_b16 a[250:251], v214 offset:24576\n"
    "v_mfma_f32_16x16x32_bf16 v[44:47], a[232:235], a[216:219], v[44:47]\n"
    "s_waitcnt lgkmcnt(6)\n"
    "buffer_load_dwordx4 v66, s[44:47], s76 offen offset:2048 lds\n"
    "v_mfma_f32_16x16x32_bf16 v[48:51], a[236:239], a[48:51], v[48:51]\n"
    "v_mfma_f32_16x16x32_bf16 v[52:55], a[236:239], a[104:107], v[52:55]\n"
    "s_waitcnt lgkmcnt(4)\n"
    "buffer_load_dwordx4 v67, s[44:47], s76 offen offset:3072 lds\n"
    "v_mfma_f32_16x16x32_bf16 v[0:3], a[240:243], a[52:55], v[0:3]\n"
    "s_mov_b32 s75, 524288\n"
    "ds_read_b64_tr_b16 a[252:253], v215 offset:16384\n"
    "ds_read_b64_tr_b16 a[254:255], v215 offset:24576\n"
    "v_mfma_f32_16x16x32_bf16 v[4:7], a[240:243], a[108:111], v[4:7]\n"
    "s_waitcnt vmcnt(8)\n"
    "s_barrier\n"
    "v_mfma_f32_16x16x32_bf16 v[8:11], a[240:243], a[164:167], v[8:11]\n"
    "s_add_u32 m0, s90, 32768\n"
    "ds_read_b64_tr_b16 a[224:225], v60 offset:32768\n"
    "ds_read_b64_tr_b16 a[226:227], v60 offset:49152\n"
    "v_mfma_f32_16x16x32_bf16 v[12:15], a[240:243], a[220:223], v[12:15]\n"
    "s_waitcnt lgkmcnt(6)\n"
    "buffer_load_dwordx4 v64, s[44:47], s75 offen offset:0 lds\n"
    "v_mfma_f32_16x16x32_bf16 v[16:19], a[244:247], a[52:55], v[16:19]\n"
    "v_mfma_f32_16x16x32_bf16 v[20:23], a[244:247], a[108:111], v[20:23]\n"
    "v_mfma_f32_16x16x32_bf16 v[24:27], a[244:247], a[164:167], v[24:27]\n"
    "s_add_u32 s76, s75, 512\n"
    "ds_read_b64_tr_b16 a[228:229], v61 offset:32768\n"
    "ds_read_b64_tr_b16 a[230:231], v61 offset:49152\n"
    "v_mfma_f32_16x16x32_bf16 v[28:31], a[244:247], a[220:223], v[28:31]\n"
    "s_waitcnt lgkmcnt(6)\n"
    "buffer_load_dwordx4 v65, s[44:47], s75 offen offset:1024 lds\n"
    "v_mfma_f32_16x16x32_bf16 v[32:35], a[248:251], a[52:55], v[32:35]\n"
    "v_mfma_f32_16x16x32_bf16 v[36:39], a[248:251], a[108:111], v[36:39]\n"
    "v_mfma_f32_16x16x32_bf16 v[40:43], a[248:251], a[164:167], v[40:43]\n"
    "ds_read_b64_tr_b16 a[232:233], v62 offset:32768\n"
    "ds_read_b64_tr_b16 a[234:235], v62 offset:49152\n"
    "v_mfma_f32_16x16x32_bf16 v[44:47], a[248:251], a[220:223], v[44:47]\n"
    "s_waitcnt lgkmcnt(6)\n"
    "buffer_load_dwordx4 v66, s[44:47], s75 offen offset:2048 lds\n"
    "v_mfma_f32_16x16x32_bf16 v[48:51], a[252:255], a[52:55], v[48:51]\n"
    "v_mfma_f32_16x16x32_bf16 v[52:55], a[252:255], a[108:111], v[52:55]\n"
    "s_waitcnt lgkmcnt(4)\n"
    "buffer_load_dwordx4 v67, s[44:47], s75 offen offset:3072 lds\n"
    "v_mfma_f32_16x16x32_bf16 v[128:131], a[224:227], a[0:3], 0\n"
    "s_add_u32 m0, s90, 36864\n"
    "ds_read_b64_tr_b16 a[236:237], v63 offset:32768\n"
    "ds_read_b64_tr_b16 a[238:239], v63 offset:49152\n"
    "v_mfma_f32_16x16x32_bf16 v[132:135], a[224:227], a[56:59], 0\n"
    "buffer_load_dwordx4 v64, s[44:47], s76 offen offset:0 lds\n"
    "v_mfma_f32_16x16x32_bf16 v[192:195], a[224:227], a[112:115], 0\n"
    "v_mfma_f32_16x16x32_bf16 v[196:199], a[224:227], a[168:171], 0\n"
    "s_waitcnt lgkmcnt(4)\n"
    "v_mfma_f32_16x16x32_bf16 v[136:139], a[228:231], a[0:3], 0\n"
    "ds_read_b64_tr_b16 a[240:241], v60 offset:33792\n"
    "ds_read_b64_tr_b16 a[242:243], v60 offset:50176\n"
    "v_mfma_f32_16x16x32_bf16 v[140:143], a[228:231], a[56:59], 0\n"
    "buffer_load_dwordx4 v65, s[44:47], s76 offen offset:1024 lds\n"
    "v_mfma_f32_16x16x32_bf16 v[200:203], a[228:231], a[112:115], 0\n"
    "v_mbcnt_lo_u32_b32 v104, -1, 0\n"
    "v_mbcnt_hi_u32_b32 v104, -1, v104\n"
    "v_mfma_f32_16x16x32_bf16 v[204:207], a[228:231], a[168:171], 0\n"
    "s_waitcnt lgkmcnt(4)\n"
    "s_mul_i32 s86, s69, 5120\n"
    "s_add_u32 s86, s86, 131072\n"
    "v_lshl_add_u32 v104, v104, 4, s86\n"
    "v_xor_b32 v105, 1024, v104\n"
    "v_mfma_f32_16x16x32_bf16 v[144:147], a[232:235], a[0:3], 0\n"
    "ds_read_b64_tr_b16 a[244:245], v61 offset:33792\n"
    "ds_read_b64_tr_b16 a[246:247], v61 offset:50176\n"
    "v_xor_b32 v106, 2048, v104\n"
    "v_mfma_f32_16x16x32_bf16 v[148:151], a[232:235], a[56:59], 0\n"
    "buffer_load_dwordx4 v66, s[44:47], s76 offen offset:2048 lds\n"
    "v_mfma_f32_16x16x32_bf16 v[208:211], a[232:235], a[112:115], 0\n"
    "v_xor_b32 v107, 3072, v104\n"
    "v_mul_f32 v84, v0, v0\n"
    "v_mul_f32 v85, v1, v1\n"
    "v_mfma_f32_16x16x32_bf16 v[212:215], a[232:235], a[168:171], 0\n"
    "s_waitcnt lgkmcnt(4)\n"
    "v_mul_f32 v86, v2, v2\n"
    "v_mul_f32 v87, v3, v3\n"
    "v_mfma_f32_16x16x32_bf16 v[152:155], a[236:239], a[0:3], 0\n"
    "ds_read_b64_tr_b16 a[248:249], v62 offset:33792\n"
    "ds_read_b64_tr_b16 a[250:251], v62 offset:50176\n"
    "v_fma_f32 v84, v84, v100, v80\n"
    "v_mfma_f32_16x16x32_bf16 v[156:159], a[236:239], a[56:59], 0\n"
    "buffer_load_dwordx4 v67, s[44:47], s76 offen offset:3072 lds\n"
    "v_fma_f32 v85, v85, v100, v80\n"
    "v_mfma_f32_16x16x32_bf16 v[216:219], a[236:239], a[112:115], 0\n"
    "v_fma_f32 v86, v86, v100, v80\n"
    "v_fma_f32 v87, v87, v100, v80\n"
    "v_mfma_f32_16x16x32_bf16 v[220:223], a[236:239], a[168:171], 0\n"
    "s_waitcnt lgkmcnt(4)\n"
    "v_mul_f32 v84, v84, v0\n"
    "v_mul_f32 v85, v85, v1\n"
    "v_mul_f32 v86, v86, v2\n"
    "v_mfma_f32_16x16x32_bf16 v[160:163], a[240:243], a[0:3], 0\n"
    "ds_read_b64_tr_b16 a[252:253], v63 offset:33792\n"
    "ds_read_b64_tr_b16 a[254:255], v63 offset:50176\n"
    "v_mfma_f32_16x16x32_bf16 v[164:167], a[240:243], a[56:59], 0\n"
    "v_mul_f32 v87, v87, v3\n"
    "v_exp_f32 v84, v84\n"
    "v_mfma_f32_16x16x32_bf16 v[224:227], a[240:243], a[112:115], 0\n"
    "s_mov_b32 s75, 786432\n"
    "v_exp_f32 v85, v85\n"
    "v_mfma_f32_16x16x32_bf16 v[228:231], a[240:243], a[168:171], 0\n"
    "s_waitcnt vmcnt(8) lgkmcnt(4)\n"
    "s_barrier\n"
    "v_mfma_f32_16x16x32_bf16 v[168:171], a[244:247], a[0:3], 0\n"
    "s_add_u32 m0, s90, 65536\n"
    "ds_read_b64_tr_b16 a[224:225], v56 offset:0\n"
    "ds_read_b64_tr_b16 a[226:227], v56 offset:16384\n"
    "v_exp_f32 v86, v86\n"
    "v_mfma_f32_16x16x32_bf16 v[172:175], a[244:247], a[56:59], 0\n"
    "buffer_load_dwordx4 v64, s[44:47], s75 offen offset:0 lds\n"
    "v_mfma_f32_16x16x32_bf16 v[232:235], a[244:247], a[112:115], 0\n"
    "v_exp_f32 v87, v87\n"
    "v_mfma_f32_16x16x32_bf16 v[236:239], a[244:247], a[168:171], 0\n"
    "s_waitcnt lgkmcnt(4)\n"
    "v_fma_f32 v84, v84, v76, v76\n"
    "v_fma_f32 v85, v85, v76, v76\n"
    "v_fma_f32 v86, v86, v76, v76\n"
    "v_mfma_f32_16x16x32_bf16 v[176:179], a[248:251], a[0:3], 0\n"
    "s_add_u32 s76, s75, 512\n"
    "ds_read_b64_tr_b16 a[228:229], v57 offset:0\n"
    "ds_read_b64_tr_b16 a[230:231], v57 offset:16384\n"
    "v_mfma_f32_16x16x32_bf16 v[180:183], a[248:251], a[56:59], 0\n"
    "buffer_load_dwordx4 v65, s[44:47], s75 offen offset:1024 lds\n"
    "v_fma_f32 v87, v87, v76, v76\n"
    "v_mfma_f32_16x16x32_bf16 v[240:243], a[248:251], a[112:115], 0\n"
    "v_rcp_f32 v84, v84\n"
    "v_mfma_f32_16x16x32_bf16 v[244:247], a[248:251], a[168:171], 0\n"
    "s_waitcnt lgkmcnt(4)\n"
    "v_rcp_f32 v85, v85\n"
    "v_rcp_f32 v86, v86\n"
    "v_mfma_f32_16x16x32_bf16 v[184:187], a[252:255], a[0:3], 0\n"
    "ds_read_b64_tr_b16 a[232:233], v58 offset:0\n"
    "ds_read_b64_tr_b16 a[234:235], v58 offset:16384\n"
    "v_mfma_f32_16x16x32_bf16 v[188:191], a[252:255], a[56:59], 0\n"
    "buffer_load_dwordx4 v66, s[44:47], s75 offen offset:2048 lds\n"
    "v_mfma_f32_16x16x32_bf16 v[248:251], a[252:255], a[112:115], 0\n"
    "v_rcp_f32 v87, v87\n"
    "v_mul_f32 v0, v0, v84\n"
    "v_mfma_f32_16x16x32_bf16 v[252:255], a[252:255], a[168:171], 0\n"
    "s_waitcnt lgkmcnt(4)\n"
    "v_mul_f32 v1, v1, v85\n"
    "v_mul_f32 v2, v2, v86\n"
    "v_mfma_f32_16x16x32_bf16 v[128:131], a[224:227], a[4:7], v[128:131]\n"
    "ds_read_b64_tr_b16 a[236:237], v59 offset:0\n"
    "ds_read_b64_tr_b16 a[238:239], v59 offset:16384\n"
    "v_mul_f32 v3, v3, v87\n"
    "v_mfma_f32_16x16x32_bf16 v[132:135], a[224:227], a[60:63], v[132:135]\n"
    "buffer_load_dwordx4 v67, s[44:47], s75 offen offset:3072 lds\n"
    "v_cvt_pk_bf16_f32 v68, v0, v1\n"
    "v_mfma_f32_16x16x32_bf16 v[192:195], a[224:227], a[116:119], v[192:195]\n"
    "v_cvt_pk_bf16_f32 v69, v2, v3\n"
    "v_mul_f32 v88, v16, v16\n"
    "v_mfma_f32_16x16x32_bf16 v[196:199], a[224:227], a[172:175], v[196:199]\n"
    "s_waitcnt lgkmcnt(4)\n"
    "v_mul_f32 v89, v17, v17\n"
    "v_mul_f32 v90, v18, v18\n"
    "v_mul_f32 v91, v19, v19\n"
    "v_mfma_f32_16x16x32_bf16 v[136:139], a[228:231], a[4:7], v[136:139]\n"
    "s_add_u32 m0, s90, 69632\n"
    "ds_read_b64_tr_b16 a[240:241], v56 offset:1024\n"
    "ds_read_b64_tr_b16 a[242:243], v56 offset:17408\n"
    "v_mfma_f32_16x16x32_bf16 v[140:143], a[228:231], a[60:63], v[140:143]\n"
    "buffer_load_dwordx4 v64, s[44:47], s76 offen offset:0 lds\n"
    "v_fma_f32 v88, v88, v100, v80\n"
    "v_mfma_f32_16x16x32_bf16 v[200:203], a[228:231], a[116:119], v[200:203]\n"
    "v_fma_f32 v89, v89, v100, v80\n"
    "v_fma_f32 v90, v90, v100, v80\n"
    "v_mfma_f32_16x16x32_bf16 v[204:207], a[228:231], a[172:175], v[204:207]\n"
    "s_waitcnt lgkmcnt(4)\n"
    "v_fma_f32 v91, v91, v100, v80\n"
    "v_mul_f32 v88, v88, v16\n"
    "v_mul_f32 v89, v89, v17\n"
    "v_mfma_f32_16x16x32_bf16 v[144:147], a[232:235], a[4:7], v[144:147]\n"
    "ds_read_b64_tr_b16 a[244:245], v57 offset:1024\n"
    "ds_read_b64_tr_b16 a[246:247], v57 offset:17408\n"
    "v_mul_f32 v90, v90, v18\n"
    "v_mfma_f32_16x16x32_bf16 v[148:151], a[232:235], a[60:63], v[148:151]\n"
    "buffer_load_dwordx4 v65, s[44:47], s76 offen offset:1024 lds\n"
    "v_mfma_f32_16x16x32_bf16 v[208:211], a[232:235], a[116:119], v[208:211]\n"
    "v_mul_f32 v91, v91, v19\n"
    "v_exp_f32 v88, v88\n"
    "v_mfma_f32_16x16x32_bf16 v[212:215], a[232:235], a[172:175], v[212:215]\n"
    "s_waitcnt lgkmcnt(4)\n"
    "v_exp_f32 v89, v89\n"
    "v_mfma_f32_16x16x32_bf16 v[152:155], a[236:239], a[4:7], v[152:155]\n"
    "ds_read_b64_tr_b16 a[248:249], v58 offset:1024\n"
    "ds_read_b64_tr_b16 a[250:251], v58 offset:17408\n"
    "v_mfma_f32_16x16x32_bf16 v[156:159], a[236:239], a[60:63], v[156:159]\n"
    "buffer_load_dwordx4 v66, s[44:47], s76 offen offset:2048 lds\n"
    "v_exp_f32 v90, v90\n"
    "v_mfma_f32_16x16x32_bf16 v[216:219], a[236:239], a[116:119], v[216:219]\n"
    "v_exp_f32 v91, v91\n"
    "v_mfma_f32_16x16x32_bf16 v[220:223], a[236:239], a[172:175], v[220:223]\n"
    "s_waitcnt lgkmcnt(4)\n"
    "v_fma_f32 v88, v88, v76, v76\n"
    "v_fma_f32 v89, v89, v76, v76\n"
    "v_fma_f32 v90, v90, v76, v76\n"
    "v_mfma_f32_16x16x32_bf16 v[160:163], a[240:243], a[4:7], v[160:163]\n"
    "ds_read_b64_tr_b16 a[252:253], v59 offset:1024\n"
    "ds_read_b64_tr_b16 a[254:255], v59 offset:17408\n"
    "v_mfma_f32_16x16x32_bf16 v[164:167], a[240:243], a[60:63], v[164:167]\n"
    "buffer_load_dwordx4 v67, s[44:47], s76 offen offset:3072 lds\n"
    "v_fma_f32 v91, v91, v76, v76\n"
    "v_mfma_f32_16x16x32_bf16 v[224:227], a[240:243], a[116:119], v[224:227]\n"
    "s_mov_b32 s75, 1048576\n"
    "v_rcp_f32 v88, v88\n"
    "v_mfma_f32_16x16x32_bf16 v[228:231], a[240:243], a[172:175], v[228:231]\n"
    "s_waitcnt vmcnt(8) lgkmcnt(4)\n"
    "s_barrier\n"
    "v_mfma_f32_16x16x32_bf16 v[168:171], a[244:247], a[4:7], v[168:171]\n"
    "s_add_u32 m0, s90, 98304\n"
    "ds_read_b64_tr_b16 a[224:225], v56 offset:32768\n"
    "ds_read_b64_tr_b16 a[226:227], v56 offset:49152\n"
    "v_mfma_f32_16x16x32_bf16 v[172:175], a[244:247], a[60:63], v[172:175]\n"
    "buffer_load_dwordx4 v64, s[44:47], s75 offen offset:0 lds\n"
    "v_rcp_f32 v89, v89\n"
    "v_mfma_f32_16x16x32_bf16 v[232:235], a[244:247], a[116:119], v[232:235]\n"
    "v_rcp_f32 v90, v90\n"
    "v_mfma_f32_16x16x32_bf16 v[236:239], a[244:247], a[172:175], v[236:239]\n"
    "s_waitcnt lgkmcnt(4)\n"
    "v_rcp_f32 v91, v91\n"
    "v_mul_f32 v16, v16, v88\n"
    "v_mfma_f32_16x16x32_bf16 v[176:179], a[248:251], a[4:7], v[176:179]\n"
    "s_add_u32 s76, s75, 512\n"
    "ds_read_b64_tr_b16 a[228:229], v57 offset:32768\n"
    "ds_read_b64_tr_b16 a[230:231], v57 offset:49152\n"
    "v_mfma_f32_16x16x32_bf16 v[180:183], a[248:251], a[60:63], v[180:183]\n"
    "buffer_load_dwordx4 v65, s[44:47], s75 offen offset:1024 lds\n"
    "v_mul_f32 v17, v17, v89\n"
    "v_mfma_f32_16x16x32_bf16 v[240:243], a[248:251], a[116:119], v[240:243]\n"
    "v_mul_f32 v18, v18, v90\n"
    "v_mul_f32 v19, v19, v91\n"
    "v_mfma_f32_16x16x32_bf16 v[244:247], a[248:251], a[172:175], v[244:247]\n"
    "s_waitcnt lgkmcnt(4)\n"
    "v_cvt_pk_bf16_f32 v70, v16, v17\n"
    "v_cvt_pk_bf16_f32 v71, v18, v19\n"
    "v_mfma_f32_16x16x32_bf16 v[184:187], a[252:255], a[4:7], v[184:187]\n"
    "ds_read_b64_tr_b16 a[232:233], v58 offset:32768\n"
    "ds_read_b64_tr_b16 a[234:235], v58 offset:49152\n"
    "v_mfma_f32_16x16x32_bf16 v[188:191], a[252:255], a[60:63], v[188:191]\n"
    "buffer_load_dwordx4 v66, s[44:47], s75 offen offset:2048 lds\n"
    "v_mfma_f32_16x16x32_bf16 v[248:251], a[252:255], a[116:119], v[248:251]\n"
    "v_mfma_f32_16x16x32_bf16 v[252:255], a[252:255], a[172:175], v[252:255]\n"
    "s_waitcnt lgkmcnt(4)\n"
    "v_mul_f32 v92, v4, v4\n"
    "v_mul_f32 v93, v5, v5\n"
    "v_mfma_f32_16x16x32_bf16 v[128:131], a[224:227], a[8:11], v[128:131]\n"
    "ds_read_b64_tr_b16 a[236:237], v59 offset:32768\n"
    "ds_read_b64_tr_b16 a[238:239], v59 offset:49152\n"
    "v_mul_f32 v94, v6, v6\n"
    "v_mfma_f32_16x16x32_bf16 v[132:135], a[224:227], a[64:67], v[132:135]\n"
    "buffer_load_dwordx4 v67, s[44:47], s75 offen offset:3072 lds\n"
    "v_mfma_f32_16x16x32_bf16 v[192:195], a[224:227], a[120:123], v[192:195]\n"
    "ds_write_b128 v104, v[68:71]\n"
    "v_mul_f32 v95, v7, v7\n"
    "v_fma_f32 v92, v92, v101, v81\n"
    "v_fma_f32 v93, v93, v101, v81\n"
    "v_mfma_f32_16x16x32_bf16 v[196:199], a[224:227], a[176:179], v[196:199]\n"
    "s_waitcnt lgkmcnt(5)\n"
    "v_fma_f32 v94, v94, v101, v81\n"
    "v_fma_f32 v95, v95, v101, v81\n"
    "v_mul_f32 v92, v92, v4\n"
    "v_mfma_f32_16x16x32_bf16 v[136:139], a[228:231], a[8:11], v[136:139]\n"
    "s_add_u32 m0, s90, 102400\n"
    "ds_read_b64_tr_b16 a[240:241], v56 offset:33792\n"
    "ds_read_b64_tr_b16 a[242:243], v56 offset:50176\n"
    "v_mfma_f32_16x16x32_bf16 v[140:143], a[228:231], a[64:67], v[140:143]\n"
    "buffer_load_dwordx4 v64, s[44:47], s76 offen offset:0 lds\n"
    "v_mul_f32 v93, v93, v5\n"
    "v_mfma_f32_16x16x32_bf16 v[200:203], a[228:231], a[120:123], v[200:203]\n"
    "v_mul_f32 v94, v94, v6\n"
    "v_mul_f32 v95, v95, v7\n"
    "v_mfma_f32_16x16x32_bf16 v[204:207], a[228:231], a[176:179], v[204:207]\n"
    "s_waitcnt lgkmcnt(5)\n"
    "v_exp_f32 v92, v92\n"
    "v_mfma_f32_16x16x32_bf16 v[144:147], a[232:235], a[8:11], v[144:147]\n"
    "ds_read_b64_tr_b16 a[244:245], v57 offset:33792\n"
    "ds_read_b64_tr_b16 a[246:247], v57 offset:50176\n"
    "v_exp_f32 v93, v93\n"
    "v_mfma_f32_16x16x32_bf16 v[148:151], a[232:235], a[64:67], v[148:151]\n"
    "buffer_load_dwordx4 v65, s[44:47], s76 offen offset:1024 lds\n"
    "v_mfma_f32_16x16x32_bf16 v[208:211], a[232:235], a[120:123], v[208:211]\n"
    "v_exp_f32 v94, v94\n"
    "v_mfma_f32_16x16x32_bf16 v[212:215], a[232:235], a[176:179], v[212:215]\n"
    "s_waitcnt lgkmcnt(5)\n"
    "v_exp_f32 v95, v95\n"
    "v_fma_f32 v92, v92, v77, v77\n"
    "v_mfma_f32_16x16x32_bf16 v[152:155], a[236:239], a[8:11], v[152:155]\n"
    "ds_read_b64_tr_b16 a[248:249], v58 offset:33792\n"
    "ds_read_b64_tr_b16 a[250:251], v58 offset:50176\n"
    "v_fma_f32 v93, v93, v77, v77\n"
    "v_mfma_f32_16x16x32_bf16 v[156:159], a[236:239], a[64:67], v[156:159]\n"
    "buffer_load_dwordx4 v66, s[44:47], s76 offen offset:2048 lds\n"
    "v_mfma_f32_16x16x32_bf16 v[216:219], a[236:239], a[120:123], v[216:219]\n"
    "v_fma_f32 v94, v94, v77, v77\n"
    "v_fma_f32 v95, v95, v77, v77\n"
    "v_mfma_f32_16x16x32_bf16 v[220:223], a[236:239], a[176:179], v[220:223]\n"
    "s_waitcnt lgkmcnt(4)\n"
    "v_rcp_f32 v92, v92\n"
    "v_rcp_f32 v93, v93\n"
    "v_mfma_f32_16x16x32_bf16 v[160:163], a[240:243], a[8:11], v[160:163]\n"
    "ds_read_b64_tr_b16 a[252:253], v59 offset:33792\n"
    "ds_read_b64_tr_b16 a[254:255], v59 offset:50176\n"
    "v_mfma_f32_16x16x32_bf16 v[164:167], a[240:243], a[64:67], v[164:167]\n"
    "buffer_load_dwordx4 v67, s[44:47], s76 offen offset:3072 lds\n"
    "v_mfma_f32_16x16x32_bf16 v[224:227], a[240:243], a[120:123], v[224:227]\n"
    "s_mov_b32 s75, 1310720\n"
    "v_rcp_f32 v94, v94\n"
    "v_rcp_f32 v95, v95\n"
    "v_mfma_f32_16x16x32_bf16 v[228:231], a[240:243], a[176:179], v[228:231]\n"
    "s_waitcnt vmcnt(8) lgkmcnt(4)\n"
    "s_barrier\n"
    "v_mfma_f32_16x16x32_bf16 v[168:171], a[244:247], a[8:11], v[168:171]\n"
    "s_add_u32 m0, s90, 0\n"
    "ds_read_b64_tr_b16 a[224:225], v60 offset:0\n"
    "ds_read_b64_tr_b16 a[226:227], v60 offset:16384\n"
    "v_mfma_f32_16x16x32_bf16 v[172:175], a[244:247], a[64:67], v[172:175]\n"
    "buffer_load_dwordx4 v64, s[44:47], s75 offen offset:0 lds\n"
    "v_mfma_f32_16x16x32_bf16 v[232:235], a[244:247], a[120:123], v[232:235]\n"
    "v_mul_f32 v4, v4, v92\n"
    "v_mul_f32 v5, v5, v93\n"
    "v_mul_f32 v6, v6, v94\n"
    "v_mfma_f32_16x16x32_bf16 v[236:239], a[244:247], a[176:179], v[236:239]\n"
    "s_waitcnt lgkmcnt(4)\n"
    "v_mul_f32 v7, v7, v95\n"
    "v_cvt_pk_bf16_f32 v72, v4, v5\n"
    "v_mfma_f32_16x16x32_bf16 v[176:179], a[248:251], a[8:11], v[176:179]\n"
    "s_add_u32 s76, s75, 512\n"
    "ds_read_b64_tr_b16 a[228:229], v61 offset:0\n"
    "ds_read_b64_tr_b16 a[230:231], v61 offset:16384\n"
    "v_cvt_pk_bf16_f32 v73, v6, v7\n"
    "v_mfma_f32_16x16x32_bf16 v[180:183], a[248:251], a[64:67], v[180:183]\n"
    "buffer_load_dwordx4 v65, s[44:47], s75 offen offset:1024 lds\n"
    "v_mul_f32 v96, v20, v20\n"
    "v_mfma_f32_16x16x32_bf16 v[240:243], a[248:251], a[120:123], v[240:243]\n"
    "v_mul_f32 v97, v21, v21\n"
    "v_mul_f32 v98, v22, v22\n"
    "v_mfma_f32_16x16x32_bf16 v[244:247], a[248:251], a[176:179], v[244:247]\n"
    "s_waitcnt lgkmcnt(4)\n"
    "v_mul_f32 v99, v23, v23\n"
    "v_fma_f32 v96, v96, v101, v81\n"
    "v_fma_f32 v97, v97, v101, v81\n"
    "v_mfma_f32_16x16x32_bf16 v[184:187], a[252:255], a[8:11], v[184:187]\n"
    "ds_read_b64_tr_b16 a[232:233], v62 offset:0\n"
    "ds_read_b64_tr_b16 a[234:235], v62 offset:16384\n"
    "v_mfma_f32_16x16x32_bf16 v[188:191], a[252:255], a[64:67], v[188:191]\n"
    "buffer_load_dwordx4 v66, s[44:47], s75 offen offset:2048 lds\n"
    "v_fma_f32 v98, v98, v101, v81\n"
    "v_mfma_f32_16x16x32_bf16 v[248:251], a[252:255], a[120:123], v[248:251]\n"
    "v_fma_f32 v99, v99, v101, v81\n"
    "v_mul_f32 v96, v96, v20\n"
    "v_mfma_f32_16x16x32_bf16 v[252:255], a[252:255], a[176:179], v[252:255]\n"
    "s_waitcnt lgkmcnt(4)\n"
    "v_mul_f32 v97, v97, v21\n"
    "v_mul_f32 v98, v98, v22\n"
    "v_mul_f32 v99, v99, v23\n"
    "v_mfma_f32_16x16x32_bf16 v[128:131], a[224:227], a[12:15], v[128:131]\n"
    "ds_read_b64_tr_b16 a[236:237], v63 offset:0\n"
    "ds_read_b64_tr_b16 a[238:239], v63 offset:16384\n"
    "v_mfma_f32_16x16x32_bf16 v[132:135], a[224:227], a[68:71], v[132:135]\n"
    "buffer_load_dwordx4 v67, s[44:47], s75 offen offset:3072 lds\n"
    "v_exp_f32 v96, v96\n"
    "v_mfma_f32_16x16x32_bf16 v[192:195], a[224:227], a[124:127], v[192:195]\n"
    "v_exp_f32 v97, v97\n"
    "v_mfma_f32_16x16x32_bf16 v[196:199], a[224:227], a[180:183], v[196:199]\n"
    "s_waitcnt lgkmcnt(4)\n"
    "v_exp_f32 v98, v98\n"
    "v_mfma_f32_16x16x32_bf16 v[136:139], a[228:231], a[12:15], v[136:139]\n"
    "s_add_u32 m0, s90, 4096\n"
    "ds_read_b64_tr_b16 a[240:241], v60 offset:1024\n"
    "ds_read_b64_tr_b16 a[242:243], v60 offset:17408\n"
    "v_mfma_f32_16x16x32_bf16 v[140:143], a[228:231], a[68:71], v[140:143]\n"
    "buffer_load_dwordx4 v64, s[44:47], s76 offen offset:0 lds\n"
    "v_exp_f32 v99, v99\n"
    "v_mfma_f32_16x16x32_bf16 v[200:203], a[228:231], a[124:127], v[200:203]\n"
    "v_fma_f32 v96, v96, v77, v77\n"
    "v_fma_f32 v97, v97, v77, v77\n"
    "v_mfma_f32_16x16x32_bf16 v[204:207], a[228:231], a[180:183], v[204:207]\n"
    "s_waitcnt lgkmcnt(4)\n"
    "v_fma_f32 v98, v98, v77, v77\n"
    "v_fma_f32 v99, v99, v77, v77\n"
    "v_mfma_f32_16x16x32_bf16 v[144:147], a[232:235], a[12:15], v[144:147]\n"
    "ds_read_b64_tr_b16 a[244:245], v61 offset:1024\n"
    "ds_read_b64_tr_b16 a[246:247], v61 offset:17408\n"
    "v_rcp_f32 v96, v96\n"
    "v_mfma_f32_16x16x32_bf16 v[148:151], a[232:235], a[68:71], v[148:151]\n"
    "buffer_load_dwordx4 v65, s[44:47], s76 offen offset:1024 lds\n"
    "v_mfma_f32_16x16x32_bf16 v[208:211], a[232:235], a[124:127], v[208:211]\n"
    "v_rcp_f32 v97, v97\n"
    "v_mfma_f32_16x16x32_bf16 v[212:215], a[232:235], a[180:183], v[212:215]\n"
    "s_waitcnt lgkmcnt(4)\n"
    "v_rcp_f32 v98, v98\n"
    "v_mfma_f32_16x16x32_bf16 v[152:155], a[236:239], a[12:15], v[152:155]\n"
    "ds_read_b64_tr_b16 a[248:249], v62 offset:1024\n"
    "ds_read_b64_tr_b16 a[250:251], v62 offset:17408\n"
    "v_rcp_f32 v99, v99\n"
    "v_mfma_f32_16x16x32_bf16 v[156:159], a[236:239], a[68:71], v[156:159]\n"
    "buffer_load_dwordx4 v66, s[44:47], s76 offen offset:2048 lds\n"
    "v_mfma_f32_16x16x32_bf16 v[216:219], a[236:239], a[124:127], v[216:219]\n"
    "v_mul_f32 v20, v20, v96\n"
    "v_mul_f32 v21, v21, v97\n"
    "v_mul_f32 v22, v22, v98\n"
    "v_mfma_f32_16x16x32_bf16 v[220:223], a[236:239], a[180:183], v[220:223]\n"
    "s_waitcnt lgkmcnt(4)\n"
    "v_mul_f32 v23, v23, v99\n"
    "v_cvt_pk_bf16_f32 v74, v20, v21\n"
    "v_mfma_f32_16x16x32_bf16 v[160:163], a[240:243], a[12:15], v[160:163]\n"
    "ds_read_b64_tr_b16 a[252:253], v63 offset:1024\n"
    "ds_read_b64_tr_b16 a[254:255], v63 offset:17408\n"
    "v_cvt_pk_bf16_f32 v75, v22, v23\n"
    "v_mfma_f32_16x16x32_bf16 v[164:167], a[240:243], a[68:71], v[164:167]\n"
    "buffer_load_dwordx4 v67, s[44:47], s76 offen offset:3072 lds\n"
    "v_mfma_f32_16x16x32_bf16 v[224:227], a[240:243], a[124:127], v[224:227]\n"
    "s_mov_b32 s75, 1572864\n"
    "v_mfma_f32_16x16x32_bf16 v[228:231], a[240:243], a[180:183], v[228:231]\n"
    "s_waitcnt vmcnt(8) lgkmcnt(4)\n"
    "s_barrier\n"
    "v_mfma_f32_16x16x32_bf16 v[168:171], a[244:247], a[12:15], v[168:171]\n"
    "s_add_u32 m0, s90, 32768\n"
    "ds_read_b64_tr_b16 a[224:225], v60 offset:32768\n"
    "ds_read_b64_tr_b16 a[226:227], v60 offset:49152\n"
    "v_mfma_f32_16x16x32_bf16 v[172:175], a[244:247], a[68:71], v[172:175]\n"
    "buffer_load_dwordx4 v64, s[44:47], s75 offen offset:0 lds\n"
    "v_mfma_f32_16x16x32_bf16 v[232:235], a[244:247], a[124:127], v[232:235]\n"
    "ds_write_b128 v105, v[72:75]\n"
    "v_mul_f32 v84, v8, v8\n"
    "v_mul_f32 v85, v9, v9\n"
    "v_mfma_f32_16x16x32_bf16 v[236:239], a[244:247], a[180:183], v[236:239]\n"
    "s_waitcnt lgkmcnt(5)\n"
    "v_mul_f32 v86, v10, v10\n"
    "v_mul_f32 v87, v11, v11\n"
    "v_mfma_f32_16x16x32_bf16 v[176:179], a[248:251], a[12:15], v[176:179]\n"
    "s_add_u32 s76, s75, 512\n"
    "ds_read_b64_tr_b16 a[228:229], v61 offset:32768\n"
    "ds_read_b64_tr_b16 a[230:231], v61 offset:49152\n"
    "v_fma_f32 v84, v84, v102, v82\n"
    "v_mfma_f32_16x16x32_bf16 v[180:183], a[248:251], a[68:71], v[180:183]\n"
    "buffer_load_dwordx4 v65, s[44:47], s75 offen offset:1024 lds\n"
    "v_mfma_f32_16x16x32_bf16 v[240:243], a[248:251], a[124:127], v[240:243]\n"
    "v_fma_f32 v85, v85, v102, v82\n"
    "v_fma_f32 v86, v86, v102, v82\n"
    "v_fma_f32 v87, v87, v102, v82\n"
    "v_mfma_f32_16x16x32_bf16 v[244:247], a[248:251], a[180:183], v[244:247]\n"
    "s_waitcnt lgkmcnt(5)\n"
    "v_mul_f32 v84, v84, v8\n"
    "v_mul_f32 v85, v85, v9\n"
    "v_mul_f32 v86, v86, v10\n"
    "v_mfma_f32_16x16x32_bf16 v[184:187], a[252:255], a[12:15], v[184:187]\n"
    "ds_read_b64_tr_b16 a[232:233], v62 offset:32768\n"
    "ds_read_b64_tr_b16 a[234:235], v62 offset:49152\n"
    "v_mfma_f32_16x16x32_bf16 v[188:191], a[252:255], a[68:71], v[188:191]\n"
    "buffer_load_dwordx4 v66, s[44:47], s75 offen offset:2048 lds\n"
    "v_mul_f32 v87, v87, v11\n"
    "v_mfma_f32_16x16x32_bf16 v[248:251], a[252:255], a[124:127], v[248:251]\n"
    "v_exp_f32 v84, v84\n"
    "v_mfma_f32_16x16x32_bf16 v[252:255], a[252:255], a[180:183], v[252:255]\n"
    "s_waitcnt lgkmcnt(5)\n"
    "v_exp_f32 v85, v85\n"
    "v_mfma_f32_16x16x32_bf16 v[128:131], a[224:227], a[16:19], v[128:131]\n"
    "ds_read_b64_tr_b16 a[236:237], v63 offset:32768\n"
    "ds_read_b64_tr_b16 a[238:239], v63 offset:49152\n"
    "v_exp_f32 v86, v86\n"
    "v_mfma_f32_16x16x32_bf16 v[132:135], a[224:227], a[72:75], v[132:135]\n"
    "buffer_load_dwordx4 v67, s[44:47], s75 offen offset:3072 lds\n"
    "v_mfma_f32_16x16x32_bf16 v[192:195], a[224:227], a[128:131], v[192:195]\n"
    "v_exp_f32 v87, v87\n"
    "v_fma_f32 v84, v84, v78, v78\n"
    "v_mfma_f32_16x16x32_bf16 v[196:199], a[224:227], a[184:187], v[196:199]\n"
    "s_waitcnt lgkmcnt(4)\n"
    "v_fma_f32 v85, v85, v78, v78\n"
    "v_fma_f32 v86, v86, v78, v78\n"
    "v_mfma_f32_16x16x32_bf16 v[136:139], a[228:231], a[16:19], v[136:139]\n"
    "s_add_u32 m0, s90, 36864\n"
    "ds_read_b64_tr_b16 a[240:241], v60 offset:33792\n"
    "ds_read_b64_tr_b16 a[242:243], v60 offset:50176\n"
    "v_fma_f32 v87, v87, v78, v78\n"
    "v_mfma_f32_16x16x32_bf16 v[140:143], a[228:231], a[72:75], v[140:143]\n"
    "buffer_load_dwordx4 v64, s[44:47], s76 offen offset:0 lds\n"
    "v_mfma_f32_16x16x32_bf16 v[200:203], a[228:231], a[128:131], v[200:203]\n"
    "v_rcp_f32 v84, v84\n"
    "v_mfma_f32_16x16x32_bf16 v[204:207], a[228:231], a[184:187], v[204:207]\n"
    "s_waitcnt lgkmcnt(4)\n"
    "v_rcp_f32 v85, v85\n"
    "v_rcp_f32 v86, v86\n"
    "v_mfma_f32_16x16x32_bf16 v[144:147], a[232:235], a[16:19], v[144:147]\n"
    "ds_read_b64_tr_b16 a[244:245], v61 offset:33792\n"
    "ds_read_b64_tr_b16 a[246:247], v61 offset:50176\n"
    "v_mfma_f32_16x16x32_bf16 v[148:151], a[232:235], a[72:75], v[148:151]\n"
    "buffer_load_dwordx4 v65, s[44:47], s76 offen offset:1024 lds\n"
    "v_mfma_f32_16x16x32_bf16 v[208:211], a[232:235], a[128:131], v[208:211]\n"
    "v_rcp_f32 v87, v87\n"
    "v_mul_f32 v8, v8, v84\n"
    "v_mfma_f32_16x16x32_bf16 v[212:215], a[232:235], a[184:187], v[212:215]\n"
    "s_waitcnt lgkmcnt(4)\n"
    "v_mul_f32 v9, v9, v85\n"
    "v_mul_f32 v10, v10, v86\n"
    "v_mul_f32 v11, v11, v87\n"
    "v_mfma_f32_16x16x32_bf16 v[152:155], a[236:239], a[16:19], v[152:155]\n"
    "ds_read_b64_tr_b16 a[248:249], v62 offset:33792\n"
    "ds_read_b64_tr_b16 a[250:251], v62 offset:50176\n"
    "v_mfma_f32_16x16x32_bf16 v[156:159], a[236:239], a[72:75], v[156:159]\n"
    "buffer_load_dwordx4 v66, s[44:47], s76 offen offset:2048 lds\n"
    "v_cvt_pk_bf16_f32 v68, v8, v9\n"
    "v_mfma_f32_16x16x32_bf16 v[216:219], a[236:239], a[128:131], v[216:219]\n"
    "v_cvt_pk_bf16_f32 v69, v10, v11\n"
    "v_mul_f32 v88, v24, v24\n"
    "v_mul_f32 v89, v25, v25\n"
    "v_mfma_f32_16x16x32_bf16 v[220:223], a[236:239], a[184:187], v[220:223]\n"
    "s_waitcnt lgkmcnt(4)\n"
    "v_mul_f32 v90, v26, v26\n"
    "v_mul_f32 v91, v27, v27\n"
    "v_mfma_f32_16x16x32_bf16 v[160:163], a[240:243], a[16:19], v[160:163]\n"
    "ds_read_b64_tr_b16 a[252:253], v63 offset:33792\n"
    "ds_read_b64_tr_b16 a[254:255], v63 offset:50176\n"
    "v_fma_f32 v88, v88, v102, v82\n"
    "v_mfma_f32_16x16x32_bf16 v[164:167], a[240:243], a[72:75], v[164:167]\n"
    "buffer_load_dwordx4 v67, s[44:47], s76 offen offset:3072 lds\n"
    "v_mfma_f32_16x16x32_bf16 v[224:227], a[240:243], a[128:131], v[224:227]\n"
    "s_mov_b32 s75, 1835008\n"
    "v_fma_f32 v89, v89, v102, v82\n"
    "v_fma_f32 v90, v90, v102, v82\n"
    "v_fma_f32 v91, v91, v102, v82\n"
    "v_mfma_f32_16x16x32_bf16 v[228:231], a[240:243], a[184:187], v[228:231]\n"
    "s_waitcnt vmcnt(8) lgkmcnt(4)\n"
    "s_barrier\n"
    "v_mfma_f32_16x16x32_bf16 v[168:171], a[244:247], a[16:19], v[168:171]\n"
    "s_add_u32 m0, s90, 65536\n"
    "ds_read_b64_tr_b16 a[224:225], v56 offset:0\n"
    "ds_read_b64_tr_b16 a[226:227], v56 offset:16384\n"
    "v_mfma_f32_16x16x32_bf16 v[172:175], a[244:247], a[72:75], v[172:175]\n"
    "buffer_load_dwordx4 v64, s[44:47], s75 offen offset:0 lds\n"
    "v_mul_f32 v88, v88, v24\n"
    "v_mfma_f32_16x16x32_bf16 v[232:235], a[244:247], a[128:131], v[232:235]\n"
    "v_mul_f32 v89, v89, v25\n"
    "v_mul_f32 v90, v90, v26\n"
    "v_mul_f32 v91, v91, v27\n"
    "v_mfma_f32_16x16x32_bf16 v[236:239], a[244:247], a[184:187], v[236:239]\n"
    "s_waitcnt lgkmcnt(4)\n"
    "v_exp_f32 v88, v88\n"
    "v_mfma_f32_16x16x32_bf16 v[176:179], a[248:251], a[16:19], v[176:179]\n"
    "s_add_u32 s76, s75, 512\n"
    "ds_read_b64_tr_b16 a[228:229], v57 offset:0\n"
    "ds_read_b64_tr_b16 a[230:231], v57 offset:16384\n"
    "v_mfma_f32_16x16x32_bf16 v[180:183], a[248:251], a[72:75], v[180:183]\n"
    "buffer_load_dwordx4 v65, s[44:47], s75 offen offset:1024 lds\n"
    "v_exp_f32 v89, v89\n"
    "v_mfma_f32_16x16x32_bf16 v[240:243], a[248:251], a[128:131], v[240:243]\n"
    "v_exp_f32 v90, v90\n"
    "v_mfma_f32_16x16x32_bf16 v[244:247], a[248:251], a[184:187], v[244:247]\n"
    "s_waitcnt lgkmcnt(4)\n"
    "v_exp_f32 v91, v91\n"
    "v_mfma_f32_16x16x32_bf16 v[184:187], a[252:255], a[16:19], v[184:187]\n"
    "ds_read_b64_tr_b16 a[232:233], v58 offset:0\n"
    "ds_read_b64_tr_b16 a[234:235], v58 offset:16384\n"
    "v_fma_f32 v88, v88, v78, v78\n"
    "v_mfma_f32_16x16x32_bf16 v[188:191], a[252:255], a[72:75], v[188:191]\n"
    "buffer_load_dwordx4 v66, s[44:47], s75 offen offset:2048 lds\n"
    "v_fma_f32 v89, v89, v78, v78\n"
    "v_mfma_f32_16x16x32_bf16 v[248:251], a[252:255], a[128:131], v[248:251]\n"
    "v_fma_f32 v90, v90, v78, v78\n"
    "v_fma_f32 v91, v91, v78, v78\n"
    "v_mfma_f32_16x16x32_bf16 v[252:255], a[252:255], a[184:187], v[252:255]\n"
    "s_waitcnt lgkmcnt(4)\n"
    "v_rcp_f32 v88, v88\n"
    "v_mfma_f32_16x16x32_bf16 v[128:131], a[224:227], a[20:23], v[128:131]\n"
    "ds_read_b64_tr_b16 a[236:237], v59 offset:0\n"
    "ds_read_b64_tr_b16 a[238:239], v59 offset:16384\n"
    "v_rcp_f32 v89, v89\n"
    "v_mfma_f32_16x16x32_bf16 v[132:135], a[224:227], a[76:79], v[132:135]\n"
    "buffer_load_dwordx4 v67, s[44:47], s75 offen offset:3072 lds\n"
    "v_mfma_f32_16x16x32_bf16 v[192:195], a[224:227], a[132:135], v[192:195]\n"
    "v_rcp_f32 v90, v90\n"
    "v_mfma_f32_16x16x32_bf16 v[196:199], a[224:227], a[188:191], v[196:199]\n"
    "s_waitcnt lgkmcnt(4)\n"
    "v_rcp_f32 v91, v91\n"
    "v_mul_f32 v24, v24, v88\n"
    "v_mfma_f32_16x16x32_bf16 v[136:139], a[228:231], a[20:23], v[136:139]\n"
    "s_add_u32 m0, s90, 69632\n"
    "ds_read_b64_tr_b16 a[240:241], v56 offset:1024\n"
    "ds_read_b64_tr_b16 a[242:243], v56 offset:17408\n"
    "v_mul_f32 v25, v25, v89\n"
    "v_mfma_f32_16x16x32_bf16 v[140:143], a[228:231], a[76:79], v[140:143]\n"
    "buffer_load_dwordx4 v64, s[44:47], s76 offen offset:0 lds\n"
    "v_mfma_f32_16x16x32_bf16 v[200:203], a[228:231], a[132:135], v[200:203]\n"
    "v_mul_f32 v26, v26, v90\n"
    "v_mul_f32 v27, v27, v91\n"
    "v_cvt_pk_bf16_f32 v70, v24, v25\n"
    "v_mfma_f32_16x16x32_bf16 v[204:207], a[228:231], a[188:191], v[204:207]\n"
    "s_waitcnt lgkmcnt(4)\n"
    "v_cvt_pk_bf16_f32 v71, v26, v27\n"
    "v_mfma_f32_16x16x32_bf16 v[144:147], a[232:235], a[20:23], v[144:147]\n"
    "ds_read_b64_tr_b16 a[244:245], v57 offset:1024\n"
    "ds_read_b64_tr_b16 a[246:247], v57 offset:17408\n"
    "v_mfma_f32_16x16x32_bf16 v[148:151], a[232:235], a[76:79], v[148:151]\n"
    "buffer_load_dwordx4 v65, s[44:47], s76 offen offset:1024 lds\n"
    "v_mfma_f32_16x16x32_bf16 v[208:211], a[232:235], a[132:135], v[208:211]\n"
    "v_mfma_f32_16x16x32_bf16 v[212:215], a[232:235], a[188:191], v[212:215]\n"
    "s_waitcnt lgkmcnt(4)\n"
    "v_mul_f32 v92, v12, v12\n"
    "v_mul_f32 v93, v13, v13\n"
    "v_mul_f32 v94, v14, v14\n"
    "v_mfma_f32_16x16x32_bf16 v[152:155], a[236:239], a[20:23], v[152:155]\n"
    "ds_read_b64_tr_b16 a[248:249], v58 offset:1024\n"
    "ds_read_b64_tr_b16 a[250:251], v58 offset:17408\n"
    "v_mfma_f32_16x16x32_bf16 v[156:159], a[236:239], a[76:79], v[156:159]\n"
    "buffer_load_dwordx4 v66, s[44:47], s76 offen offset:2048 lds\n"
    "v_mul_f32 v95, v15, v15\n"
    "v_mfma_f32_16x16x32_bf16 v[216:219], a[236:239], a[132:135], v[216:219]\n"
    "ds_write_b128 v106, v[68:71]\n"
    "v_fma_f32 v92, v92, v103, v83\n"
    "v_fma_f32 v93, v93, v103, v83\n"
    "v_mfma_f32_16x16x32_bf16 v[220:223], a[236:239], a[188:191], v[220:223]\n"
    "s_waitcnt lgkmcnt(5)\n"
    "v_fma_f32 v94, v94, v103, v83\n"
    "v_fma_f32 v95, v95, v103, v83\n"
    "v_mul_f32 v92, v92, v12\n"
    "v_mfma_f32_16x16x32_bf16 v[160:163], a[240:243], a[20:23], v[160:163]\n"
    "ds_read_b64_tr_b16 a[252:253], v59 offset:1024\n"
    "ds_read_b64_tr_b16 a[254:255], v59 offset:17408\n"
    "v_mfma_f32_16x16x32_bf16 v[164:167], a[240:243], a[76:79], v[164:167]\n"
    "buffer_load_dwordx4 v67, s[44:47], s76 offen offset:3072 lds\n"
    "v_mul_f32 v93, v93, v13\n"
    "v_mfma_f32_16x16x32_bf16 v[224:227], a[240:243], a[132:135], v[224:227]\n"
    "s_mov_b32 s75, 2097152\n"
    "v_mul_f32 v94, v94, v14\n"
    "v_mul_f32 v95, v95, v15\n"
    "v_mfma_f32_16x16x32_bf16 v[228:231], a[240:243], a[188:191], v[228:231]\n"
    "s_waitcnt vmcnt(8) lgkmcnt(5)\n"
    "s_barrier\n"
    "v_mfma_f32_16x16x32_bf16 v[168:171], a[244:247], a[20:23], v[168:171]\n"
    "s_add_u32 m0, s90, 98304\n"
    "ds_read_b64_tr_b16 a[224:225], v56 offset:32768\n"
    "ds_read_b64_tr_b16 a[226:227], v56 offset:49152\n"
    "v_exp_f32 v92, v92\n"
    "v_mfma_f32_16x16x32_bf16 v[172:175], a[244:247], a[76:79], v[172:175]\n"
    "buffer_load_dwordx4 v64, s[44:47], s75 offen offset:0 lds\n"
    "v_mfma_f32_16x16x32_bf16 v[232:235], a[244:247], a[132:135], v[232:235]\n"
    "v_exp_f32 v93, v93\n"
    "v_mfma_f32_16x16x32_bf16 v[236:239], a[244:247], a[188:191], v[236:239]\n"
    "s_waitcnt lgkmcnt(5)\n"
    "v_exp_f32 v94, v94\n"
    "v_mfma_f32_16x16x32_bf16 v[176:179], a[248:251], a[20:23], v[176:179]\n"
    "s_add_u32 s76, s75, 512\n"
    "ds_read_b64_tr_b16 a[228:229], v57 offset:32768\n"
    "ds_read_b64_tr_b16 a[230:231], v57 offset:49152\n"
    "v_exp_f32 v95, v95\n"
    "v_mfma_f32_16x16x32_bf16 v[180:183], a[248:251], a[76:79], v[180:183]\n"
    "buffer_load_dwordx4 v65, s[44:47], s75 offen offset:1024 lds\n"
    "v_mfma_f32_16x16x32_bf16 v[240:243], a[248:251], a[132:135], v[240:243]\n"
    "v_fma_f32 v92, v92, v79, v79\n"
    "v_fma_f32 v93, v93, v79, v79\n"
    "v_fma_f32 v94, v94, v79, v79\n"
    "v_mfma_f32_16x16x32_bf16 v[244:247], a[248:251], a[188:191], v[244:247]\n"
    "s_waitcnt lgkmcnt(4)\n"
    "v_fma_f32 v95, v95, v79, v79\n"
    "v_rcp_f32 v92, v92\n"
    "v_mfma_f32_16x16x32_bf16 v[184:187], a[252:255], a[20:23], v[184:187]\n"
    "ds_read_b64_tr_b16 a[232:233], v58 offset:32768\n"
    "ds_read_b64_tr_b16 a[234:235], v58 offset:49152\n"
    "v_mfma_f32_16x16x32_bf16 v[188:191], a[252:255], a[76:79], v[188:191]\n"
    "buffer_load_dwordx4 v66, s[44:47], s75 offen offset:2048 lds\n"
    "v_mfma_f32_16x16x32_bf16 v[248:251], a[252:255], a[132:135], v[248:251]\n"
    "v_rcp_f32 v93, v93\n"
    "v_mfma_f32_16x16x32_bf16 v[252:255], a[252:255], a[188:191], v[252:255]\n"
    "s_waitcnt lgkmcnt(4)\n"
    "v_rcp_f32 v94, v94\n"
    "v_rcp_f32 v95, v95\n"
    "v_mfma_f32_16x16x32_bf16 v[128:131], a[224:227], a[24:27], v[128:131]\n"
    "ds_read_b64_tr_b16 a[236:237], v59 offset:32768\n"
    "ds_read_b64_tr_b16 a[238:239], v59 offset:49152\n"
    "v_mfma_f32_16x16x32_bf16 v[132:135], a[224:227], a[80:83], v[132:135]\n"
    "buffer_load_dwordx4 v67, s[44:47], s75 offen offset:3072 lds\n"
    "v_mul_f32 v12, v12, v92\n"
    "v_mfma_f32_16x16x32_bf16 v[192:195], a[224:227], a[136:139], v[192:195]\n"
    "v_mul_f32 v13, v13, v93\n"
    "v_mul_f32 v14, v14, v94\n"
    "v_mfma_f32_16x16x32_bf16 v[196:199], a[224:227], a[192:195], v[196:199]\n"
    "s_waitcnt lgkmcnt(4)\n"
    "v_mul_f32 v15, v15, v95\n"
    "v_cvt_pk_bf16_f32 v72, v12, v13\n"
    "v_cvt_pk_bf16_f32 v73, v14, v15\n"
    "v_mfma_f32_16x16x32_bf16 v[136:139], a[228:231], a[24:27], v[136:139]\n"
    "s_add_u32 m0, s90, 102400\n"
    "ds_read_b64_tr_b16 a[240:241], v56 offset:33792\n"
    "ds_read_b64_tr_b16 a[242:243], v56 offset:50176\n"
    "v_mfma_f32_16x16x32_bf16 v[140:143], a[228:231], a[80:83], v[140:143]\n"
    "buffer_load_dwordx4 v64, s[44:47], s76 offen offset:0 lds\n"
    "v_mul_f32 v96, v28, v28\n"
    "v_mfma_f32_16x16x32_bf16 v[200:203], a[228:231], a[136:139], v[200:203]\n"
    "v_mul_f32 v97, v29, v29\n"
    "v_mul_f32 v98, v30, v30\n"
    "v_mul_f32 v99, v31, v31\n"
    "v_mfma_f32_16x16x32_bf16 v[204:207], a[228:231], a[192:195], v[204:207]\n"
    "s_waitcnt lgkmcnt(4)\n"
    "v_fma_f32 v96, v96, v103, v83\n"
    "v_fma_f32 v97, v97, v103, v83\n"
    "v_mfma_f32_16x16x32_bf16 v[144:147], a[232:235], a[24:27], v[144:147]\n"
    "ds_read_b64_tr_b16 a[244:245], v57 offset:33792\n"
    "ds_read_b64_tr_b16 a[246:247], v57 offset:50176\n"
    "v_fma_f32 v98, v98, v103, v83\n"
    "v_mfma_f32_16x16x32_bf16 v[148:151], a[232:235], a[80:83], v[148:151]\n"
    "buffer_load_dwordx4 v65, s[44:47], s76 offen offset:1024 lds\n"
    "v_mfma_f32_16x16x32_bf16 v[208:211], a[232:235], a[136:139], v[208:211]\n"
    "v_fma_f32 v99, v99, v103, v83\n"
    "v_mul_f32 v96, v96, v28\n"
    "v_mul_f32 v97, v97, v29\n"
    "v_mfma_f32_16x16x32_bf16 v[212:215], a[232:235], a[192:195], v[212:215]\n"
    "s_waitcnt lgkmcnt(4)\n"
    "v_mul_f32 v98, v98, v30\n"
    "v_mul_f32 v99, v99, v31\n"
    "v_mfma_f32_16x16x32_bf16 v[152:155], a[236:239], a[24:27], v[152:155]\n"
    "ds_read_b64_tr_b16 a[248:249], v58 offset:33792\n"
    "ds_read_b64_tr_b16 a[250:251], v58 offset:50176\n"
    "v_exp_f32 v96, v96\n"
    "v_mfma_f32_16x16x32_bf16 v[156:159], a[236:239], a[80:83], v[156:159]\n"
    "buffer_load_dwordx4 v66, s[44:47], s76 offen offset:2048 lds\n"
    "v_mfma_f32_16x16x32_bf16 v[216:219], a[236:239], a[136:139], v[216:219]\n"
    "v_exp_f32 v97, v97\n"
    "v_mfma_f32_16x16x32_bf16 v[220:223], a[236:239], a[192:195], v[220:223]\n"
    "s_waitcnt lgkmcnt(4)\n"
    "v_exp_f32 v98, v98\n"
    "v_mfma_f32_16x16x32_bf16 v[160:163], a[240:243], a[24:27], v[160:163]\n"
    "ds_read_b64_tr_b16 a[252:253], v59 offset:33792\n"
    "ds_read_b64_tr_b16 a[254:255], v59 offset:50176\n"
    "v_exp_f32 v99, v99\n"
    "v_mfma_f32_16x16x32_bf16 v[164:167], a[240:243], a[80:83], v[164:167]\n"
    "buffer_load_dwordx4 v67, s[44:47], s76 offen offset:3072 lds\n"
    "v_mfma_f32_16x16x32_bf16 v[224:227], a[240:243], a[136:139], v[224:227]\n"
    "s_mov_b32 s75, 2359296\n"
    "v_fma_f32 v96, v96, v79, v79\n"
    "v_fma_f32 v97, v97, v79, v79\n"
    "v_mfma_f32_16x16x32_bf16 v[228:231], a[240:243], a[192:195], v[228:231]\n"
    "s_waitcnt vmcnt(8) lgkmcnt(4)\n"
    "s_barrier\n"
    "v_mfma_f32_16x16x32_bf16 v[168:171], a[244:247], a[24:27], v[168:171]\n"
    "s_add_u32 m0, s90, 0\n"
    "ds_read_b64_tr_b16 a[224:225], v60 offset:0\n"
    "ds_read_b64_tr_b16 a[226:227], v60 offset:16384\n"
    "v_fma_f32 v98, v98, v79, v79\n"
    "v_mfma_f32_16x16x32_bf16 v[172:175], a[244:247], a[80:83], v[172:175]\n"
    "buffer_load_dwordx4 v64, s[44:47], s75 offen offset:0 lds\n"
    "v_fma_f32 v99, v99, v79, v79\n"
    "v_mfma_f32_16x16x32_bf16 v[232:235], a[244:247], a[136:139], v[232:235]\n"
    "v_rcp_f32 v96, v96\n"
    "v_mfma_f32_16x16x32_bf16 v[236:239], a[244:247], a[192:195], v[236:239]\n"
    "s_waitcnt lgkmcnt(4)\n"
    "v_rcp_f32 v97, v97\n"
    "v_mfma_f32_16x16x32_bf16 v[176:179], a[248:251], a[24:27], v[176:179]\n"
    "s_add_u32 s76, s75, 512\n"
    "ds_read_b64_tr_b16 a[228:229], v61 offset:0\n"
    "ds_read_b64_tr_b16 a[230:231], v61 offset:16384\n"
    "v_rcp_f32 v98, v98\n"
    "v_mfma_f32_16x16x32_bf16 v[180:183], a[248:251], a[80:83], v[180:183]\n"
    "buffer_load_dwordx4 v65, s[44:47], s75 offen offset:1024 lds\n"
    "v_mfma_f32_16x16x32_bf16 v[240:243], a[248:251], a[136:139], v[240:243]\n"
    "v_rcp_f32 v99, v99\n"
    "v_mfma_f32_16x16x32_bf16 v[244:247], a[248:251], a[192:195], v[244:247]\n"
    "s_waitcnt lgkmcnt(4)\n"
    "v_mul_f32 v28, v28, v96\n"
    "v_mul_f32 v29, v29, v97\n"
    "v_mul_f32 v30, v30, v98\n"
    "v_mfma_f32_16x16x32_bf16 v[184:187], a[252:255], a[24:27], v[184:187]\n"
    "ds_read_b64_tr_b16 a[232:233], v62 offset:0\n"
    "ds_read_b64_tr_b16 a[234:235], v62 offset:16384\n"
    "v_mul_f32 v31, v31, v99\n"
    "v_mfma_f32_16x16x32_bf16 v[188:191], a[252:255], a[80:83], v[188:191]\n"
    "buffer_load_dwordx4 v66, s[44:47], s75 offen offset:2048 lds\n"
    "v_mfma_f32_16x16x32_bf16 v[248:251], a[252:255], a[136:139], v[248:251]\n"
    "v_cvt_pk_bf16_f32 v74, v28, v29\n"
    "v_cvt_pk_bf16_f32 v75, v30, v31\n"
    "v_mfma_f32_16x16x32_bf16 v[252:255], a[252:255], a[192:195], v[252:255]\n"
    "s_waitcnt lgkmcnt(4)\n"
    "v_mfma_f32_16x16x32_bf16 v[128:131], a[224:227], a[28:31], v[128:131]\n"
    "ds_read_b64_tr_b16 a[236:237], v63 offset:0\n"
    "ds_read_b64_tr_b16 a[238:239], v63 offset:16384\n"
    "v_mfma_f32_16x16x32_bf16 v[132:135], a[224:227], a[84:87], v[132:135]\n"
    "buffer_load_dwordx4 v67, s[44:47], s75 offen offset:3072 lds\n"
    "v_mfma_f32_16x16x32_bf16 v[192:195], a[224:227], a[140:143], v[192:195]\n"
    "ds_write_b128 v107, v[72:75]\n"
    "v_mul_f32 v84, v32, v32\n"
    "v_mul_f32 v85, v33, v33\n"
    "v_mfma_f32_16x16x32_bf16 v[196:199], a[224:227], a[196:199], v[196:199]\n"
    "s_waitcnt lgkmcnt(5)\n"
    "v_mul_f32 v86, v34, v34\n"
    "v_mul_f32 v87, v35, v35\n"
    "v_fma_f32 v84, v84, v100, v80\n"
    "v_mfma_f32_16x16x32_bf16 v[136:139], a[228:231], a[28:31], v[136:139]\n"
    "s_add_u32 m0, s90, 4096\n"
    "ds_read_b64_tr_b16 a[240:241], v60 offset:1024\n"
    "ds_read_b64_tr_b16 a[242:243], v60 offset:17408\n"
    "v_mfma_f32_16x16x32_bf16 v[140:143], a[228:231], a[84:87], v[140:143]\n"
    "buffer_load_dwordx4 v64, s[44:47], s76 offen offset:0 lds\n"
    "v_fma_f32 v85, v85, v100, v80\n"
    "v_mfma_f32_16x16x32_bf16 v[200:203], a[228:231], a[140:143], v[200:203]\n"
    "v_fma_f32 v86, v86, v100, v80\n"
    "v_fma_f32 v87, v87, v100, v80\n"
    "v_mfma_f32_16x16x32_bf16 v[204:207], a[228:231], a[196:199], v[204:207]\n"
    "s_waitcnt lgkmcnt(5)\n"
    "v_mul_f32 v84, v84, v32\n"
    "v_mul_f32 v85, v85, v33\n"
    "v_mul_f32 v86, v86, v34\n"
    "v_mfma_f32_16x16x32_bf16 v[144:147], a[232:235], a[28:31], v[144:147]\n"
    "ds_read_b64_tr_b16 a[244:245], v61 offset:1024\n"
    "ds_read_b64_tr_b16 a[246:247], v61 offset:17408\n"
    "v_mul_f32 v87, v87, v35\n"
    "v_mfma_f32_16x16x32_bf16 v[148:151], a[232:235], a[84:87], v[148:151]\n"
    "buffer_load_dwordx4 v65, s[44:47], s76 offen offset:1024 lds\n"
    "v_mfma_f32_16x16x32_bf16 v[208:211], a[232:235], a[140:143], v[208:211]\n"
    "s_sub_u32 s86, s73, s69\n"
    "s_add_u32 s86, s86, 4\n"
    "s_lshl_b32 s86, s86, 12\n"
    "s_lshl_b32 s87, s72, 3\n"
    "s_add_u32 s89, s86, s87\n"
    "v_add_u32 v104, s89, v104\n"
    "v_mfma_f32_16x16x32_bf16 v[212:215], a[232:235], a[196:199], v[212:215]\n"
    "s_waitcnt lgkmcnt(5)\n"
    "v_mfma_f32_16x16x32_bf16 v[152:155], a[236:239], a[28:31], v[152:155]\n"
    "ds_read_b64_tr_b16 a[248:249], v62 offset:1024\n"
    "ds_read_b64_tr_b16 a[250:251], v62 offset:17408\n"
    "v_mfma_f32_16x16x32_bf16 v[156:159], a[236:239], a[84:87], v[156:159]\n"
    "buffer_load_dwordx4 v66, s[44:47], s76 offen offset:2048 lds\n"
    "v_exp_f32 v84, v84\n"
    "v_mfma_f32_16x16x32_bf16 v[216:219], a[236:239], a[140:143], v[216:219]\n"
    "v_exp_f32 v85, v85\n"
    "v_mfma_f32_16x16x32_bf16 v[220:223], a[236:239], a[196:199], v[220:223]\n"
    "s_waitcnt lgkmcnt(4)\n"
    "v_exp_f32 v86, v86\n"
    "v_mfma_f32_16x16x32_bf16 v[160:163], a[240:243], a[28:31], v[160:163]\n"
    "ds_read_b64_tr_b16 a[252:253], v63 offset:1024\n"
    "ds_read_b64_tr_b16 a[254:255], v63 offset:17408\n"
    "v_exp_f32 v87, v87\n"
    "v_mfma_f32_16x16x32_bf16 v[164:167], a[240:243], a[84:87], v[164:167]\n"
    "buffer_load_dwordx4 v67, s[44:47], s76 offen offset:3072 lds\n"
    "v_mfma_f32_16x16x32_bf16 v[224:227], a[240:243], a[140:143], v[224:227]\n"
    "s_mov_b32 s75, 2621440\n"
    "v_fma_f32 v84, v84, v76, v76\n"
    "v_fma_f32 v85, v85, v76, v76\n"
    "v_mfma_f32_16x16x32_bf16 v[228:231], a[240:243], a[196:199], v[228:231]\n"
    "s_waitcnt vmcnt(8) lgkmcnt(4)\n"
    "s_barrier\n"
    "v_mfma_f32_16x16x32_bf16 v[168:171], a[244:247], a[28:31], v[168:171]\n"
    "s_add_u32 m0, s90, 32768\n"
    "ds_read_b64_tr_b16 a[224:225], v60 offset:32768\n"
    "ds_read_b64_tr_b16 a[226:227], v60 offset:49152\n"
    "v_fma_f32 v86, v86, v76, v76\n"
    "v_mfma_f32_16x16x32_bf16 v[172:175], a[244:247], a[84:87], v[172:175]\n"
    "buffer_load_dwordx4 v64, s[44:47], s75 offen offset:0 lds\n"
    "v_mfma_f32_16x16x32_bf16 v[232:235], a[244:247], a[140:143], v[232:235]\n"
    "v_fma_f32 v87, v87, v76, v76\n"
    "v_rcp_f32 v84, v84\n"
    "v_mfma_f32_16x16x32_bf16 v[236:239], a[244:247], a[196:199], v[236:239]\n"
    "s_waitcnt lgkmcnt(4)\n"
    "v_rcp_f32 v85, v85\n"
    "v_mfma_f32_16x16x32_bf16 v[176:179], a[248:251], a[28:31], v[176:179]\n"
    "s_add_u32 s76, s75, 512\n"
    "ds_read_b64_tr_b16 a[228:229], v61 offset:32768\n"
    "ds_read_b64_tr_b16 a[230:231], v61 offset:49152\n"
    "v_rcp_f32 v86, v86\n"
    "v_mfma_f32_16x16x32_bf16 v[180:183], a[248:251], a[84:87], v[180:183]\n"
    "buffer_load_dwordx4 v65, s[44:47], s75 offen offset:1024 lds\n"
    "v_mfma_f32_16x16x32_bf16 v[240:243], a[248:251], a[140:143], v[240:243]\n"
    "v_rcp_f32 v87, v87\n"
    "v_mfma_f32_16x16x32_bf16 v[244:247], a[248:251], a[196:199], v[244:247]\n"
    "s_waitcnt lgkmcnt(4)\n"
    "v_mul_f32 v32, v32, v84\n"
    "v_mul_f32 v33, v33, v85\n"
    "v_mul_f32 v34, v34, v86\n"
    "v_mfma_f32_16x16x32_bf16 v[184:187], a[252:255], a[28:31], v[184:187]\n"
    "ds_read_b64_tr_b16 a[232:233], v62 offset:32768\n"
    "ds_read_b64_tr_b16 a[234:235], v62 offset:49152\n"
    "v_mfma_f32_16x16x32_bf16 v[188:191], a[252:255], a[84:87], v[188:191]\n"
    "buffer_load_dwordx4 v66, s[44:47], s75 offen offset:2048 lds\n"
    "v_mul_f32 v35, v35, v87\n"
    "v_mfma_f32_16x16x32_bf16 v[248:251], a[252:255], a[140:143], v[248:251]\n"
    "v_cvt_pk_bf16_f32 v68, v32, v33\n"
    "v_cvt_pk_bf16_f32 v69, v34, v35\n"
    "v_mfma_f32_16x16x32_bf16 v[252:255], a[252:255], a[196:199], v[252:255]\n"
    "s_waitcnt lgkmcnt(4)\n"
    "v_mfma_f32_16x16x32_bf16 v[128:131], a[224:227], a[32:35], v[128:131]\n"
    "ds_read_b64_tr_b16 a[236:237], v63 offset:32768\n"
    "ds_read_b64_tr_b16 a[238:239], v63 offset:49152\n"
    "v_mul_f32 v88, v36, v36\n"
    "v_mfma_f32_16x16x32_bf16 v[132:135], a[224:227], a[88:91], v[132:135]\n"
    "buffer_load_dwordx4 v67, s[44:47], s75 offen offset:3072 lds\n"
    "v_mfma_f32_16x16x32_bf16 v[192:195], a[224:227], a[144:147], v[192:195]\n"
    "ds_write_b64 v104, v[68:69]\n"
    "v_mul_f32 v89, v37, v37\n"
    "v_mul_f32 v90, v38, v38\n"
    "v_mul_f32 v91, v39, v39\n"
    "v_mfma_f32_16x16x32_bf16 v[196:199], a[224:227], a[200:203], v[196:199]\n"
    "s_waitcnt lgkmcnt(5)\n"
    "v_fma_f32 v88, v88, v101, v81\n"
    "v_fma_f32 v89, v89, v101, v81\n"
    "v_mfma_f32_16x16x32_bf16 v[136:139], a[228:231], a[32:35], v[136:139]\n"
    "s_add_u32 m0, s90, 36864\n"
    "ds_read_b64_tr_b16 a[240:241], v60 offset:33792\n"
    "ds_read_b64_tr_b16 a[242:243], v60 offset:50176\n"
    "v_fma_f32 v90, v90, v101, v81\n"
    "v_mfma_f32_16x16x32_bf16 v[140:143], a[228:231], a[88:91], v[140:143]\n"
    "buffer_load_dwordx4 v64, s[44:47], s76 offen offset:0 lds\n"
    "v_fma_f32 v91, v91, v101, v81\n"
    "v_mfma_f32_16x16x32_bf16 v[200:203], a[228:231], a[144:147], v[200:203]\n"
    "v_mul_f32 v88, v88, v36\n"
    "v_mul_f32 v89, v89, v37\n"
    "v_mfma_f32_16x16x32_bf16 v[204:207], a[228:231], a[200:203], v[204:207]\n"
    "s_waitcnt lgkmcnt(5)\n"
    "v_mul_f32 v90, v90, v38\n"
    "v_mul_f32 v91, v91, v39\n"
    "v_add_u32 v105, s89, v105\n"
    "v_mfma_f32_16x16x32_bf16 v[144:147], a[232:235], a[32:35], v[144:147]\n"
    "ds_read_b64_tr_b16 a[244:245], v61 offset:33792\n"
    "ds_read_b64_tr_b16 a[246:247], v61 offset:50176\n"
    "v_mfma_f32_16x16x32_bf16 v[148:151], a[232:235], a[88:91], v[148:151]\n"
    "buffer_load_dwordx4 v65, s[44:47], s76 offen offset:1024 lds\n"
    "v_mfma_f32_16x16x32_bf16 v[208:211], a[232:235], a[144:147], v[208:211]\n"
    "v_exp_f32 v88, v88\n"
    "v_exp_f32 v89, v89\n"
    "v_mfma_f32_16x16x32_bf16 v[212:215], a[232:235], a[200:203], v[212:215]\n"
    "s_waitcnt lgkmcnt(5)\n"
    "v_exp_f32 v90, v90\n"
    "v_mfma_f32_16x16x32_bf16 v[152:155], a[236:239], a[32:35], v[152:155]\n"
    "ds_read_b64_tr_b16 a[248:249], v62 offset:33792\n"
    "ds_read_b64_tr_b16 a[250:251], v62 offset:50176\n"
    "v_mfma_f32_16x16x32_bf16 v[156:159], a[236:239], a[88:91], v[156:159]\n"
    "buffer_load_dwordx4 v66, s[44:47], s76 offen offset:2048 lds\n"
    "v_exp_f32 v91, v91\n"
    "v_mfma_f32_16x16x32_bf16 v[216:219], a[236:239], a[144:147], v[216:219]\n"
    "v_fma_f32 v88, v88, v77, v77\n"
    "v_fma_f32 v89, v89, v77, v77\n"
    "v_mfma_f32_16x16x32_bf16 v[220:223], a[236:239], a[200:203], v[220:223]\n"
    "s_waitcnt lgkmcnt(4)\n"
    "v_fma_f32 v90, v90, v77, v77\n"
    "v_fma_f32 v91, v91, v77, v77\n"
    "v_mfma_f32_16x16x32_bf16 v[160:163], a[240:243], a[32:35], v[160:163]\n"
    "ds_read_b64_tr_b16 a[252:253], v63 offset:33792\n"
    "ds_read_b64_tr_b16 a[254:255], v63 offset:50176\n"
    "v_mfma_f32_16x16x32_bf16 v[164:167], a[240:243], a[88:91], v[164:167]\n"
    "buffer_load_dwordx4 v67, s[44:47], s76 offen offset:3072 lds\n"
    "v_rcp_f32 v88, v88\n"
    "v_mfma_f32_16x16x32_bf16 v[224:227], a[240:243], a[144:147], v[224:227]\n"
    "s_mov_b32 s75, 2883584\n"
    "v_rcp_f32 v89, v89\n"
    "v_mfma_f32_16x16x32_bf16 v[228:231], a[240:243], a[200:203], v[228:231]\n"
    "s_waitcnt vmcnt(8) lgkmcnt(4)\n"
    "s_barrier\n"
    "v_mfma_f32_16x16x32_bf16 v[168:171], a[244:247], a[32:35], v[168:171]\n"
    "s_add_u32 m0, s90, 65536\n"
    "ds_read_b64_tr_b16 a[224:225], v56 offset:0\n"
    "ds_read_b64_tr_b16 a[226:227], v56 offset:16384\n"
    "v_mfma_f32_16x16x32_bf16 v[172:175], a[244:247], a[88:91], v[172:175]\n"
    "buffer_load_dwordx4 v64, s[44:47], s75 offen offset:0 lds\n"
    "v_rcp_f32 v90, v90\n"
    "v_mfma_f32_16x16x32_bf16 v[232:235], a[244:247], a[144:147], v[232:235]\n"
    "v_rcp_f32 v91, v91\n"
    "v_mfma_f32_16x16x32_bf16 v[236:239], a[244:247], a[200:203], v[236:239]\n"
    "s_waitcnt lgkmcnt(4)\n"
    "v_mul_f32 v36, v36, v88\n"
    "v_mul_f32 v37, v37, v89\n"
    "v_mfma_f32_16x16x32_bf16 v[176:179], a[248:251], a[32:35], v[176:179]\n"
    "s_add_u32 s76, s75, 512\n"
    "ds_read_b64_tr_b16 a[228:229], v57 offset:0\n"
    "ds_read_b64_tr_b16 a[230:231], v57 offset:16384\n"
    "v_mul_f32 v38, v38, v90\n"
    "v_mfma_f32_16x16x32_bf16 v[180:183], a[248:251], a[88:91], v[180:183]\n"
    "buffer_load_dwordx4 v65, s[44:47], s75 offen offset:1024 lds\n"
    "v_mul_f32 v39, v39, v91\n"
    "v_mfma_f32_16x16x32_bf16 v[240:243], a[248:251], a[144:147], v[240:243]\n"
    "v_cvt_pk_bf16_f32 v70, v36, v37\n"
    "v_cvt_pk_bf16_f32 v71, v38, v39\n"
    "v_mfma_f32_16x16x32_bf16 v[244:247], a[248:251], a[200:203], v[244:247]\n"
    "s_waitcnt lgkmcnt(4)\n"
    "v_mfma_f32_16x16x32_bf16 v[184:187], a[252:255], a[32:35], v[184:187]\n"
    "ds_read_b64_tr_b16 a[232:233], v58 offset:0\n"
    "ds_read_b64_tr_b16 a[234:235], v58 offset:16384\n"
    "v_mfma_f32_16x16x32_bf16 v[188:191], a[252:255], a[88:91], v[188:191]\n"
    "buffer_load_dwordx4 v66, s[44:47], s75 offen offset:2048 lds\n"
    "v_mul_f32 v92, v40, v40\n"
    "v_mfma_f32_16x16x32_bf16 v[248:251], a[252:255], a[144:147], v[248:251]\n"
    "ds_write_b64 v105, v[70:71]\n"
    "v_mul_f32 v93, v41, v41\n"
    "v_mul_f32 v94, v42, v42\n"
    "v_mfma_f32_16x16x32_bf16 v[252:255], a[252:255], a[200:203], v[252:255]\n"
    "s_waitcnt lgkmcnt(5)\n"
    "v_mul_f32 v95, v43, v43\n"
    "v_fma_f32 v92, v92, v102, v82\n"
    "v_fma_f32 v93, v93, v102, v82\n"
    "v_mfma_f32_16x16x32_bf16 v[128:131], a[224:227], a[36:39], v[128:131]\n"
    "ds_read_b64_tr_b16 a[236:237], v59 offset:0\n"
    "ds_read_b64_tr_b16 a[238:239], v59 offset:16384\n"
    "v_fma_f32 v94, v94, v102, v82\n"
    "v_mfma_f32_16x16x32_bf16 v[132:135], a[224:227], a[92:95], v[132:135]\n"
    "buffer_load_dwordx4 v67, s[44:47], s75 offen offset:3072 lds\n"
    "v_mfma_f32_16x16x32_bf16 v[192:195], a[224:227], a[148:151], v[192:195]\n"
    "v_fma_f32 v95, v95, v102, v82\n"
    "v_mul_f32 v92, v92, v40\n"
    "v_mul_f32 v93, v93, v41\n"
    "v_mfma_f32_16x16x32_bf16 v[196:199], a[224:227], a[204:207], v[196:199]\n"
    "s_waitcnt lgkmcnt(5)\n"
    "v_mul_f32 v94, v94, v42\n"
    "v_mul_f32 v95, v95, v43\n"
    "v_mfma_f32_16x16x32_bf16 v[136:139], a[228:231], a[36:39], v[136:139]\n"
    "s_add_u32 m0, s90, 69632\n"
    "ds_read_b64_tr_b16 a[240:241], v56 offset:1024\n"
    "ds_read_b64_tr_b16 a[242:243], v56 offset:17408\n"
    "v_add_u32 v106, s89, v106\n"
    "v_mfma_f32_16x16x32_bf16 v[140:143], a[228:231], a[92:95], v[140:143]\n"
    "buffer_load_dwordx4 v64, s[44:47], s76 offen offset:0 lds\n"
    "v_mfma_f32_16x16x32_bf16 v[200:203], a[228:231], a[148:151], v[200:203]\n"
    "v_exp_f32 v92, v92\n"
    "v_exp_f32 v93, v93\n"
    "v_mfma_f32_16x16x32_bf16 v[204:207], a[228:231], a[204:207], v[204:207]\n"
    "s_waitcnt lgkmcnt(5)\n"
    "v_exp_f32 v94, v94\n"
    "v_mfma_f32_16x16x32_bf16 v[144:147], a[232:235], a[36:39], v[144:147]\n"
    "ds_read_b64_tr_b16 a[244:245], v57 offset:1024\n"
    "ds_read_b64_tr_b16 a[246:247], v57 offset:17408\n"
    "v_mfma_f32_16x16x32_bf16 v[148:151], a[232:235], a[92:95], v[148:151]\n"
    "buffer_load_dwordx4 v65, s[44:47], s76 offen offset:1024 lds\n"
    "v_mfma_f32_16x16x32_bf16 v[208:211], a[232:235], a[148:151], v[208:211]\n"
    "v_exp_f32 v95, v95\n"
    "v_fma_f32 v92, v92, v78, v78\n"
    "v_mfma_f32_16x16x32_bf16 v[212:215], a[232:235], a[204:207], v[212:215]\n"
    "s_waitcnt lgkmcnt(4)\n"
    "v_fma_f32 v93, v93, v78, v78\n"
    "v_fma_f32 v94, v94, v78, v78\n"
    "v_fma_f32 v95, v95, v78, v78\n"
    "v_mfma_f32_16x16x32_bf16 v[152:155], a[236:239], a[36:39], v[152:155]\n"
    "ds_read_b64_tr_b16 a[248:249], v58 offset:1024\n"
    "ds_read_b64_tr_b16 a[250:251], v58 offset:17408\n"
    "v_mfma_f32_16x16x32_bf16 v[156:159], a[236:239], a[92:95], v[156:159]\n"
    "buffer_load_dwordx4 v66, s[44:47], s76 offen offset:2048 lds\n"
    "v_rcp_f32 v92, v92\n"
    "v_mfma_f32_16x16x32_bf16 v[216:219], a[236:239], a[148:151], v[216:219]\n"
    "v_rcp_f32 v93, v93\n"
    "v_mfma_f32_16x16x32_bf16 v[220:223], a[236:239], a[204:207], v[220:223]\n"
    "s_waitcnt lgkmcnt(4)\n"
    "v_rcp_f32 v94, v94\n"
    "v_mfma_f32_16x16x32_bf16 v[160:163], a[240:243], a[36:39], v[160:163]\n"
    "ds_read_b64_tr_b16 a[252:253], v59 offset:1024\n"
    "ds_read_b64_tr_b16 a[254:255], v59 offset:17408\n"
    "v_mfma_f32_16x16x32_bf16 v[164:167], a[240:243], a[92:95], v[164:167]\n"
    "buffer_load_dwordx4 v67, s[44:47], s76 offen offset:3072 lds\n"
    "v_rcp_f32 v95, v95\n"
    "v_mfma_f32_16x16x32_bf16 v[224:227], a[240:243], a[148:151], v[224:227]\n"
    "s_mov_b32 s75, 3145728\n"
    "v_mul_f32 v40, v40, v92\n"
    "v_mul_f32 v41, v41, v93\n"
    "v_mfma_f32_16x16x32_bf16 v[228:231], a[240:243], a[204:207], v[228:231]\n"
    "s_waitcnt vmcnt(8) lgkmcnt(4)\n"
    "s_barrier\n"
    "v_mfma_f32_16x16x32_bf16 v[168:171], a[244:247], a[36:39], v[168:171]\n"
    "s_add_u32 m0, s90, 98304\n"
    "ds_read_b64_tr_b16 a[224:225], v56 offset:32768\n"
    "ds_read_b64_tr_b16 a[226:227], v56 offset:49152\n"
    "v_mul_f32 v42, v42, v94\n"
    "v_mfma_f32_16x16x32_bf16 v[172:175], a[244:247], a[92:95], v[172:175]\n"
    "buffer_load_dwordx4 v64, s[44:47], s75 offen offset:0 lds\n"
    "v_mfma_f32_16x16x32_bf16 v[232:235], a[244:247], a[148:151], v[232:235]\n"
    "v_mul_f32 v43, v43, v95\n"
    "v_cvt_pk_bf16_f32 v72, v40, v41\n"
    "v_cvt_pk_bf16_f32 v73, v42, v43\n"
    "v_mfma_f32_16x16x32_bf16 v[236:239], a[244:247], a[204:207], v[236:239]\n"
    "s_waitcnt lgkmcnt(4)\n"
    "v_mfma_f32_16x16x32_bf16 v[176:179], a[248:251], a[36:39], v[176:179]\n"
    "s_add_u32 s76, s75, 512\n"
    "ds_read_b64_tr_b16 a[228:229], v57 offset:32768\n"
    "ds_read_b64_tr_b16 a[230:231], v57 offset:49152\n"
    "v_mfma_f32_16x16x32_bf16 v[180:183], a[248:251], a[92:95], v[180:183]\n"
    "buffer_load_dwordx4 v65, s[44:47], s75 offen offset:1024 lds\n"
    "v_mfma_f32_16x16x32_bf16 v[240:243], a[248:251], a[148:151], v[240:243]\n"
    "ds_write_b64 v106, v[72:73]\n"
    "v_mul_f32 v96, v44, v44\n"
    "v_mul_f32 v97, v45, v45\n"
    "v_mul_f32 v98, v46, v46\n"
    "v_mfma_f32_16x16x32_bf16 v[244:247], a[248:251], a[204:207], v[244:247]\n"
    "s_waitcnt lgkmcnt(5)\n"
    "v_mul_f32 v99, v47, v47\n"
    "v_fma_f32 v96, v96, v103, v83\n"
    "v_fma_f32 v97, v97, v103, v83\n"
    "v_mfma_f32_16x16x32_bf16 v[184:187], a[252:255], a[36:39], v[184:187]\n"
    "ds_read_b64_tr_b16 a[232:233], v58 offset:32768\n"
    "ds_read_b64_tr_b16 a[234:235], v58 offset:49152\n"
    "v_mfma_f32_16x16x32_bf16 v[188:191], a[252:255], a[92:95], v[188:191]\n"
    "buffer_load_dwordx4 v66, s[44:47], s75 offen offset:2048 lds\n"
    "v_fma_f32 v98, v98, v103, v83\n"
    "v_mfma_f32_16x16x32_bf16 v[248:251], a[252:255], a[148:151], v[248:251]\n"
    "v_fma_f32 v99, v99, v103, v83\n"
    "v_mul_f32 v96, v96, v44\n"
    "v_mfma_f32_16x16x32_bf16 v[252:255], a[252:255], a[204:207], v[252:255]\n"
    "s_waitcnt lgkmcnt(5)\n"
    "v_mul_f32 v97, v97, v45\n"
    "v_mul_f32 v98, v98, v46\n"
    "v_mul_f32 v99, v99, v47\n"
    "v_mfma_f32_16x16x32_bf16 v[128:131], a[224:227], a[40:43], v[128:131]\n"
    "ds_read_b64_tr_b16 a[236:237], v59 offset:32768\n"
    "ds_read_b64_tr_b16 a[238:239], v59 offset:49152\n"
    "v_mfma_f32_16x16x32_bf16 v[132:135], a[224:227], a[96:99], v[132:135]\n"
    "buffer_load_dwordx4 v67, s[44:47], s75 offen offset:3072 lds\n"
    "v_add_u32 v107, s89, v107\n"
    "v_mfma_f32_16x16x32_bf16 v[192:195], a[224:227], a[152:155], v[192:195]\n"
    "v_exp_f32 v96, v96\n"
    "v_mfma_f32_16x16x32_bf16 v[196:199], a[224:227], a[208:211], v[196:199]\n"
    "s_waitcnt lgkmcnt(5)\n"
    "v_exp_f32 v97, v97\n"
    "v_exp_f32 v98, v98\n"
    "v_mfma_f32_16x16x32_bf16 v[136:139], a[228:231], a[40:43], v[136:139]\n"
    "s_add_u32 m0, s90, 102400\n"
    "ds_read_b64_tr_b16 a[240:241], v56 offset:33792\n"
    "ds_read_b64_tr_b16 a[242:243], v56 offset:50176\n"
    "v_mfma_f32_16x16x32_bf16 v[140:143], a[228:231], a[96:99], v[140:143]\n"
    "buffer_load_dwordx4 v64, s[44:47], s76 offen offset:0 lds\n"
    "v_mfma_f32_16x16x32_bf16 v[200:203], a[228:231], a[152:155], v[200:203]\n"
    "v_exp_f32 v99, v99\n"
    "v_fma_f32 v96, v96, v79, v79\n"
    "v_mfma_f32_16x16x32_bf16 v[204:207], a[228:231], a[208:211], v[204:207]\n"
    "s_waitcnt lgkmcnt(4)\n"
    "v_fma_f32 v97, v97, v79, v79\n"
    "v_fma_f32 v98, v98, v79, v79\n"
    "v_mfma_f32_16x16x32_bf16 v[144:147], a[232:235], a[40:43], v[144:147]\n"
    "ds_read_b64_tr_b16 a[244:245], v57 offset:33792\n"
    "ds_read_b64_tr_b16 a[246:247], v57 offset:50176\n"
    "v_fma_f32 v99, v99, v79, v79\n"
    "v_mfma_f32_16x16x32_bf16 v[148:151], a[232:235], a[96:99], v[148:151]\n"
    "buffer_load_dwordx4 v65, s[44:47], s76 offen offset:1024 lds\n"
    "v_mfma_f32_16x16x32_bf16 v[208:211], a[232:235], a[152:155], v[208:211]\n"
    "v_rcp_f32 v96, v96\n"
    "v_rcp_f32 v97, v97\n"
    "v_mfma_f32_16x16x32_bf16 v[212:215], a[232:235], a[208:211], v[212:215]\n"
    "s_waitcnt lgkmcnt(4)\n"
    "v_rcp_f32 v98, v98\n"
    "v_mfma_f32_16x16x32_bf16 v[152:155], a[236:239], a[40:43], v[152:155]\n"
    "ds_read_b64_tr_b16 a[248:249], v58 offset:33792\n"
    "ds_read_b64_tr_b16 a[250:251], v58 offset:50176\n"
    "v_mfma_f32_16x16x32_bf16 v[156:159], a[236:239], a[96:99], v[156:159]\n"
    "buffer_load_dwordx4 v66, s[44:47], s76 offen offset:2048 lds\n"
    "v_mfma_f32_16x16x32_bf16 v[216:219], a[236:239], a[152:155], v[216:219]\n"
    "v_rcp_f32 v99, v99\n"
    "v_mul_f32 v44, v44, v96\n"
    "v_mfma_f32_16x16x32_bf16 v[220:223], a[236:239], a[208:211], v[220:223]\n"
    "s_waitcnt lgkmcnt(4)\n"
    "v_mul_f32 v45, v45, v97\n"
    "v_mul_f32 v46, v46, v98\n"
    "v_mul_f32 v47, v47, v99\n"
    "v_mfma_f32_16x16x32_bf16 v[160:163], a[240:243], a[40:43], v[160:163]\n"
    "ds_read_b64_tr_b16 a[252:253], v59 offset:33792\n"
    "ds_read_b64_tr_b16 a[254:255], v59 offset:50176\n"
    "v_cvt_pk_bf16_f32 v74, v44, v45\n"
    "v_mfma_f32_16x16x32_bf16 v[164:167], a[240:243], a[96:99], v[164:167]\n"
    "buffer_load_dwordx4 v67, s[44:47], s76 offen offset:3072 lds\n"
    "v_mfma_f32_16x16x32_bf16 v[224:227], a[240:243], a[152:155], v[224:227]\n"
    "s_mov_b32 s75, 3407872\n"
    "v_cvt_pk_bf16_f32 v75, v46, v47\n"
    "v_mfma_f32_16x16x32_bf16 v[228:231], a[240:243], a[208:211], v[228:231]\n"
    "s_waitcnt vmcnt(8) lgkmcnt(4)\n"
    "s_barrier\n"
    "v_mfma_f32_16x16x32_bf16 v[168:171], a[244:247], a[40:43], v[168:171]\n"
    "s_add_u32 m0, s90, 0\n"
    "ds_read_b64_tr_b16 a[224:225], v60 offset:0\n"
    "ds_read_b64_tr_b16 a[226:227], v60 offset:16384\n"
    "v_mfma_f32_16x16x32_bf16 v[172:175], a[244:247], a[96:99], v[172:175]\n"
    "buffer_load_dwordx4 v64, s[44:47], s75 offen offset:0 lds\n"
    "v_mfma_f32_16x16x32_bf16 v[232:235], a[244:247], a[152:155], v[232:235]\n"
    "ds_write_b64 v107, v[74:75]\n"
    "v_mul_f32 v84, v48, v48\n"
    "v_mul_f32 v85, v49, v49\n"
    "v_mul_f32 v86, v50, v50\n"
    "v_mfma_f32_16x16x32_bf16 v[236:239], a[244:247], a[208:211], v[236:239]\n"
    "s_waitcnt lgkmcnt(5)\n"
    "v_mul_f32 v87, v51, v51\n"
    "v_fma_f32 v84, v84, v100, v80\n"
    "v_mfma_f32_16x16x32_bf16 v[176:179], a[248:251], a[40:43], v[176:179]\n"
    "s_add_u32 s76, s75, 512\n"
    "ds_read_b64_tr_b16 a[228:229], v61 offset:0\n"
    "ds_read_b64_tr_b16 a[230:231], v61 offset:16384\n"
    "v_fma_f32 v85, v85, v100, v80\n"
    "v_mfma_f32_16x16x32_bf16 v[180:183], a[248:251], a[96:99], v[180:183]\n"
    "buffer_load_dwordx4 v65, s[44:47], s75 offen offset:1024 lds\n"
    "v_mfma_f32_16x16x32_bf16 v[240:243], a[248:251], a[152:155], v[240:243]\n"
    "v_fma_f32 v86, v86, v100, v80\n"
    "v_fma_f32 v87, v87, v100, v80\n"
    "v_mul_f32 v84, v84, v48\n"
    "v_mfma_f32_16x16x32_bf16 v[244:247], a[248:251], a[208:211], v[244:247]\n"
    "s_waitcnt lgkmcnt(5)\n"
    "v_mul_f32 v85, v85, v49\n"
    "v_mul_f32 v86, v86, v50\n"
    "v_mfma_f32_16x16x32_bf16 v[184:187], a[252:255], a[40:43], v[184:187]\n"
    "ds_read_b64_tr_b16 a[232:233], v62 offset:0\n"
    "ds_read_b64_tr_b16 a[234:235], v62 offset:16384\n"
    "v_mul_f32 v87, v87, v51\n"
    "v_mfma_f32_16x16x32_bf16 v[188:191], a[252:255], a[96:99], v[188:191]\n"
    "buffer_load_dwordx4 v66, s[44:47], s75 offen offset:2048 lds\n"
    "v_mfma_f32_16x16x32_bf16 v[248:251], a[252:255], a[152:155], v[248:251]\n"
    "s_sub_u32 s89, 2, s73\n"
    "s_lshl_b32 s89, s89, 12\n"
    "v_add_u32 v104, s89, v104\n"
    "v_exp_f32 v84, v84\n"
    "v_mfma_f32_16x16x32_bf16 v[252:255], a[252:255], a[208:211], v[252:255]\n"
    "s_waitcnt lgkmcnt(5)\n"
    "v_exp_f32 v85, v85\n"
    "v_mfma_f32_16x16x32_bf16 v[128:131], a[224:227], a[44:47], v[128:131]\n"
    "ds_read_b64_tr_b16 a[236:237], v63 offset:0\n"
    "ds_read_b64_tr_b16 a[238:239], v63 offset:16384\n"
    "v_mfma_f32_16x16x32_bf16 v[132:135], a[224:227], a[100:103], v[132:135]\n"
    "buffer_load_dwordx4 v67, s[44:47], s75 offen offset:3072 lds\n"
    "v_mfma_f32_16x16x32_bf16 v[192:195], a[224:227], a[156:159], v[192:195]\n"
    "v_exp_f32 v86, v86\n"
    "v_exp_f32 v87, v87\n"
    "v_mfma_f32_16x16x32_bf16 v[196:199], a[224:227], a[212:215], v[196:199]\n"
    "s_waitcnt lgkmcnt(4)\n"
    "v_fma_f32 v84, v84, v76, v76\n"
    "v_fma_f32 v85, v85, v76, v76\n"
    "v_mfma_f32_16x16x32_bf16 v[136:139], a[228:231], a[44:47], v[136:139]\n"
    "s_add_u32 m0, s90, 4096\n"
    "ds_read_b64_tr_b16 a[240:241], v60 offset:1024\n"
    "ds_read_b64_tr_b16 a[242:243], v60 offset:17408\n"
    "v_fma_f32 v86, v86, v76, v76\n"
    "v_mfma_f32_16x16x32_bf16 v[140:143], a[228:231], a[100:103], v[140:143]\n"
    "buffer_load_dwordx4 v64, s[44:47], s76 offen offset:0 lds\n"
    "v_mfma_f32_16x16x32_bf16 v[200:203], a[228:231], a[156:159], v[200:203]\n"
    "v_fma_f32 v87, v87, v76, v76\n"
    "v_rcp_f32 v84, v84\n"
    "v_mfma_f32_16x16x32_bf16 v[204:207], a[228:231], a[212:215], v[204:207]\n"
    "s_waitcnt lgkmcnt(4)\n"
    "v_rcp_f32 v85, v85\n"
    "v_mfma_f32_16x16x32_bf16 v[144:147], a[232:235], a[44:47], v[144:147]\n"
    "ds_read_b64_tr_b16 a[244:245], v61 offset:1024\n"
    "ds_read_b64_tr_b16 a[246:247], v61 offset:17408\n"
    "v_mfma_f32_16x16x32_bf16 v[148:151], a[232:235], a[100:103], v[148:151]\n"
    "buffer_load_dwordx4 v65, s[44:47], s76 offen offset:1024 lds\n"
    "v_rcp_f32 v86, v86\n"
    "v_mfma_f32_16x16x32_bf16 v[208:211], a[232:235], a[156:159], v[208:211]\n"
    "v_rcp_f32 v87, v87\n"
    "v_mfma_f32_16x16x32_bf16 v[212:215], a[232:235], a[212:215], v[212:215]\n"
    "s_waitcnt lgkmcnt(4)\n"
    "v_mul_f32 v48, v48, v84\n"
    "v_mul_f32 v49, v49, v85\n"
    "v_mul_f32 v50, v50, v86\n"
    "v_mfma_f32_16x16x32_bf16 v[152:155], a[236:239], a[44:47], v[152:155]\n"
    "ds_read_b64_tr_b16 a[248:249], v62 offset:1024\n"
    "ds_read_b64_tr_b16 a[250:251], v62 offset:17408\n"
    "v_mfma_f32_16x16x32_bf16 v[156:159], a[236:239], a[100:103], v[156:159]\n"
    "buffer_load_dwordx4 v66, s[44:47], s76 offen offset:2048 lds\n"
    "v_mul_f32 v51, v51, v87\n"
    "v_mfma_f32_16x16x32_bf16 v[216:219], a[236:239], a[156:159], v[216:219]\n"
    "v_cvt_pk_bf16_f32 v68, v48, v49\n"
    "v_cvt_pk_bf16_f32 v69, v50, v51\n"
    "v_mfma_f32_16x16x32_bf16 v[220:223], a[236:239], a[212:215], v[220:223]\n"
    "s_waitcnt lgkmcnt(4)\n"
    "v_mfma_f32_16x16x32_bf16 v[160:163], a[240:243], a[44:47], v[160:163]\n"
    "ds_read_b64_tr_b16 a[252:253], v63 offset:1024\n"
    "ds_read_b64_tr_b16 a[254:255], v63 offset:17408\n"
    "v_mfma_f32_16x16x32_bf16 v[164:167], a[240:243], a[100:103], v[164:167]\n"
    "buffer_load_dwordx4 v67, s[44:47], s76 offen offset:3072 lds\n"
    "v_mul_f32 v88, v52, v52\n"
    "v_mfma_f32_16x16x32_bf16 v[224:227], a[240:243], a[156:159], v[224:227]\n"
    "s_mov_b32 s75, 0\n"
    "ds_write_b64 v104, v[68:69]\n"
    "v_mul_f32 v89, v53, v53\n"
    "v_mul_f32 v90, v54, v54\n"
    "v_mul_f32 v91, v55, v55\n"
    "v_mfma_f32_16x16x32_bf16 v[228:231], a[240:243], a[212:215], v[228:231]\n"
    "s_waitcnt vmcnt(8) lgkmcnt(5)\n"
    "s_barrier\n"
    "v_mfma_f32_16x16x32_bf16 v[168:171], a[244:247], a[44:47], v[168:171]\n"
    "s_add_u32 m0, s90, 32768\n"
    "ds_read_b64_tr_b16 a[224:225], v60 offset:32768\n"
    "ds_read_b64_tr_b16 a[226:227], v60 offset:49152\n"
    "v_mfma_f32_16x16x32_bf16 v[172:175], a[244:247], a[100:103], v[172:175]\n"
    "buffer_load_dwordx4 v120, s[48:51], s75 offen offset:0 lds\n"
    "v_fma_f32 v88, v88, v101, v81\n"
    "v_mfma_f32_16x16x32_bf16 v[232:235], a[244:247], a[156:159], v[232:235]\n"
    "v_fma_f32 v89, v89, v101, v81\n"
    "v_fma_f32 v90, v90, v101, v81\n"
    "v_mfma_f32_16x16x32_bf16 v[236:239], a[244:247], a[212:215], v[236:239]\n"
    "s_waitcnt lgkmcnt(5)\n"
    "v_fma_f32 v91, v91, v101, v81\n"
    "v_mul_f32 v88, v88, v52\n"
    "v_mul_f32 v89, v89, v53\n"
    "v_mfma_f32_16x16x32_bf16 v[176:179], a[248:251], a[44:47], v[176:179]\n"
    "s_add_u32 s76, s75, 512\n"
    "ds_read_b64_tr_b16 a[228:229], v61 offset:32768\n"
    "ds_read_b64_tr_b16 a[230:231], v61 offset:49152\n"
    "v_mfma_f32_16x16x32_bf16 v[180:183], a[248:251], a[100:103], v[180:183]\n"
    "buffer_load_dwordx4 v121, s[48:51], s75 offen offset:1024 lds\n"
    "v_mul_f32 v90, v90, v54\n"
    "v_mfma_f32_16x16x32_bf16 v[240:243], a[248:251], a[156:159], v[240:243]\n"
    "v_mul_f32 v91, v91, v55\n"
    "v_add_u32 v105, s89, v105\n"
    "v_mfma_f32_16x16x32_bf16 v[244:247], a[248:251], a[212:215], v[244:247]\n"
    "s_waitcnt lgkmcnt(5)\n"
    "v_exp_f32 v88, v88\n"
    "v_exp_f32 v89, v89\n"
    "v_mfma_f32_16x16x32_bf16 v[184:187], a[252:255], a[44:47], v[184:187]\n"
    "ds_read_b64_tr_b16 a[232:233], v62 offset:32768\n"
    "ds_read_b64_tr_b16 a[234:235], v62 offset:49152\n"
    "v_mfma_f32_16x16x32_bf16 v[188:191], a[252:255], a[100:103], v[188:191]\n"
    "buffer_load_dwordx4 v122, s[48:51], s75 offen offset:2048 lds\n"
    "v_mfma_f32_16x16x32_bf16 v[248:251], a[252:255], a[156:159], v[248:251]\n"
    "v_exp_f32 v90, v90\n"
    "v_mfma_f32_16x16x32_bf16 v[252:255], a[252:255], a[212:215], v[252:255]\n"
    "s_waitcnt lgkmcnt(4)\n"
    "v_exp_f32 v91, v91\n"
    "v_fma_f32 v88, v88, v77, v77\n"
    "v_fma_f32 v89, v89, v77, v77\n"
    "v_mfma_f32_16x16x32_bf16 v[128:131], a[224:227], a[48:51], v[128:131]\n"
    "ds_read_b64_tr_b16 a[236:237], v63 offset:32768\n"
    "ds_read_b64_tr_b16 a[238:239], v63 offset:49152\n"
    "v_mfma_f32_16x16x32_bf16 v[132:135], a[224:227], a[104:107], v[132:135]\n"
    "buffer_load_dwordx4 v123, s[48:51], s75 offen offset:3072 lds\n"
    "v_fma_f32 v90, v90, v77, v77\n"
    "v_mfma_f32_16x16x32_bf16 v[192:195], a[224:227], a[160:163], v[192:195]\n"
    "v_fma_f32 v91, v91, v77, v77\n"
    "v_rcp_f32 v88, v88\n"
    "v_mfma_f32_16x16x32_bf16 v[196:199], a[224:227], a[216:219], v[196:199]\n"
    "s_waitcnt lgkmcnt(4)\n"
    "v_rcp_f32 v89, v89\n"
    "v_mfma_f32_16x16x32_bf16 v[136:139], a[228:231], a[48:51], v[136:139]\n"
    "s_add_u32 m0, s90, 36864\n"
    "ds_read_b64_tr_b16 a[240:241], v60 offset:33792\n"
    "ds_read_b64_tr_b16 a[242:243], v60 offset:50176\n"
    "v_mfma_f32_16x16x32_bf16 v[140:143], a[228:231], a[104:107], v[140:143]\n"
    "buffer_load_dwordx4 v120, s[48:51], s76 offen offset:0 lds\n"
    "v_mfma_f32_16x16x32_bf16 v[200:203], a[228:231], a[160:163], v[200:203]\n"
    "v_rcp_f32 v90, v90\n"
    "v_rcp_f32 v91, v91\n"
    "v_mfma_f32_16x16x32_bf16 v[204:207], a[228:231], a[216:219], v[204:207]\n"
    "s_waitcnt lgkmcnt(4)\n"
    "v_mul_f32 v52, v52, v88\n"
    "v_mul_f32 v53, v53, v89\n"
    "v_mfma_f32_16x16x32_bf16 v[144:147], a[232:235], a[48:51], v[144:147]\n"
    "ds_read_b64_tr_b16 a[244:245], v61 offset:33792\n"
    "ds_read_b64_tr_b16 a[246:247], v61 offset:50176\n"
    "v_mul_f32 v54, v54, v90\n"
    "v_mfma_f32_16x16x32_bf16 v[148:151], a[232:235], a[104:107], v[148:151]\n"
    "buffer_load_dwordx4 v121, s[48:51], s76 offen offset:1024 lds\n"
    "v_mfma_f32_16x16x32_bf16 v[208:211], a[232:235], a[160:163], v[208:211]\n"
    "v_mul_f32 v55, v55, v91\n"
    "v_cvt_pk_bf16_f32 v70, v52, v53\n"
    "v_cvt_pk_bf16_f32 v71, v54, v55\n"
    "v_mfma_f32_16x16x32_bf16 v[212:215], a[232:235], a[216:219], v[212:215]\n"
    "s_waitcnt lgkmcnt(4)\n"
    "v_mfma_f32_16x16x32_bf16 v[152:155], a[236:239], a[48:51], v[152:155]\n"
    "ds_read_b64_tr_b16 a[248:249], v62 offset:33792\n"
    "ds_read_b64_tr_b16 a[250:251], v62 offset:50176\n"
    "v_mfma_f32_16x16x32_bf16 v[156:159], a[236:239], a[104:107], v[156:159]\n"
    "buffer_load_dwordx4 v122, s[48:51], s76 offen offset:2048 lds\n"
    "v_mfma_f32_16x16x32_bf16 v[216:219], a[236:239], a[160:163], v[216:219]\n"
    "ds_write_b64 v105, v[70:71]\n"
    "v_mfma_f32_16x16x32_bf16 v[220:223], a[236:239], a[216:219], v[220:223]\n"
    "s_waitcnt lgkmcnt(5)\n"
    "v_mfma_f32_16x16x32_bf16 v[160:163], a[240:243], a[48:51], v[160:163]\n"
    "ds_read_b64_tr_b16 a[252:253], v63 offset:33792\n"
    "ds_read_b64_tr_b16 a[254:255], v63 offset:50176\n"
    "v_mfma_f32_16x16x32_bf16 v[164:167], a[240:243], a[104:107], v[164:167]\n"
    "v_mfma_f32_16x16x32_bf16 v[224:227], a[240:243], a[160:163], v[224:227]\n"
    "s_mov_b32 s75, 43008\n"
    "v_mfma_f32_16x16x32_bf16 v[228:231], a[240:243], a[216:219], v[228:231]\n"
    "s_waitcnt lgkmcnt(0)\n"
    "s_waitcnt vmcnt(7)\n"
    "s_barrier\n"
    "v_mfma_f32_16x16x32_bf16 v[168:171], a[244:247], a[48:51], v[168:171]\n"
    "s_add_u32 m0, s90, 65536\n"
    "ds_read_b64_tr_b16 a[224:225], v56 offset:0\n"
    "ds_read_b64_tr_b16 a[226:227], v56 offset:16384\n"
    "v_mfma_f32_16x16x32_bf16 v[172:175], a[244:247], a[104:107], v[172:175]\n"
    "buffer_load_dwordx4 v120, s[48:51], s75 offen offset:0 lds\n"
    "v_mfma_f32_16x16x32_bf16 v[232:235], a[244:247], a[160:163], v[232:235]\n"
    "v_mfma_f32_16x16x32_bf16 v[236:239], a[244:247], a[216:219], v[236:239]\n"
    "v_mfma_f32_16x16x32_bf16 v[176:179], a[248:251], a[48:51], v[176:179]\n"
    "s_add_u32 s76, s75, 512\n"
    "ds_read_b64_tr_b16 a[228:229], v57 offset:0\n"
    "ds_read_b64_tr_b16 a[230:231], v57 offset:16384\n"
    "v_mfma_f32_16x16x32_bf16 v[180:183], a[248:251], a[104:107], v[180:183]\n"
    "buffer_load_dwordx4 v121, s[48:51], s75 offen offset:1024 lds\n"
    "v_mfma_f32_16x16x32_bf16 v[240:243], a[248:251], a[160:163], v[240:243]\n"
    "v_mfma_f32_16x16x32_bf16 v[244:247], a[248:251], a[216:219], v[244:247]\n"
    "v_mfma_f32_16x16x32_bf16 v[184:187], a[252:255], a[48:51], v[184:187]\n"
    "ds_read_b64_tr_b16 a[232:233], v58 offset:0\n"
    "ds_read_b64_tr_b16 a[234:235], v58 offset:16384\n"
    "v_mfma_f32_16x16x32_bf16 v[188:191], a[252:255], a[104:107], v[188:191]\n"
    "buffer_load_dwordx4 v122, s[48:51], s75 offen offset:2048 lds\n"
    "v_mfma_f32_16x16x32_bf16 v[248:251], a[252:255], a[160:163], v[248:251]\n"
    "v_mfma_f32_16x16x32_bf16 v[252:255], a[252:255], a[216:219], v[252:255]\n"
    "s_waitcnt lgkmcnt(4)\n"
    "v_mfma_f32_16x16x32_bf16 v[128:131], a[224:227], a[52:55], v[128:131]\n"
    "ds_read_b64_tr_b16 a[236:237], v59 offset:0\n"
    "ds_read_b64_tr_b16 a[238:239], v59 offset:16384\n"
    "v_mfma_f32_16x16x32_bf16 v[132:135], a[224:227], a[108:111], v[132:135]\n"
    "buffer_load_dwordx4 v123, s[48:51], s75 offen offset:3072 lds\n"
    "v_mfma_f32_16x16x32_bf16 v[192:195], a[224:227], a[164:167], v[192:195]\n"
    "ds_read_b128 a[0:3], v124 offset:0\n"
    "v_mfma_f32_16x16x32_bf16 v[196:199], a[224:227], a[220:223], v[196:199]\n"
    "s_waitcnt lgkmcnt(5)\n"
    "v_mfma_f32_16x16x32_bf16 v[136:139], a[228:231], a[52:55], v[136:139]\n"
    "s_add_u32 m0, s90, 69632\n"
    "ds_read_b64_tr_b16 a[240:241], v56 offset:1024\n"
    "ds_read_b64_tr_b16 a[242:243], v56 offset:17408\n"
    "v_mfma_f32_16x16x32_bf16 v[140:143], a[228:231], a[108:111], v[140:143]\n"
    "buffer_load_dwordx4 v120, s[48:51], s76 offen offset:0 lds\n"
    "v_mfma_f32_16x16x32_bf16 v[200:203], a[228:231], a[164:167], v[200:203]\n"
    "ds_read_b128 a[4:7], v125 offset:1024\n"
    "v_mfma_f32_16x16x32_bf16 v[204:207], a[228:231], a[220:223], v[204:207]\n"
    "s_waitcnt lgkmcnt(6)\n"
    "v_mfma_f32_16x16x32_bf16 v[144:147], a[232:235], a[52:55], v[144:147]\n"
    "ds_read_b64_tr_b16 a[244:245], v57 offset:1024\n"
    "ds_read_b64_tr_b16 a[246:247], v57 offset:17408\n"
    "v_mfma_f32_16x16x32_bf16 v[148:151], a[232:235], a[108:111], v[148:151]\n"
    "buffer_load_dwordx4 v121, s[48:51], s76 offen offset:1024 lds\n"
    "v_mfma_f32_16x16x32_bf16 v[208:211], a[232:235], a[164:167], v[208:211]\n"
    "ds_read_b128 a[8:11], v124 offset:2048\n"
    "v_mfma_f32_16x16x32_bf16 v[212:215], a[232:235], a[220:223], v[212:215]\n"
    "s_waitcnt lgkmcnt(7)\n"
    "v_mfma_f32_16x16x32_bf16 v[152:155], a[236:239], a[52:55], v[152:155]\n"
    "ds_read_b64_tr_b16 a[248:249], v58 offset:1024\n"
    "ds_read_b64_tr_b16 a[250:251], v58 offset:17408\n"
    "v_mfma_f32_16x16x32_bf16 v[156:159], a[236:239], a[108:111], v[156:159]\n"
    "buffer_load_dwordx4 v122, s[48:51], s76 offen offset:2048 lds\n"
    "v_mfma_f32_16x16x32_bf16 v[216:219], a[236:239], a[164:167], v[216:219]\n"
    "ds_read_b128 a[12:15], v125 offset:3072\n"
    "v_mfma_f32_16x16x32_bf16 v[220:223], a[236:239], a[220:223], v[220:223]\n"
    "s_waitcnt lgkmcnt(7)\n"
    "v_mfma_f32_16x16x32_bf16 v[160:163], a[240:243], a[52:55], v[160:163]\n"
    "ds_read_b64_tr_b16 a[252:253], v59 offset:1024\n"
    "ds_read_b64_tr_b16 a[254:255], v59 offset:17408\n"
    "v_mfma_f32_16x16x32_bf16 v[164:167], a[240:243], a[108:111], v[164:167]\n"
    "v_mfma_f32_16x16x32_bf16 v[224:227], a[240:243], a[164:167], v[224:227]\n"
    "s_mov_b32 s75, 86016\n"
    "v_mfma_f32_16x16x32_bf16 v[228:231], a[240:243], a[220:223], v[228:231]\n"
    "s_waitcnt vmcnt(7) lgkmcnt(6)\n"
    "s_barrier\n"
    "v_mfma_f32_16x16x32_bf16 v[168:171], a[244:247], a[52:55], v[168:171]\n"
    "s_add_u32 m0, s90, 98304\n"
    "ds_read_b64_tr_b16 a[224:225], v112 offset:32768\n"
    "ds_read_b64_tr_b16 a[226:227], v112 offset:49152\n"
    "v_mfma_f32_16x16x32_bf16 v[172:175], a[244:247], a[108:111], v[172:175]\n"
    "buffer_load_dwordx4 v120, s[48:51], s75 offen offset:0 lds\n"
    "v_mfma_f32_16x16x32_bf16 v[232:235], a[244:247], a[164:167], v[232:235]\n"
    "v_mfma_f32_16x16x32_bf16 v[236:239], a[244:247], a[220:223], v[236:239]\n"
    "s_waitcnt lgkmcnt(5)\n"
    "v_mfma_f32_16x16x32_bf16 v[176:179], a[248:251], a[52:55], v[176:179]\n"
    "s_add_u32 s76, s75, 512\n"
    "ds_read_b64_tr_b16 a[228:229], v113 offset:32768\n"
    "ds_read_b64_tr_b16 a[230:231], v113 offset:49152\n"
    "v_mfma_f32_16x16x32_bf16 v[180:183], a[248:251], a[108:111], v[180:183]\n"
    "buffer_load_dwordx4 v121, s[48:51], s75 offen offset:1024 lds\n"
    "v_mfma_f32_16x16x32_bf16 v[240:243], a[248:251], a[164:167], v[240:243]\n"
    "v_mfma_f32_16x16x32_bf16 v[244:247], a[248:251], a[220:223], v[244:247]\n"
    "s_waitcnt lgkmcnt(4)\n"
    "v_mfma_f32_16x16x32_bf16 v[184:187], a[252:255], a[52:55], v[184:187]\n"
    "ds_read_b64_tr_b16 a[232:233], v114 offset:32768\n"
    "ds_read_b64_tr_b16 a[234:235], v114 offset:49152\n"
    "v_mfma_f32_16x16x32_bf16 v[188:191], a[252:255], a[108:111], v[188:191]\n"
    "buffer_load_dwordx4 v122, s[48:51], s75 offen offset:2048 lds\n"
    "v_mfma_f32_16x16x32_bf16 v[248:251], a[252:255], a[164:167], v[248:251]\n"
    "v_mfma_f32_16x16x32_bf16 v[252:255], a[252:255], a[220:223], v[252:255]\n"
    "s_waitcnt lgkmcnt(4)\n"
    "v_mfma_f32_16x16x32_bf16 a[64:67], a[224:227], a[0:3], 0\n"
    "ds_read_b64_tr_b16 a[236:237], v115 offset:32768\n"
    "ds_read_b64_tr_b16 a[238:239], v115 offset:49152\n"
    "v_mfma_f32_16x16x32_bf16 a[68:71], a[224:227], a[4:7], 0\n"
    "buffer_load_dwordx4 v123, s[48:51], s75 offen offset:3072 lds\n"
    "v_mfma_f32_16x16x32_bf16 a[72:75], a[224:227], a[8:11], 0\n"
    "ds_read_b128 a[16:19], v124 offset:4096\n"
    "v_mfma_f32_16x16x32_bf16 a[76:79], a[224:227], a[12:15], 0\n"
    "s_waitcnt lgkmcnt(5)\n"
    "v_mfma_f32_16x16x32_bf16 a[80:83], a[228:231], a[0:3], 0\n"
    "s_add_u32 m0, s90, 102400\n"
    "ds_read_b64_tr_b16 a[240:241], v112 offset:33792\n"
    "ds_read_b64_tr_b16 a[242:243], v112 offset:50176\n"
    "v_mfma_f32_16x16x32_bf16 a[84:87], a[228:231], a[4:7], 0\n"
    "buffer_load_dwordx4 v120, s[48:51], s76 offen offset:0 lds\n"
    "v_mfma_f32_16x16x32_bf16 a[88:91], a[228:231], a[8:11], 0\n"
    "ds_read_b128 a[20:23], v125 offset:5120\n"
    "v_cvt_pk_bf16_f32 v128, v128, v129\n"
    "v_mfma_f32_16x16x32_bf16 a[92:95], a[228:231], a[12:15], 0\n"
    "s_waitcnt lgkmcnt(6)\n"
    "v_cvt_pk_bf16_f32 v129, v130, v131\n"
    "v_mfma_f32_16x16x32_bf16 a[96:99], a[232:235], a[0:3], 0\n"
    "ds_read_b64_tr_b16 a[244:245], v113 offset:33792\n"
    "ds_read_b64_tr_b16 a[246:247], v113 offset:50176\n"
    "v_mfma_f32_16x16x32_bf16 a[100:103], a[232:235], a[4:7], 0\n"
    "buffer_load_dwordx4 v121, s[48:51], s76 offen offset:1024 lds\n"
    "v_mfma_f32_16x16x32_bf16 a[104:107], a[232:235], a[8:11], 0\n"
    "ds_read_b128 a[24:27], v124 offset:6144\n"
    "v_cvt_pk_bf16_f32 v130, v136, v137\n"
    "v_mfma_f32_16x16x32_bf16 a[108:111], a[232:235], a[12:15], 0\n"
    "s_waitcnt lgkmcnt(7)\n"
    "v_cvt_pk_bf16_f32 v131, v138, v139\n"
    "v_mfma_f32_16x16x32_bf16 a[112:115], a[236:239], a[0:3], 0\n"
    "ds_read_b64_tr_b16 a[248:249], v114 offset:33792\n"
    "ds_read_b64_tr_b16 a[250:251], v114 offset:50176\n"
    "v_mfma_f32_16x16x32_bf16 a[116:119], a[236:239], a[4:7], 0\n"
    "buffer_load_dwordx4 v122, s[48:51], s76 offen offset:2048 lds\n"
    "v_mfma_f32_16x16x32_bf16 a[120:123], a[236:239], a[8:11], 0\n"
    "s_mov_b32 s75, 129024\n"
    "ds_read_b128 a[28:31], v125 offset:7168\n"
    "v_cvt_pk_bf16_f32 v132, v132, v133\n"
    "v_mfma_f32_16x16x32_bf16 a[124:127], a[236:239], a[12:15], 0\n"
    "s_waitcnt vmcnt(7) lgkmcnt(7)\n"
    "s_barrier\n"
    "v_mfma_f32_16x16x32_bf16 a[128:131], a[240:243], a[0:3], 0\n"
    "s_add_u32 m0, s90, 0\n"
    "ds_read_b64_tr_b16 a[252:253], v116 offset:0\n"
    "ds_read_b64_tr_b16 a[254:255], v116 offset:16384\n"
    "v_mfma_f32_16x16x32_bf16 a[132:135], a[240:243], a[4:7], 0\n"
    "buffer_load_dwordx4 v120, s[48:51], s75 offen offset:0 lds\n"
    "v_mfma_f32_16x16x32_bf16 a[136:139], a[240:243], a[8:11], 0\n"
    "v_cvt_pk_bf16_f32 v133, v134, v135\n"
    "v_cvt_pk_bf16_f32 v134, v140, v141\n"
    "v_mfma_f32_16x16x32_bf16 a[140:143], a[240:243], a[12:15], 0\n"
    "s_waitcnt lgkmcnt(6)\n"
    "v_cvt_pk_bf16_f32 v135, v142, v143\n"
    "v_mfma_f32_16x16x32_bf16 a[144:147], a[244:247], a[0:3], 0\n"
    "s_add_u32 s76, s75, 512\n"
    "ds_read_b64_tr_b16 a[224:225], v117 offset:0\n"
    "ds_read_b64_tr_b16 a[226:227], v117 offset:16384\n"
    "v_mfma_f32_16x16x32_bf16 a[148:151], a[244:247], a[4:7], 0\n"
    "buffer_load_dwordx4 v121, s[48:51], s75 offen offset:1024 lds\n"
    "v_mfma_f32_16x16x32_bf16 a[152:155], a[244:247], a[8:11], 0\n"
    "v_permlane32_swap_b32 v128, v130\n"
    "v_mfma_f32_16x16x32_bf16 a[156:159], a[244:247], a[12:15], 0\n"
    "s_waitcnt lgkmcnt(5)\n"
    "v_permlane32_swap_b32 v129, v131\n"
    "v_mfma_f32_16x16x32_bf16 a[160:163], a[248:251], a[0:3], 0\n"
    "ds_read_b64_tr_b16 a[228:229], v118 offset:0\n"
    "ds_read_b64_tr_b16 a[230:231], v118 offset:16384\n"
    "v_mfma_f32_16x16x32_bf16 a[164:167], a[248:251], a[4:7], 0\n"
    "buffer_load_dwordx4 v122, s[48:51], s75 offen offset:2048 lds\n"
    "v_mfma_f32_16x16x32_bf16 a[168:171], a[248:251], a[8:11], 0\n"
    "v_cvt_pk_bf16_f32 v192, v192, v193\n"
    "v_mfma_f32_16x16x32_bf16 a[172:175], a[248:251], a[12:15], 0\n"
    "s_waitcnt lgkmcnt(4)\n"
    "v_cvt_pk_bf16_f32 v193, v194, v195\n"
    "v_mfma_f32_16x16x32_bf16 a[64:67], a[252:255], a[16:19], a[64:67]\n"
    "ds_read_b64_tr_b16 a[232:233], v119 offset:0\n"
    "ds_read_b64_tr_b16 a[234:235], v119 offset:16384\n"
    "v_mfma_f32_16x16x32_bf16 a[68:71], a[252:255], a[20:23], a[68:71]\n"
    "buffer_load_dwordx4 v123, s[48:51], s75 offen offset:3072 lds\n"
    "v_mfma_f32_16x16x32_bf16 a[72:75], a[252:255], a[24:27], a[72:75]\n"
    "ds_read_b128 a[0:3], v124 offset:8192\n"
    "v_cvt_pk_bf16_f32 v194, v200, v201\n"
    "v_mfma_f32_16x16x32_bf16 a[76:79], a[252:255], a[28:31], a[76:79]\n"
    "s_waitcnt lgkmcnt(5)\n"
    "v_cvt_pk_bf16_f32 v195, v202, v203\n"
    "v_mfma_f32_16x16x32_bf16 a[80:83], a[224:227], a[16:19], a[80:83]\n"
    "s_add_u32 m0, s90, 4096\n"
    "ds_read_b64_tr_b16 a[236:237], v116 offset:1024\n"
    "ds_read_b64_tr_b16 a[238:239], v116 offset:17408\n"
    "v_mfma_f32_16x16x32_bf16 a[84:87], a[224:227], a[20:23], a[84:87]\n"
    "buffer_load_dwordx4 v120, s[48:51], s76 offen offset:0 lds\n"
    "v_mfma_f32_16x16x32_bf16 a[88:91], a[224:227], a[24:27], a[88:91]\n"
    "ds_read_b128 a[4:7], v125 offset:9216\n"
    "v_permlane32_swap_b32 v132, v134\n"
    "v_mfma_f32_16x16x32_bf16 a[92:95], a[224:227], a[28:31], a[92:95]\n"
    "s_waitcnt lgkmcnt(6)\n"
    "v_mfma_f32_16x16x32_bf16 a[96:99], a[228:231], a[16:19], a[96:99]\n"
    "ds_read_b64_tr_b16 a[240:241], v117 offset:1024\n"
    "ds_read_b64_tr_b16 a[242:243], v117 offset:17408\n"
    "v_mfma_f32_16x16x32_bf16 a[100:103], a[228:231], a[20:23], a[100:103]\n"
    "buffer_load_dwordx4 v121, s[48:51], s76 offen offset:1024 lds\n"
    "v_mfma_f32_16x16x32_bf16 a[104:107], a[228:231], a[24:27], a[104:107]\n"
    "ds_read_b128 a[8:11], v124 offset:10240\n"
    "v_permlane32_swap_b32 v133, v135\n"
    "v_mfma_f32_16x16x32_bf16 a[108:111], a[228:231], a[28:31], a[108:111]\n"
    "s_waitcnt lgkmcnt(7)\n"
    "v_cvt_pk_bf16_f32 v196, v196, v197\n"
    "v_mfma_f32_16x16x32_bf16 a[112:115], a[232:235], a[16:19], a[112:115]\n"
    "ds_read_b64_tr_b16 a[244:245], v118 offset:1024\n"
    "ds_read_b64_tr_b16 a[246:247], v118 offset:17408\n"
    "v_mfma_f32_16x16x32_bf16 a[116:119], a[232:235], a[20:23], a[116:119]\n"
    "buffer_load_dwordx4 v122, s[48:51], s76 offen offset:2048 lds\n"
    "v_mfma_f32_16x16x32_bf16 a[120:123], a[232:235], a[24:27], a[120:123]\n"
    "s_mul_i32 s75, s73, 28672\n"
    "s_add_u32 s75, s75, 28672\n"
    "ds_read_b128 a[12:15], v125 offset:11264\n"
    "v_mfma_f32_16x16x32_bf16 a[124:127], a[232:235], a[28:31], a[124:127]\n"
    "s_waitcnt vmcnt(7) lgkmcnt(7)\n"
    "s_barrier\n"
    "v_mfma_f32_16x16x32_bf16 a[128:131], a[236:239], a[16:19], a[128:131]\n"
    "s_add_u32 m0, s90, 32768\n"
    "ds_read_b64_tr_b16 a[248:249], v116 offset:32768\n"
    "ds_read_b64_tr_b16 a[250:251], v116 offset:49152\n"
    "v_mfma_f32_16x16x32_bf16 a[132:135], a[236:239], a[20:23], a[132:135]\n"
    "buffer_load_dwordx4 v120, s[48:51], s75 offen offset:0 lds\n"
    "v_mfma_f32_16x16x32_bf16 a[136:139], a[236:239], a[24:27], a[136:139]\n"
    "v_cvt_pk_bf16_f32 v197, v198, v199\n"
    "v_mfma_f32_16x16x32_bf16 a[140:143], a[236:239], a[28:31], a[140:143]\n"
    "s_waitcnt lgkmcnt(6)\n"
    "v_cvt_pk_bf16_f32 v198, v204, v205\n"
    "v_cvt_pk_bf16_f32 v199, v206, v207\n"
    "v_mfma_f32_16x16x32_bf16 a[144:147], a[240:243], a[16:19], a[144:147]\n"
    "s_add_u32 s76, s75, 512\n"
    "ds_read_b64_tr_b16 a[252:253], v117 offset:32768\n"
    "ds_read_b64_tr_b16 a[254:255], v117 offset:49152\n"
    "v_mfma_f32_16x16x32_bf16 a[148:151], a[240:243], a[20:23], a[148:151]\n"
    "buffer_load_dwordx4 v121, s[48:51], s75 offen offset:1024 lds\n"
    "v_mfma_f32_16x16x32_bf16 a[152:155], a[240:243], a[24:27], a[152:155]\n"
    "v_permlane32_swap_b32 v192, v194\n"
    "v_mfma_f32_16x16x32_bf16 a[156:159], a[240:243], a[28:31], a[156:159]\n"
    "s_waitcnt lgkmcnt(5)\n"
    "v_mfma_f32_16x16x32_bf16 a[160:163], a[244:247], a[16:19], a[160:163]\n"
    "ds_read_b64_tr_b16 a[224:225], v118 offset:32768\n"
    "ds_read_b64_tr_b16 a[226:227], v118 offset:49152\n"
    "v_mfma_f32_16x16x32_bf16 a[164:167], a[244:247], a[20:23], a[164:167]\n"
    "buffer_load_dwordx4 v122, s[48:51], s75 offen offset:2048 lds\n"
    "v_mfma_f32_16x16x32_bf16 a[168:171], a[244:247], a[24:27], a[168:171]\n"
    "v_permlane32_swap_b32 v193, v195\n"
    "v_mfma_f32_16x16x32_bf16 a[172:175], a[244:247], a[28:31], a[172:175]\n"
    "s_waitcnt lgkmcnt(4)\n"
    "v_cvt_pk_bf16_f32 v136, v144, v145\n"
    "v_cvt_pk_bf16_f32 v137, v146, v147\n"
    "v_mfma_f32_16x16x32_bf16 a[64:67], a[248:251], a[0:3], a[64:67]\n"
    "ds_read_b64_tr_b16 a[228:229], v119 offset:32768\n"
    "ds_read_b64_tr_b16 a[230:231], v119 offset:49152\n"
    "v_mfma_f32_16x16x32_bf16 a[68:71], a[248:251], a[4:7], a[68:71]\n"
    "buffer_load_dwordx4 v123, s[48:51], s75 offen offset:3072 lds\n"
    "v_mfma_f32_16x16x32_bf16 a[72:75], a[248:251], a[8:11], a[72:75]\n"
    "ds_read_b128 a[16:19], v124 offset:12288\n"
    "v_mfma_f32_16x16x32_bf16 a[76:79], a[248:251], a[12:15], a[76:79]\n"
    "s_waitcnt lgkmcnt(5)\n"
    "v_cvt_pk_bf16_f32 v138, v152, v153\n"
    "v_cvt_pk_bf16_f32 v139, v154, v155\n"
    "v_mfma_f32_16x16x32_bf16 a[80:83], a[252:255], a[0:3], a[80:83]\n"
    "s_add_u32 m0, s90, 36864\n"
    "ds_read_b64_tr_b16 a[232:233], v116 offset:33792\n"
    "ds_read_b64_tr_b16 a[234:235], v116 offset:50176\n"
    "v_mfma_f32_16x16x32_bf16 a[84:87], a[252:255], a[4:7], a[84:87]\n"
    "buffer_load_dwordx4 v120, s[48:51], s76 offen offset:0 lds\n"
    "v_mfma_f32_16x16x32_bf16 a[88:91], a[252:255], a[8:11], a[88:91]\n"
    "ds_read_b128 a[20:23], v125 offset:13312\n"
    "v_mfma_f32_16x16x32_bf16 a[92:95], a[252:255], a[12:15], a[92:95]\n"
    "s_waitcnt lgkmcnt(6)\n"
    "v_permlane32_swap_b32 v196, v198\n"
    "v_mfma_f32_16x16x32_bf16 a[96:99], a[224:227], a[0:3], a[96:99]\n"
    "ds_read_b64_tr_b16 a[236:237], v117 offset:33792\n"
    "ds_read_b64_tr_b16 a[238:239], v117 offset:50176\n"
    "v_mfma_f32_16x16x32_bf16 a[100:103], a[224:227], a[4:7], a[100:103]\n"
    "buffer_load_dwordx4 v121, s[48:51], s76 offen offset:1024 lds\n"
    "v_mfma_f32_16x16x32_bf16 a[104:107], a[224:227], a[8:11], a[104:107]\n"
    "ds_read_b128 a[24:27], v124 offset:14336\n"
    "v_mfma_f32_16x16x32_bf16 a[108:111], a[224:227], a[12:15], a[108:111]\n"
    "s_waitcnt lgkmcnt(7)\n"
    "v_permlane32_swap_b32 v197, v199\n"
    "v_mfma_f32_16x16x32_bf16 a[112:115], a[228:231], a[0:3], a[112:115]\n"
    "ds_read_b64_tr_b16 a[240:241], v118 offset:33792\n"
    "ds_read_b64_tr_b16 a[242:243], v118 offset:50176\n"
    "v_mfma_f32_16x16x32_bf16 a[116:119], a[228:231], a[4:7], a[116:119]\n"
    "buffer_load_dwordx4 v122, s[48:51], s76 offen offset:2048 lds\n"
    "v_mfma_f32_16x16x32_bf16 a[120:123], a[228:231], a[8:11], a[120:123]\n"
    "s_mul_i32 s75, s73, 28672\n"
    "s_add_u32 s75, s75, 114688\n"
    "ds_read_b128 a[28:31], v125 offset:15360\n"
    "v_mfma_f32_16x16x32_bf16 a[124:127], a[228:231], a[12:15], a[124:127]\n"
    "s_waitcnt vmcnt(7) lgkmcnt(7)\n"
    "s_barrier\n"
    "v_mfma_f32_16x16x32_bf16 a[128:131], a[232:235], a[0:3], a[128:131]\n"
    "s_add_u32 m0, s90, 65536\n"
    "ds_read_b64_tr_b16 a[244:245], v112 offset:0\n"
    "ds_read_b64_tr_b16 a[246:247], v112 offset:16384\n"
    "v_mfma_f32_16x16x32_bf16 a[132:135], a[232:235], a[4:7], a[132:135]\n"
    "buffer_load_dwordx4 v120, s[48:51], s75 offen offset:0 lds\n"
    "v_mfma_f32_16x16x32_bf16 a[136:139], a[232:235], a[8:11], a[136:139]\n"
    "v_cvt_pk_bf16_f32 v140, v148, v149\n"
    "v_cvt_pk_bf16_f32 v141, v150, v151\n"
    "v_mfma_f32_16x16x32_bf16 a[140:143], a[232:235], a[12:15], a[140:143]\n"
    "s_waitcnt lgkmcnt(6)\n"
    "v_cvt_pk_bf16_f32 v142, v156, v157\n"
    "v_mfma_f32_16x16x32_bf16 a[144:147], a[236:239], a[0:3], a[144:147]\n"
    "s_add_u32 s76, s75, 512\n"
    "ds_read_b64_tr_b16 a[248:249], v113 offset:0\n"
    "ds_read_b64_tr_b16 a[250:251], v113 offset:16384\n"
    "v_mfma_f32_16x16x32_bf16 a[148:151], a[236:239], a[4:7], a[148:151]\n"
    "buffer_load_dwordx4 v121, s[48:51], s75 offen offset:1024 lds\n"
    "v_mfma_f32_16x16x32_bf16 a[152:155], a[236:239], a[8:11], a[152:155]\n"
    "v_cvt_pk_bf16_f32 v143, v158, v159\n"
    "v_mfma_f32_16x16x32_bf16 a[156:159], a[236:239], a[12:15], a[156:159]\n"
    "s_waitcnt lgkmcnt(5)\n"
    "v_permlane32_swap_b32 v136, v138\n"
    "v_mfma_f32_16x16x32_bf16 a[160:163], a[240:243], a[0:3], a[160:163]\n"
    "ds_read_b64_tr_b16 a[252:253], v114 offset:0\n"
    "ds_read_b64_tr_b16 a[254:255], v114 offset:16384\n"
    "v_mfma_f32_16x16x32_bf16 a[164:167], a[240:243], a[4:7], a[164:167]\n"
    "buffer_load_dwordx4 v122, s[48:51], s75 offen offset:2048 lds\n"
    "v_mfma_f32_16x16x32_bf16 a[168:171], a[240:243], a[8:11], a[168:171]\n"
    "v_permlane32_swap_b32 v137, v139\n"
    "v_mfma_f32_16x16x32_bf16 a[172:175], a[240:243], a[12:15], a[172:175]\n"
    "s_waitcnt lgkmcnt(4)\n"
    "v_cvt_pk_bf16_f32 v200, v208, v209\n"
    "v_mfma_f32_16x16x32_bf16 a[64:67], a[244:247], a[16:19], a[64:67]\n"
    "ds_read_b64_tr_b16 a[224:225], v115 offset:0\n"
    "ds_read_b64_tr_b16 a[226:227], v115 offset:16384\n"
    "v_mfma_f32_16x16x32_bf16 a[68:71], a[244:247], a[20:23], a[68:71]\n"
    "buffer_load_dwordx4 v123, s[48:51], s75 offen offset:3072 lds\n"
    "v_mfma_f32_16x16x32_bf16 a[72:75], a[244:247], a[24:27], a[72:75]\n"
    "ds_read_b128 a[0:3], v124 offset:16384\n"
    "v_cvt_pk_bf16_f32 v201, v210, v211\n"
    "v_mfma_f32_16x16x32_bf16 a[76:79], a[244:247], a[28:31], a[76:79]\n"
    "s_waitcnt lgkmcnt(5)\n"
    "v_cvt_pk_bf16_f32 v202, v216, v217\n"
    "v_cvt_pk_bf16_f32 v203, v218, v219\n"
    "v_mfma_f32_16x16x32_bf16 a[80:83], a[248:251], a[16:19], a[80:83]\n"
    "s_add_u32 m0, s90, 69632\n"
    "ds_read_b64_tr_b16 a[228:229], v112 offset:1024\n"
    "ds_read_b64_tr_b16 a[230:231], v112 offset:17408\n"
    "v_mfma_f32_16x16x32_bf16 a[84:87], a[248:251], a[20:23], a[84:87]\n"
    "buffer_load_dwordx4 v120, s[48:51], s76 offen offset:0 lds\n"
    "v_mfma_f32_16x16x32_bf16 a[88:91], a[248:251], a[24:27], a[88:91]\n"
    "ds_read_b128 a[4:7], v125 offset:17408\n"
    "v_mfma_f32_16x16x32_bf16 a[92:95], a[248:251], a[28:31], a[92:95]\n"
    "s_waitcnt lgkmcnt(6)\n"
    "v_permlane32_swap_b32 v140, v142\n"
    "v_mfma_f32_16x16x32_bf16 a[96:99], a[252:255], a[16:19], a[96:99]\n"
    "ds_read_b64_tr_b16 a[232:233], v113 offset:1024\n"
    "ds_read_b64_tr_b16 a[234:235], v113 offset:17408\n"
    "v_mfma_f32_16x16x32_bf16 a[100:103], a[252:255], a[20:23], a[100:103]\n"
    "buffer_load_dwordx4 v121, s[48:51], s76 offen offset:1024 lds\n"
    "v_mfma_f32_16x16x32_bf16 a[104:107], a[252:255], a[24:27], a[104:107]\n"
    "ds_read_b128 a[8:11], v124 offset:18432\n"
    "v_mfma_f32_16x16x32_bf16 a[108:111], a[252:255], a[28:31], a[108:111]\n"
    "s_waitcnt lgkmcnt(7)\n"
    "v_permlane32_swap_b32 v141, v143\n"
    "v_mfma_f32_16x16x32_bf16 a[112:115], a[224:227], a[16:19], a[112:115]\n"
    "ds_read_b64_tr_b16 a[236:237], v114 offset:1024\n"
    "ds_read_b64_tr_b16 a[238:239], v114 offset:17408\n"
    "v_mfma_f32_16x16x32_bf16 a[116:119], a[224:227], a[20:23], a[116:119]\n"
    "buffer_load_dwordx4 v122, s[48:51], s76 offen offset:2048 lds\n"
    "v_mfma_f32_16x16x32_bf16 a[120:123], a[224:227], a[24:27], a[120:123]\n"
    "s_mov_b32 s75, 172032\n"
    "ds_read_b128 a[12:15], v125 offset:19456\n"
    "v_cvt_pk_bf16_f32 v204, v212, v213\n"
    "v_mfma_f32_16x16x32_bf16 a[124:127], a[224:227], a[28:31], a[124:127]\n"
    "s_waitcnt vmcnt(7) lgkmcnt(7)\n"
    "s_barrier\n"
    "v_mfma_f32_16x16x32_bf16 a[128:131], a[228:231], a[16:19], a[128:131]\n"
    "s_add_u32 m0, s90, 98304\n"
    "ds_read_b64_tr_b16 a[240:241], v112 offset:32768\n"
    "ds_read_b64_tr_b16 a[242:243], v112 offset:49152\n"
    "v_mfma_f32_16x16x32_bf16 a[132:135], a[228:231], a[20:23], a[132:135]\n"
    "buffer_load_dwordx4 v120, s[48:51], s75 offen offset:0 lds\n"
    "v_mfma_f32_16x16x32_bf16 a[136:139], a[228:231], a[24:27], a[136:139]\n"
    "v_cvt_pk_bf16_f32 v205, v214, v215\n"
    "v_mfma_f32_16x16x32_bf16 a[140:143], a[228:231], a[28:31], a[140:143]\n"
    "s_waitcnt lgkmcnt(6)\n"
    "v_cvt_pk_bf16_f32 v206, v220, v221\n"
    "v_cvt_pk_bf16_f32 v207, v222, v223\n"
    "v_mfma_f32_16x16x32_bf16 a[144:147], a[232:235], a[16:19], a[144:147]\n"
    "s_add_u32 s76, s75, 512\n"
    "ds_read_b64_tr_b16 a[244:245], v113 offset:32768\n"
    "ds_read_b64_tr_b16 a[246:247], v113 offset:49152\n"
    "v_mfma_f32_16x16x32_bf16 a[148:151], a[232:235], a[20:23], a[148:151]\n"
    "buffer_load_dwordx4 v121, s[48:51], s75 offen offset:1024 lds\n"
    "v_mfma_f32_16x16x32_bf16 a[152:155], a[232:235], a[24:27], a[152:155]\n"
    "v_permlane32_swap_b32 v200, v202\n"
    "v_mfma_f32_16x16x32_bf16 a[156:159], a[232:235], a[28:31], a[156:159]\n"
    "s_waitcnt lgkmcnt(5)\n"
    "v_mfma_f32_16x16x32_bf16 a[160:163], a[236:239], a[16:19], a[160:163]\n"
    "ds_read_b64_tr_b16 a[248:249], v114 offset:32768\n"
    "ds_read_b64_tr_b16 a[250:251], v114 offset:49152\n"
    "v_mfma_f32_16x16x32_bf16 a[164:167], a[236:239], a[20:23], a[164:167]\n"
    "buffer_load_dwordx4 v122, s[48:51], s75 offen offset:2048 lds\n"
    "v_mfma_f32_16x16x32_bf16 a[168:171], a[236:239], a[24:27], a[168:171]\n"
    "v_permlane32_swap_b32 v201, v203\n"
    "v_mfma_f32_16x16x32_bf16 a[172:175], a[236:239], a[28:31], a[172:175]\n"
    "s_waitcnt lgkmcnt(4)\n"
    "v_cvt_pk_bf16_f32 v144, v160, v161\n"
    "v_cvt_pk_bf16_f32 v145, v162, v163\n"
    "v_mfma_f32_16x16x32_bf16 a[64:67], a[240:243], a[0:3], a[64:67]\n"
    "ds_read_b64_tr_b16 a[252:253], v115 offset:32768\n"
    "ds_read_b64_tr_b16 a[254:255], v115 offset:49152\n"
    "v_mfma_f32_16x16x32_bf16 a[68:71], a[240:243], a[4:7], a[68:71]\n"
    "buffer_load_dwordx4 v123, s[48:51], s75 offen offset:3072 lds\n"
    "v_mfma_f32_16x16x32_bf16 a[72:75], a[240:243], a[8:11], a[72:75]\n"
    "ds_read_b128 a[16:19], v124 offset:20480\n"
    "v_mfma_f32_16x16x32_bf16 a[76:79], a[240:243], a[12:15], a[76:79]\n"
    "s_waitcnt lgkmcnt(5)\n"
    "v_cvt_pk_bf16_f32 v146, v168, v169\n"
    "v_cvt_pk_bf16_f32 v147, v170, v171\n"
    "v_mfma_f32_16x16x32_bf16 a[80:83], a[244:247], a[0:3], a[80:83]\n"
    "s_add_u32 m0, s90, 102400\n"
    "ds_read_b64_tr_b16 a[224:225], v112 offset:33792\n"
    "ds_read_b64_tr_b16 a[226:227], v112 offset:50176\n"
    "v_mfma_f32_16x16x32_bf16 a[84:87], a[244:247], a[4:7], a[84:87]\n"
    "buffer_load_dwordx4 v120, s[48:51], s76 offen offset:0 lds\n"
    "v_mfma_f32_16x16x32_bf16 a[88:91], a[244:247], a[8:11], a[88:91]\n"
    "ds_read_b128 a[20:23], v125 offset:21504\n"
    "v_mfma_f32_16x16x32_bf16 a[92:95], a[244:247], a[12:15], a[92:95]\n"
    "s_waitcnt lgkmcnt(6)\n"
    "v_permlane32_swap_b32 v204, v206\n"
    "v_mfma_f32_16x16x32_bf16 a[96:99], a[248:251], a[0:3], a[96:99]\n"
    "ds_read_b64_tr_b16 a[228:229], v113 offset:33792\n"
    "ds_read_b64_tr_b16 a[230:231], v113 offset:50176\n"
    "v_mfma_f32_16x16x32_bf16 a[100:103], a[248:251], a[4:7], a[100:103]\n"
    "buffer_load_dwordx4 v121, s[48:51], s76 offen offset:1024 lds\n"
    "v_mfma_f32_16x16x32_bf16 a[104:107], a[248:251], a[8:11], a[104:107]\n"
    "ds_read_b128 a[24:27], v124 offset:22528\n"
    "v_mfma_f32_16x16x32_bf16 a[108:111], a[248:251], a[12:15], a[108:111]\n"
    "s_waitcnt lgkmcnt(7)\n"
    "v_permlane32_swap_b32 v205, v207\n"
    "v_mfma_f32_16x16x32_bf16 a[112:115], a[252:255], a[0:3], a[112:115]\n"
    "ds_read_b64_tr_b16 a[232:233], v114 offset:33792\n"
    "ds_read_b64_tr_b16 a[234:235], v114 offset:50176\n"
    "v_mfma_f32_16x16x32_bf16 a[116:119], a[252:255], a[4:7], a[116:119]\n"
    "buffer_load_dwordx4 v122, s[48:51], s76 offen offset:2048 lds\n"
    "v_mfma_f32_16x16x32_bf16 a[120:123], a[252:255], a[8:11], a[120:123]\n"
    "ds_read_b128 a[28:31], v125 offset:23552\n"
    "v_cvt_pk_bf16_f32 v148, v164, v165\n"
    "v_mfma_f32_16x16x32_bf16 a[124:127], a[252:255], a[12:15], a[124:127]\n"
    "s_waitcnt vmcnt(7) lgkmcnt(7)\n"
    "s_barrier\n"
    "v_mfma_f32_16x16x32_bf16 a[128:131], a[224:227], a[0:3], a[128:131]\n"
    "ds_read_b64_tr_b16 a[236:237], v116 offset:0\n"
    "ds_read_b64_tr_b16 a[238:239], v116 offset:16384\n"
    "v_mfma_f32_16x16x32_bf16 a[132:135], a[224:227], a[4:7], a[132:135]\n"
    "v_cvt_pk_bf16_f32 v149, v166, v167\n"
    "v_cvt_pk_bf16_f32 v150, v172, v173\n"
    "v_mfma_f32_16x16x32_bf16 a[136:139], a[224:227], a[8:11], a[136:139]\n"
    "v_cvt_pk_bf16_f32 v151, v174, v175\n"
    "v_mfma_f32_16x16x32_bf16 a[140:143], a[224:227], a[12:15], a[140:143]\n"
    "s_waitcnt lgkmcnt(6)\n"
    "v_permlane32_swap_b32 v144, v146\n"
    "v_mfma_f32_16x16x32_bf16 a[144:147], a[228:231], a[0:3], a[144:147]\n"
    "ds_read_b64_tr_b16 a[240:241], v117 offset:0\n"
    "ds_read_b64_tr_b16 a[242:243], v117 offset:16384\n"
    "v_mfma_f32_16x16x32_bf16 a[148:151], a[228:231], a[4:7], a[148:151]\n"
    "v_permlane32_swap_b32 v145, v147\n"
    "v_mfma_f32_16x16x32_bf16 a[152:155], a[228:231], a[8:11], a[152:155]\n"
    "v_cvt_pk_bf16_f32 v208, v224, v225\n"
    "v_mfma_f32_16x16x32_bf16 a[156:159], a[228:231], a[12:15], a[156:159]\n"
    "s_waitcnt lgkmcnt(5)\n"
    "v_cvt_pk_bf16_f32 v209, v226, v227\n"
    "v_mfma_f32_16x16x32_bf16 a[160:163], a[232:235], a[0:3], a[160:163]\n"
    "ds_read_b64_tr_b16 a[244:245], v118 offset:0\n"
    "ds_read_b64_tr_b16 a[246:247], v118 offset:16384\n"
    "v_mfma_f32_16x16x32_bf16 a[164:167], a[232:235], a[4:7], a[164:167]\n"
    "v_cvt_pk_bf16_f32 v210, v232, v233\n"
    "v_mfma_f32_16x16x32_bf16 a[168:171], a[232:235], a[8:11], a[168:171]\n"
    "v_cvt_pk_bf16_f32 v211, v234, v235\n"
    "v_mfma_f32_16x16x32_bf16 a[172:175], a[232:235], a[12:15], a[172:175]\n"
    "s_waitcnt lgkmcnt(4)\n"
    "v_permlane32_swap_b32 v148, v150\n"
    "v_mfma_f32_16x16x32_bf16 a[64:67], a[236:239], a[16:19], a[64:67]\n"
    "ds_read_b64_tr_b16 a[248:249], v119 offset:0\n"
    "ds_read_b64_tr_b16 a[250:251], v119 offset:16384\n"
    "v_mfma_f32_16x16x32_bf16 a[68:71], a[236:239], a[20:23], a[68:71]\n"
    "v_permlane32_swap_b32 v149, v151\n"
    "v_mfma_f32_16x16x32_bf16 a[72:75], a[236:239], a[24:27], a[72:75]\n"
    "ds_read_b128 a[0:3], v124 offset:24576\n"
    "v_cvt_pk_bf16_f32 v212, v228, v229\n"
    "v_mfma_f32_16x16x32_bf16 a[76:79], a[236:239], a[28:31], a[76:79]\n"
    "s_waitcnt lgkmcnt(5)\n"
    "v_cvt_pk_bf16_f32 v213, v230, v231\n"
    "v_mfma_f32_16x16x32_bf16 a[80:83], a[240:243], a[16:19], a[80:83]\n"
    "ds_read_b64_tr_b16 a[252:253], v116 offset:1024\n"
    "ds_read_b64_tr_b16 a[254:255], v116 offset:17408\n"
    "v_mfma_f32_16x16x32_bf16 a[84:87], a[240:243], a[20:23], a[84:87]\n"
    "v_cvt_pk_bf16_f32 v214, v236, v237\n"
    "v_cvt_pk_bf16_f32 v215, v238, v239\n"
    "v_mfma_f32_16x16x32_bf16 a[88:91], a[240:243], a[24:27], a[88:91]\n"
    "ds_read_b128 a[4:7], v125 offset:25600\n"
    "v_mfma_f32_16x16x32_bf16 a[92:95], a[240:243], a[28:31], a[92:95]\n"
    "s_waitcnt lgkmcnt(6)\n"
    "v_permlane32_swap_b32 v208, v210\n"
    "v_mfma_f32_16x16x32_bf16 a[96:99], a[244:247], a[16:19], a[96:99]\n"
    "ds_read_b64_tr_b16 a[224:225], v117 offset:1024\n"
    "ds_read_b64_tr_b16 a[226:227], v117 offset:17408\n"
    "v_mfma_f32_16x16x32_bf16 a[100:103], a[244:247], a[20:23], a[100:103]\n"
    "v_permlane32_swap_b32 v209, v211\n"
    "v_mfma_f32_16x16x32_bf16 a[104:107], a[244:247], a[24:27], a[104:107]\n"
    "ds_read_b128 a[8:11], v124 offset:26624\n"
    "v_mfma_f32_16x16x32_bf16 a[108:111], a[244:247], a[28:31], a[108:111]\n"
    "s_waitcnt lgkmcnt(7)\n"
    "v_cvt_pk_bf16_f32 v152, v176, v177\n"
    "v_cvt_pk_bf16_f32 v153, v178, v179\n"
    "v_mfma_f32_16x16x32_bf16 a[112:115], a[248:251], a[16:19], a[112:115]\n"
    "ds_read_b64_tr_b16 a[228:229], v118 offset:1024\n"
    "ds_read_b64_tr_b16 a[230:231], v118 offset:17408\n"
    "v_mfma_f32_16x16x32_bf16 a[116:119], a[248:251], a[20:23], a[116:119]\n"
    "v_cvt_pk_bf16_f32 v154, v184, v185\n"
    "v_mfma_f32_16x16x32_bf16 a[120:123], a[248:251], a[24:27], a[120:123]\n"
    "ds_read_b128 a[12:15], v125 offset:27648\n"
    "v_cvt_pk_bf16_f32 v155, v186, v187\n"
    "v_mfma_f32_16x16x32_bf16 a[124:127], a[248:251], a[28:31], a[124:127]\n"
    "s_waitcnt vmcnt(0) lgkmcnt(7)\n"
    "s_barrier\n"
    "v_mfma_f32_16x16x32_bf16 a[128:131], a[252:255], a[16:19], a[128:131]\n"
    "s_add_u32 m0, s74, 0\n"
    "ds_read_b64_tr_b16 a[232:233], v116 offset:32768\n"
    "ds_read_b64_tr_b16 a[234:235], v116 offset:49152\n"
    "v_mfma_f32_16x16x32_bf16 a[132:135], a[252:255], a[20:23], a[132:135]\n"
    "buffer_load_dwordx4 v126, s[52:55], s77 offen offset:0 lds\n"
    "v_mfma_f32_16x16x32_bf16 a[136:139], a[252:255], a[24:27], a[136:139]\n"
    "v_permlane32_swap_b32 v212, v214\n"
    "v_mfma_f32_16x16x32_bf16 a[140:143], a[252:255], a[28:31], a[140:143]\n"
    "s_waitcnt lgkmcnt(6)\n"
    "v_mfma_f32_16x16x32_bf16 a[144:147], a[224:227], a[16:19], a[144:147]\n"
    "ds_read_b64_tr_b16 a[236:237], v117 offset:32768\n"
    "ds_read_b64_tr_b16 a[238:239], v117 offset:49152\n"
    "v_mfma_f32_16x16x32_bf16 a[148:151], a[224:227], a[20:23], a[148:151]\n"
    "buffer_load_dwordx4 v126, s[52:55], s78 offen offset:1056 lds\n"
    "v_mfma_f32_16x16x32_bf16 a[152:155], a[224:227], a[24:27], a[152:155]\n"
    "v_permlane32_swap_b32 v213, v215\n"
    "v_mfma_f32_16x16x32_bf16 a[156:159], a[224:227], a[28:31], a[156:159]\n"
    "s_waitcnt lgkmcnt(5)\n"
    "v_cvt_pk_bf16_f32 v156, v180, v181\n"
    "v_cvt_pk_bf16_f32 v157, v182, v183\n"
    "v_mfma_f32_16x16x32_bf16 a[160:163], a[228:231], a[16:19], a[160:163]\n"
    "ds_read_b64_tr_b16 a[240:241], v118 offset:32768\n"
    "ds_read_b64_tr_b16 a[242:243], v118 offset:49152\n"
    "v_mfma_f32_16x16x32_bf16 a[164:167], a[228:231], a[20:23], a[164:167]\n"
    "buffer_load_dwordx4 v126, s[52:55], s79 offen offset:2112 lds\n"
    "v_mfma_f32_16x16x32_bf16 a[168:171], a[228:231], a[24:27], a[168:171]\n"
    "v_cvt_pk_bf16_f32 v158, v188, v189\n"
    "v_mfma_f32_16x16x32_bf16 a[172:175], a[228:231], a[28:31], a[172:175]\n"
    "s_waitcnt lgkmcnt(4)\n"
    "v_cvt_pk_bf16_f32 v159, v190, v191\n"
    "v_mfma_f32_16x16x32_bf16 a[64:67], a[232:235], a[0:3], a[64:67]\n"
    "ds_read_b64_tr_b16 a[244:245], v119 offset:32768\n"
    "ds_read_b64_tr_b16 a[246:247], v119 offset:49152\n"
    "v_mfma_f32_16x16x32_bf16 a[68:71], a[232:235], a[4:7], a[68:71]\n"
    "buffer_load_dwordx4 v126, s[52:55], s80 offen offset:3168 lds\n"
    "v_mfma_f32_16x16x32_bf16 a[72:75], a[232:235], a[8:11], a[72:75]\n"
    "v_permlane32_swap_b32 v152, v154\n"
    "v_mfma_f32_16x16x32_bf16 a[76:79], a[232:235], a[12:15], a[76:79]\n"
    "s_waitcnt lgkmcnt(4)\n"
    "v_permlane32_swap_b32 v153, v155\n"
    "v_mfma_f32_16x16x32_bf16 a[80:83], a[236:239], a[0:3], a[80:83]\n"
    "s_add_u32 m0, s74, 4224\n"
    "ds_read_b64_tr_b16 a[248:249], v116 offset:33792\n"
    "ds_read_b64_tr_b16 a[250:251], v116 offset:50176\n"
    "v_mfma_f32_16x16x32_bf16 a[84:87], a[236:239], a[4:7], a[84:87]\n"
    "buffer_load_dwordx4 v126, s[52:55], s81 offen offset:0 lds\n"
    "v_mfma_f32_16x16x32_bf16 a[88:91], a[236:239], a[8:11], a[88:91]\n"
    "v_cvt_pk_bf16_f32 v216, v240, v241\n"
    "v_mfma_f32_16x16x32_bf16 a[92:95], a[236:239], a[12:15], a[92:95]\n"
    "s_waitcnt lgkmcnt(4)\n"
    "v_cvt_pk_bf16_f32 v217, v242, v243\n"
    "v_cvt_pk_bf16_f32 v218, v248, v249\n"
    "v_mfma_f32_16x16x32_bf16 a[96:99], a[240:243], a[0:3], a[96:99]\n"
    "ds_read_b64_tr_b16 a[252:253], v117 offset:33792\n"
    "ds_read_b64_tr_b16 a[254:255], v117 offset:50176\n"
    "v_mfma_f32_16x16x32_bf16 a[100:103], a[240:243], a[4:7], a[100:103]\n"
    "buffer_load_dwordx4 v126, s[52:55], s82 offen offset:1056 lds\n"
    "v_mfma_f32_16x16x32_bf16 a[104:107], a[240:243], a[8:11], a[104:107]\n"
    "v_cvt_pk_bf16_f32 v219, v250, v251\n"
    "v_mfma_f32_16x16x32_bf16 a[108:111], a[240:243], a[12:15], a[108:111]\n"
    "s_waitcnt lgkmcnt(4)\n"
    "v_permlane32_swap_b32 v156, v158\n"
    "v_mfma_f32_16x16x32_bf16 a[112:115], a[244:247], a[0:3], a[112:115]\n"
    "ds_read_b64_tr_b16 a[224:225], v118 offset:33792\n"
    "ds_read_b64_tr_b16 a[226:227], v118 offset:50176\n"
    "v_mfma_f32_16x16x32_bf16 a[116:119], a[244:247], a[4:7], a[116:119]\n"
    "buffer_load_dwordx4 v126, s[52:55], s83 offen offset:2112 lds\n"
    "v_mfma_f32_16x16x32_bf16 a[120:123], a[244:247], a[8:11], a[120:123]\n"
    "v_permlane32_swap_b32 v157, v159\n"
    "v_mfma_f32_16x16x32_bf16 a[124:127], a[244:247], a[12:15], a[124:127]\n"
    "s_waitcnt lgkmcnt(4)\n"
    "v_cvt_pk_bf16_f32 v220, v244, v245\n"
    "v_mfma_f32_16x16x32_bf16 a[128:131], a[248:251], a[0:3], a[128:131]\n"
    "v_cvt_pk_bf16_f32 v221, v246, v247\n"
    "v_mfma_f32_16x16x32_bf16 a[132:135], a[248:251], a[4:7], a[132:135]\n"
    "buffer_load_dwordx4 v126, s[52:55], s84 offen offset:3168 lds\n"
    "v_mfma_f32_16x16x32_bf16 a[136:139], a[248:251], a[8:11], a[136:139]\n"
    "s_add_u32 s52, s52, 32768\n"
    "s_addc_u32 s53, s53, 0\n"
    "v_cvt_pk_bf16_f32 v222, v252, v253\n"
    "v_mfma_f32_16x16x32_bf16 a[140:143], a[248:251], a[12:15], a[140:143]\n"
    "s_waitcnt lgkmcnt(2)\n"
    "s_sub_u32 s54, s54, 32768\n"
    "v_cvt_pk_bf16_f32 v223, v254, v255\n"
    "v_mfma_f32_16x16x32_bf16 a[144:147], a[252:255], a[0:3], a[144:147]\n"
    "v_permlane32_swap_b32 v216, v218\n"
    "v_mfma_f32_16x16x32_bf16 a[148:151], a[252:255], a[4:7], a[148:151]\n"
    "v_mfma_f32_16x16x32_bf16 a[152:155], a[252:255], a[8:11], a[152:155]\n"
    "v_permlane32_swap_b32 v217, v219\n"
    "v_mfma_f32_16x16x32_bf16 a[156:159], a[252:255], a[12:15], a[156:159]\n"
    "s_waitcnt lgkmcnt(0)\n"
    "s_nop 1\n"
    "v_mfma_f32_16x16x32_bf16 a[160:163], a[224:227], a[0:3], a[160:163]\n"
    "v_permlane32_swap_b32 v220, v222\n"
    "v_mfma_f32_16x16x32_bf16 a[164:167], a[224:227], a[4:7], a[164:167]\n"
    "v_mfma_f32_16x16x32_bf16 a[168:171], a[224:227], a[8:11], a[168:171]\n"
    "v_permlane32_swap_b32 v221, v223\n"
    "v_mfma_f32_16x16x32_bf16 a[172:175], a[224:227], a[12:15], a[172:175]\n"
    "v_mbcnt_lo_u32_b32 v127, -1, 0\n"
    "v_mbcnt_hi_u32_b32 v127, -1, v127\n"
    "v_and_b32 v124, 15, v127\n"
    "v_mul_u32_u24 v124, 1056, v124\n"
    "v_lshrrev_b32 v120, 4, v127\n"
    "v_lshl_add_u32 v124, v120, 4, v124\n"
    "s_lshl_b32 s86, s73, 9\n"
    "v_add_u32 v124, s86, v124\n"
    "s_lshl_b32 s86, s73, 8\n"
    "v_subrev_u32 v119, s86, v124\n"
    "v_add_u32 v124, s86, v124\n"
    "v_mbcnt_lo_u32_b32 v127, -1, 0\n"
    "v_mbcnt_hi_u32_b32 v127, -1, v127\n"
    "v_lshrrev_b32 v125, 2, v127\n"
    "v_mul_u32_u24 v125, 1056, v125\n"
    "v_and_b32 v127, 3, v127\n"
    "v_lshl_add_u32 v125, v127, 3, v125\n"
    "s_mul_i32 s89, s73, 512\n"
    "s_add_u32 s89, s89, 67584\n"
    "v_add_u32 v125, s89, v125\n"
    "v_mbcnt_lo_u32_b32 v127, -1, 0\n"
    "v_mbcnt_hi_u32_b32 v127, -1, v127\n"
    "s_and_b32 s86, s71, 15\n"
    "s_lshl_b32 s86, s86, 2\n"
    "s_add_u32 s86, s86, s69\n"
    "s_lshl_b32 s86, s86, 9\n"
    "s_add_u32 s86, s86, 101472\n"
    "v_lshl_add_u32 v116, v127, 7, s86\n"
    "v_mov_b32 v120, 0x80000000\n"
    "v_cmp_gt_u32 vcc, 4, v127\n"
    "s_nop 1\n"
    "v_cndmask_b32 v116, v120, v116, vcc\n"
    "s_barrier\n"
    "s_add_u32 m0, s74, 33792\n"
    "s_nop 0\n"
    "buffer_load_dwordx4 v126, s[52:55], s77 offen offset:0 lds\n"
    "buffer_load_dwordx4 v126, s[52:55], s78 offen offset:1056 lds\n"
    "buffer_load_dwordx4 v126, s[52:55], s79 offen offset:2112 lds\n"
    "buffer_load_dwordx4 v126, s[52:55], s80 offen offset:3168 lds\n"
    "s_add_u32 m0, s74, 38016\n"
    "s_nop 0\n"
    "buffer_load_dwordx4 v126, s[52:55], s81 offen offset:0 lds\n"
    "buffer_load_dwordx4 v126, s[52:55], s82 offen offset:1056 lds\n"
    "buffer_load_dwordx4 v126, s[52:55], s83 offen offset:2112 lds\n"
    "buffer_load_dwordx4 v126, s[52:55], s84 offen offset:3168 lds\n"
    "s_add_u32 s52, s52, 32768\n"
    "s_addc_u32 s53, s53, 0\n"
    "s_sub_u32 s54, s54, 32768\n"
    "v_mbcnt_lo_u32_b32 v127, -1, 0\n"
    "v_mbcnt_hi_u32_b32 v127, -1, v127\n"
    "s_lshl_b32 s86, s69, 14\n"
    "s_add_u32 s86, s86, 67584\n"
    "v_lshl_add_u32 v120, v127, 4, s86\n"
    "ds_write_b128 v120, v[128:131] offset:0\n"
    "ds_write_b128 v120, v[136:139] offset:1024\n"
    "ds_write_b128 v120, v[144:147] offset:2048\n"
    "ds_write_b128 v120, v[152:155] offset:3072\n"
    "ds_write_b128 v120, v[132:135] offset:4096\n"
    "ds_write_b128 v120, v[140:143] offset:5120\n"
    "ds_write_b128 v120, v[148:151] offset:6144\n"
    "ds_write_b128 v120, v[156:159] offset:7168\n"
    "ds_write_b128 v120, v[192:195] offset:8192\n"
    "ds_write_b128 v120, v[200:203] offset:9216\n"
    "ds_write_b128 v120, v[208:211] offset:10240\n"
    "ds_write_b128 v120, v[216:219] offset:11264\n"
    "ds_write_b128 v120, v[196:199] offset:12288\n"
    "ds_write_b128 v120, v[204:207] offset:13312\n"
    "ds_write_b128 v120, v[212:215] offset:14336\n"
    "ds_write_b128 v120, v[220:223] offset:15360\n"
    "v_mbcnt_lo_u32_b32 v121, -1, 0\n"
    "v_mbcnt_hi_u32_b32 v121, -1, v121\n"
    "s_mul_i32 s86, s69, 7168\n"
    "v_lshl_add_u32 v121, v121, 4, s86\n"
    "s_lshl_b32 s86, s71, 3\n"
    "s_add_u32 s86, s86, s70\n"
    "s_mul_i32 s86, s86, 57344\n"
    "s_add_u32 s64, s36, s86\n"
    "s_addc_u32 s65, s37, 0\n"
    "s_add_u32 s64, s64, 8192\n"
    "s_addc_u32 s65, s65, 0\n"
    "s_and_b32 s65, s65, 0xffff\n"
    "s_mov_b32 s66, 57344\n"
    "s_mov_b32 s67, 0x20000\n"
    "v_accvgpr_read_b32 v32, a64\n"
    "v_accvgpr_read_b32 v33, a65\n"
    "v_accvgpr_read_b32 v34, a66\n"
    "v_accvgpr_read_b32 v35, a67\n"
    "v_accvgpr_read_b32 v36, a68\n"
    "v_accvgpr_read_b32 v37, a69\n"
    "v_accvgpr_read_b32 v38, a70\n"
    "v_accvgpr_read_b32 v39, a71\n"
    "v_cvt_pk_bf16_f32 v32, v32, v33\n"
    "v_cvt_pk_bf16_f32 v33, v34, v35\n"
    "v_cvt_pk_bf16_f32 v34, v36, v37\n"
    "v_cvt_pk_bf16_f32 v35, v38, v39\n"
    "s_mov_b32 s101, 0\n"
    "buffer_store_dwordx4 v[32:35], v121, s[64:67], s101 offen offset:0 sc1\n"
    "v_accvgpr_read_b32 v40, a72\n"
    "v_accvgpr_read_b32 v41, a73\n"
    "v_accvgpr_read_b32 v42, a74\n"
    "v_accvgpr_read_b32 v43, a75\n"
    "v_accvgpr_read_b32 v44, a76\n"
    "v_accvgpr_read_b32 v45, a77\n"
    "v_accvgpr_read_b32 v46, a78\n"
    "v_accvgpr_read_b32 v47, a79\n"
    "v_cvt_pk_bf16_f32 v40, v40, v41\n"
    "v_cvt_pk_bf16_f32 v41, v42, v43\n"
    "v_cvt_pk_bf16_f32 v42, v44, v45\n"
    "v_cvt_pk_bf16_f32 v43, v46, v47\n"
    "s_mov_b32 s101, 28672\n"
    "buffer_store_dwordx4 v[40:43], v121, s[64:67], s101 offen offset:0 sc1\n"
    "v_accvgpr_read_b32 v48, a80\n"
    "v_accvgpr_read_b32 v49, a81\n"
    "v_accvgpr_read_b32 v50, a82\n"
    "v_accvgpr_read_b32 v51, a83\n"
    "v_accvgpr_read_b32 v52, a84\n"
    "v_accvgpr_read_b32 v53, a85\n"
    "v_accvgpr_read_b32 v54, a86\n"
    "v_accvgpr_read_b32 v55, a87\n"
    "v_cvt_pk_bf16_f32 v48, v48, v49\n"
    "v_cvt_pk_bf16_f32 v49, v50, v51\n"
    "v_cvt_pk_bf16_f32 v50, v52, v53\n"
    "v_cvt_pk_bf16_f32 v51, v54, v55\n"
    "s_mov_b32 s101, 0\n"
    "buffer_store_dwordx4 v[48:51], v121, s[64:67], s101 offen offset:1024 sc1\n"
    "v_accvgpr_read_b32 v56, a88\n"
    "v_accvgpr_read_b32 v57, a89\n"
    "v_accvgpr_read_b32 v58, a90\n"
    "v_accvgpr_read_b32 v59, a91\n"
    "v_accvgpr_read_b32 v60, a92\n"
    "v_accvgpr_read_b32 v61, a93\n"
    "v_accvgpr_read_b32 v62, a94\n"
    "v_accvgpr_read_b32 v63, a95\n"
    "v_cvt_pk_bf16_f32 v56, v56, v57\n"
    "v_cvt_pk_bf16_f32 v57, v58, v59\n"
    "v_cvt_pk_bf16_f32 v58, v60, v61\n"
    "v_cvt_pk_bf16_f32 v59, v62, v63\n"
    "s_mov_b32 s101, 28672\n"
    "buffer_store_dwordx4 v[56:59], v121, s[64:67], s101 offen offset:1024 sc1\n"
    "v_accvgpr_read_b32 v32, a96\n"
    "v_accvgpr_read_b32 v33, a97\n"
    "v_accvgpr_read_b32 v34, a98\n"
    "v_accvgpr_read_b32 v35, a99\n"
    "v_accvgpr_read_b32 v36, a100\n"
    "v_accvgpr_read_b32 v37, a101\n"
    "v_accvgpr_read_b32 v38, a102\n"
    "v_accvgpr_read_b32 v39, a103\n"
    "v_cvt_pk_bf16_f32 v32, v32, v33\n"
    "v_cvt_pk_bf16_f32 v33, v34, v35\n"
    "v_cvt_pk_bf16_f32 v34, v36, v37\n"
    "v_cvt_pk_bf16_f32 v35, v38, v39\n"
    "s_mov_b32 s101, 0\n"
    "buffer_store_dwordx4 v[32:35], v121, s[64:67], s101 offen offset:2048 sc1\n"
    "v_accvgpr_read_b32 v40, a104\n"
    "v_accvgpr_read_b32 v41, a105\n"
    "v_accvgpr_read_b32 v42, a106\n"
    "v_accvgpr_read_b32 v43, a107\n"
    "v_accvgpr_read_b32 v44, a108\n"
    "v_accvgpr_read_b32 v45, a109\n"
    "v_accvgpr_read_b32 v46, a110\n"
    "v_accvgpr_read_b32 v47, a111\n"
    "v_cvt_pk_bf16_f32 v40, v40, v41\n"
    "v_cvt_pk_bf16_f32 v41, v42, v43\n"
    "v_cvt_pk_bf16_f32 v42, v44, v45\n"
    "v_cvt_pk_bf16_f32 v43, v46, v47\n"
    "s_mov_b32 s101, 28672\n"
    "buffer_store_dwordx4 v[40:43], v121, s[64:67], s101 offen offset:2048 sc1\n"
    "v_accvgpr_read_b32 v48, a112\n"
    "v_accvgpr_read_b32 v49, a113\n"
    "v_accvgpr_read_b32 v50, a114\n"
    "v_accvgpr_read_b32 v51, a115\n"
    "v_accvgpr_read_b32 v52, a116\n"
    "v_accvgpr_read_b32 v53, a117\n"
    "v_accvgpr_read_b32 v54, a118\n"
    "v_accvgpr_read_b32 v55, a119\n"
    "v_cvt_pk_bf16_f32 v48, v48, v49\n"
    "v_cvt_pk_bf16_f32 v49, v50, v51\n"
    "v_cvt_pk_bf16_f32 v50, v52, v53\n"
    "v_cvt_pk_bf16_f32 v51, v54, v55\n"
    "s_mov_b32 s101, 0\n"
    "buffer_store_dwordx4 v[48:51], v121, s[64:67], s101 offen offset:3072 sc1\n"
    "v_accvgpr_read_b32 v56, a120\n"
    "v_accvgpr_read_b32 v57, a121\n"
    "v_accvgpr_read_b32 v58, a122\n"
    "v_accvgpr_read_b32 v59, a123\n"
    "v_accvgpr_read_b32 v60, a124\n"
    "v_accvgpr_read_b32 v61, a125\n"
    "v_accvgpr_read_b32 v62, a126\n"
    "v_accvgpr_read_b32 v63, a127\n"
    "v_cvt_pk_bf16_f32 v56, v56, v57\n"
    "v_cvt_pk_bf16_f32 v57, v58, v59\n"
    "v_cvt_pk_bf16_f32 v58, v60, v61\n"
    "v_cvt_pk_bf16_f32 v59, v62, v63\n"
    "s_mov_b32 s101, 28672\n"
    "buffer_store_dwordx4 v[56:59], v121, s[64:67], s101 offen offset:3072 sc1\n"
    "v_accvgpr_read_b32 v32, a128\n"
    "v_accvgpr_read_b32 v33, a129\n"
    "v_accvgpr_read_b32 v34, a130\n"
    "v_accvgpr_read_b32 v35, a131\n"
    "v_accvgpr_read_b32 v36, a132\n"
    "v_accvgpr_read_b32 v37, a133\n"
    "v_accvgpr_read_b32 v38, a134\n"
    "v_accvgpr_read_b32 v39, a135\n"
    "v_cvt_pk_bf16_f32 v32, v32, v33\n"
    "v_cvt_pk_bf16_f32 v33, v34, v35\n"
    "v_cvt_pk_bf16_f32 v34, v36, v37\n"
    "v_cvt_pk_bf16_f32 v35, v38, v39\n"
    "s_mov_b32 s101, 4096\n"
    "buffer_store_dwordx4 v[32:35], v121, s[64:67], s101 offen offset:0 sc1\n"
    "v_accvgpr_read_b32 v40, a136\n"
    "v_accvgpr_read_b32 v41, a137\n"
    "v_accvgpr_read_b32 v42, a138\n"
    "v_accvgpr_read_b32 v43, a139\n"
    "v_accvgpr_read_b32 v44, a140\n"
    "v_accvgpr_read_b32 v45, a141\n"
    "v_accvgpr_read_b32 v46, a142\n"
    "v_accvgpr_read_b32 v47, a143\n"
    "v_cvt_pk_bf16_f32 v40, v40, v41\n"
    "v_cvt_pk_bf16_f32 v41, v42, v43\n"
    "v_cvt_pk_bf16_f32 v42, v44, v45\n"
    "v_cvt_pk_bf16_f32 v43, v46, v47\n"
    "s_mov_b32 s101, 32768\n"
    "buffer_store_dwordx4 v[40:43], v121, s[64:67], s101 offen offset:0 sc1\n"
    "v_accvgpr_read_b32 v48, a144\n"
    "v_accvgpr_read_b32 v49, a145\n"
    "v_accvgpr_read_b32 v50, a146\n"
    "v_accvgpr_read_b32 v51, a147\n"
    "v_accvgpr_read_b32 v52, a148\n"
    "v_accvgpr_read_b32 v53, a149\n"
    "v_accvgpr_read_b32 v54, a150\n"
    "v_accvgpr_read_b32 v55, a151\n"
    "v_cvt_pk_bf16_f32 v48, v48, v49\n"
    "v_cvt_pk_bf16_f32 v49, v50, v51\n"
    "v_cvt_pk_bf16_f32 v50, v52, v53\n"
    "v_cvt_pk_bf16_f32 v51, v54, v55\n"
    "s_mov_b32 s101, 4096\n"
    "buffer_store_dwordx4 v[48:51], v121, s[64:67], s101 offen offset:1024 sc1\n"
    "v_accvgpr_read_b32 v56, a152\n"
    "v_accvgpr_read_b32 v57, a153\n"
    "v_accvgpr_read_b32 v58, a154\n"
    "v_accvgpr_read_b32 v59, a155\n"
    "v_accvgpr_read_b32 v60, a156\n"
    "v_accvgpr_read_b32 v61, a157\n"
    "v_accvgpr_read_b32 v62, a158\n"
    "v_accvgpr_read_b32 v63, a159\n"
    "v_cvt_pk_bf16_f32 v56, v56, v57\n"
    "v_cvt_pk_bf16_f32 v57, v58, v59\n"
    "v_cvt_pk_bf16_f32 v58, v60, v61\n"
    "v_cvt_pk_bf16_f32 v59, v62, v63\n"
    "s_mov_b32 s101, 32768\n"
    "buffer_store_dwordx4 v[56:59], v121, s[64:67], s101 offen offset:1024 sc1\n"
    "v_accvgpr_read_b32 v32, a160\n"
    "v_accvgpr_read_b32 v33, a161\n"
    "v_accvgpr_read_b32 v34, a162\n"
    "v_accvgpr_read_b32 v35, a163\n"
    "v_accvgpr_read_b32 v36, a164\n"
    "v_accvgpr_read_b32 v37, a165\n"
    "v_accvgpr_read_b32 v38, a166\n"
    "v_accvgpr_read_b32 v39, a167\n"
    "v_cvt_pk_bf16_f32 v32, v32, v33\n"
    "v_cvt_pk_bf16_f32 v33, v34, v35\n"
    "v_cvt_pk_bf16_f32 v34, v36, v37\n"
    "v_cvt_pk_bf16_f32 v35, v38, v39\n"
    "s_mov_b32 s101, 4096\n"
    "buffer_store_dwordx4 v[32:35], v121, s[64:67], s101 offen offset:2048 sc1\n"
    "v_accvgpr_read_b32 v40, a168\n"
    "v_accvgpr_read_b32 v41, a169\n"
    "v_accvgpr_read_b32 v42, a170\n"
    "v_accvgpr_read_b32 v43, a171\n"
    "v_accvgpr_read_b32 v44, a172\n"
    "v_accvgpr_read_b32 v45, a173\n"
    "v_accvgpr_read_b32 v46, a174\n"
    "v_accvgpr_read_b32 v47, a175\n"
    "v_cvt_pk_bf16_f32 v40, v40, v41\n"
    "v_cvt_pk_bf16_f32 v41, v42, v43\n"
    "v_cvt_pk_bf16_f32 v42, v44, v45\n"
    "v_cvt_pk_bf16_f32 v43, v46, v47\n"
    "s_mov_b32 s101, 32768\n"
    "buffer_store_dwordx4 v[40:43], v121, s[64:67], s101 offen offset:2048 sc1\n"
    "v_mbcnt_lo_u32_b32 v122, -1, 0\n"
    "v_mbcnt_hi_u32_b32 v122, -1, v122\n"
    "v_and_b32 v122, 7, v122\n"
    "v_lshlrev_b32 v122, 2, v122\n"
    "s_lshl_b32 s86, s71, 8\n"
    "v_add_u32 v122, s86, v122\n"
    "global_load_dword v117, v122, s[36:37] sc1\n"
    "s_waitcnt vmcnt(23) lgkmcnt(0)\n"
    "s_barrier\n"
    "v_mbcnt_lo_u32_b32 v127, -1, 0\n"
    "v_mbcnt_hi_u32_b32 v127, -1, v127\n"
    "s_xor_b32 s86, s69, 2\n"
    "s_lshl_b32 s86, s86, 14\n"
    "s_add_u32 s86, s86, 67584\n"
    "v_lshl_add_u32 v120, v127, 4, s86\n"
    "ds_read_b128 v[160:163], v120 offset:8192\n"
    "ds_read_b128 v[164:167], v120 offset:12288\n"
    "ds_read_b128 v[224:227], v120 offset:0\n"
    "ds_read_b128 v[228:231], v120 offset:4096\n"
    "ds_read_b128 v[168:171], v120 offset:9216\n"
    "ds_read_b128 v[172:175], v120 offset:13312\n"
    "ds_read_b128 v[232:235], v120 offset:1024\n"
    "ds_read_b128 v[236:239], v120 offset:5120\n"
    "ds_read_b128 v[176:179], v120 offset:10240\n"
    "ds_read_b128 v[180:183], v120 offset:14336\n"
    "ds_read_b128 v[240:243], v120 offset:2048\n"
    "ds_read_b128 v[244:247], v120 offset:6144\n"
    "ds_read_b128 v[184:187], v120 offset:11264\n"
    "ds_read_b128 v[188:191], v120 offset:15360\n"
    "ds_read_b128 v[248:251], v120 offset:3072\n"
    "ds_read_b128 v[252:255], v120 offset:7168\n"
    "ds_read_b128 v[84:87], v124 offset:0\n"
    "ds_read_b128 v[88:91], v124 offset:64\n"
    "ds_read_b128 v[92:95], v124 offset:128\n"
    "ds_read_b128 v[96:99], v124 offset:192\n"
    "ds_read_b128 v[100:103], v119 offset:256\n"
    "s_waitcnt lgkmcnt(4)\n"
    "v_mfma_f32_16x16x32_bf16 v[0:3], v[84:87], v[128:131], 0\n"
    "ds_read_b128 v[104:107], v119 offset:320\n"
    "v_mfma_f32_16x16x32_bf16 v[8:11], v[84:87], v[132:135], 0\n"
    "v_mfma_f32_16x16x32_bf16 v[16:19], v[84:87], v[192:195], 0\n"
    "v_mfma_f32_16x16x32_bf16 v[24:27], v[84:87], v[196:199], 0\n"
    "s_waitcnt lgkmcnt(4)\n"
    "v_mfma_f32_16x16x32_bf16 v[0:3], v[88:91], v[136:139], v[0:3]\n"
    "ds_read_b128 v[108:111], v119 offset:384\n"
    "v_mfma_f32_16x16x32_bf16 v[8:11], v[88:91], v[140:143], v[8:11]\n"
    "v_mfma_f32_16x16x32_bf16 v[16:19], v[88:91], v[200:203], v[16:19]\n"
    "v_mfma_f32_16x16x32_bf16 v[24:27], v[88:91], v[204:207], v[24:27]\n"
    "s_waitcnt lgkmcnt(3)\n"
    "v_mfma_f32_16x16x32_bf16 v[0:3], v[92:95], v[144:147], v[0:3]\n"
    "ds_read_b128 v[112:115], v119 offset:448\n"
    "v_mfma_f32_16x16x32_bf16 v[8:11], v[92:95], v[148:151], v[8:11]\n"
    "v_mfma_f32_16x16x32_bf16 v[16:19], v[92:95], v[208:211], v[16:19]\n"
    "v_mfma_f32_16x16x32_bf16 v[24:27], v[92:95], v[212:215], v[24:27]\n"
    "v_mfma_f32_16x16x32_bf16 v[0:3], v[96:99], v[152:155], v[0:3]\n"
    "ds_read_b128 v[84:87], v124 offset:16896\n"
    "v_mfma_f32_16x16x32_bf16 v[8:11], v[96:99], v[156:159], v[8:11]\n"
    "v_mfma_f32_16x16x32_bf16 v[16:19], v[96:99], v[216:219], v[16:19]\n"
    "v_mfma_f32_16x16x32_bf16 v[24:27], v[96:99], v[220:223], v[24:27]\n"
    "s_waitcnt lgkmcnt(3)\n"
    "v_mfma_f32_16x16x32_bf16 v[0:3], v[100:103], v[160:163], v[0:3]\n"
    "ds_read_b128 v[88:91], v124 offset:16960\n"
    "v_mfma_f32_16x16x32_bf16 v[8:11], v[100:103], v[164:167], v[8:11]\n"
    "v_mfma_f32_16x16x32_bf16 v[16:19], v[100:103], v[224:227], v[16:19]\n"
    "v_mfma_f32_16x16x32_bf16 v[24:27], v[100:103], v[228:231], v[24:27]\n"
    "v_mfma_f32_16x16x32_bf16 v[0:3], v[104:107], v[168:171], v[0:3]\n"
    "ds_read_b128 v[92:95], v124 offset:17024\n"
    "v_mfma_f32_16x16x32_bf16 v[8:11], v[104:107], v[172:175], v[8:11]\n"
    "v_mfma_f32_16x16x32_bf16 v[16:19], v[104:107], v[232:235], v[16:19]\n"
    "v_mfma_f32_16x16x32_bf16 v[24:27], v[104:107], v[236:239], v[24:27]\n"
    "s_waitcnt lgkmcnt(3)\n"
    "v_mfma_f32_16x16x32_bf16 v[0:3], v[108:111], v[176:179], v[0:3]\n"
    "ds_read_b128 v[96:99], v124 offset:17088\n"
    "v_mfma_f32_16x16x32_bf16 v[8:11], v[108:111], v[180:183], v[8:11]\n"
    "v_mfma_f32_16x16x32_bf16 v[16:19], v[108:111], v[240:243], v[16:19]\n"
    "v_mfma_f32_16x16x32_bf16 v[24:27], v[108:111], v[244:247], v[24:27]\n"
    "v_mfma_f32_16x16x32_bf16 v[0:3], v[112:115], v[184:187], v[0:3]\n"
    "ds_read_b128 v[100:103], v119 offset:17152\n"
    "v_mfma_f32_16x16x32_bf16 v[8:11], v[112:115], v[188:191], v[8:11]\n"
    "v_mfma_f32_16x16x32_bf16 v[16:19], v[112:115], v[248:251], v[16:19]\n"
    "v_mfma_f32_16x16x32_bf16 v[24:27], v[112:115], v[252:255], v[24:27]\n"
    "s_waitcnt lgkmcnt(3)\n"
    "v_mfma_f32_16x16x32_bf16 v[4:7], v[84:87], v[128:131], 0\n"
    "ds_read_b128 v[104:107], v119 offset:17216\n"
    "v_mfma_f32_16x16x32_bf16 v[12:15], v[84:87], v[132:135], 0\n"
    "v_mfma_f32_16x16x32_bf16 v[20:23], v[84:87], v[192:195], 0\n"
    "v_mfma_f32_16x16x32_bf16 v[28:31], v[84:87], v[196:199], 0\n"
    "v_mfma_f32_16x16x32_bf16 v[4:7], v[88:91], v[136:139], v[4:7]\n"
    "ds_read_b128 v[108:111], v119 offset:17280\n"
    "v_mfma_f32_16x16x32_bf16 v[12:15], v[88:91], v[140:143], v[12:15]\n"
    "v_mfma_f32_16x16x32_bf16 v[20:23], v[88:91], v[200:203], v[20:23]\n"
    "v_mfma_f32_16x16x32_bf16 v[28:31], v[88:91], v[204:207], v[28:31]\n"
    "s_waitcnt lgkmcnt(3)\n"
    "v_mfma_f32_16x16x32_bf16 v[4:7], v[92:95], v[144:147], v[4:7]\n"
    "ds_read_b128 v[112:115], v119 offset:17344\n"
    "v_mfma_f32_16x16x32_bf16 v[12:15], v[92:95], v[148:151], v[12:15]\n"
    "v_mfma_f32_16x16x32_bf16 v[20:23], v[92:95], v[208:211], v[20:23]\n"
    "v_mfma_f32_16x16x32_bf16 v[28:31], v[92:95], v[212:215], v[28:31]\n"
    "v_mfma_f32_16x16x32_bf16 v[4:7], v[96:99], v[152:155], v[4:7]\n"
    "v_mfma_f32_16x16x32_bf16 v[12:15], v[96:99], v[156:159], v[12:15]\n"
    "v_mfma_f32_16x16x32_bf16 v[20:23], v[96:99], v[216:219], v[20:23]\n"
    "v_mfma_f32_16x16x32_bf16 v[28:31], v[96:99], v[220:223], v[28:31]\n"
    "s_waitcnt lgkmcnt(2)\n"
    "v_mfma_f32_16x16x32_bf16 v[4:7], v[100:103], v[160:163], v[4:7]\n"
    "v_mfma_f32_16x16x32_bf16 v[12:15], v[100:103], v[164:167], v[12:15]\n"
    "v_mfma_f32_16x16x32_bf16 v[20:23], v[100:103], v[224:227], v[20:23]\n"
    "v_mfma_f32_16x16x32_bf16 v[28:31], v[100:103], v[228:231], v[28:31]\n"
    "v_mfma_f32_16x16x32_bf16 v[4:7], v[104:107], v[168:171], v[4:7]\n"
    "v_mfma_f32_16x16x32_bf16 v[12:15], v[104:107], v[172:175], v[12:15]\n"
    "v_mfma_f32_16x16x32_bf16 v[20:23], v[104:107], v[232:235], v[20:23]\n"
    "v_mfma_f32_16x16x32_bf16 v[28:31], v[104:107], v[236:239], v[28:31]\n"
    "s_waitcnt lgkmcnt(0)\n"
    "v_mfma_f32_16x16x32_bf16 v[4:7], v[108:111], v[176:179], v[4:7]\n"
    "v_mfma_f32_16x16x32_bf16 v[12:15], v[108:111], v[180:183], v[12:15]\n"
    "v_mfma_f32_16x16x32_bf16 v[20:23], v[108:111], v[240:243], v[20:23]\n"
    "v_mfma_f32_16x16x32_bf16 v[28:31], v[108:111], v[244:247], v[28:31]\n"
    "v_mfma_f32_16x16x32_bf16 v[4:7], v[112:115], v[184:187], v[4:7]\n"
    "v_mfma_f32_16x16x32_bf16 v[12:15], v[112:115], v[188:191], v[12:15]\n"
    "v_mfma_f32_16x16x32_bf16 v[20:23], v[112:115], v[248:251], v[20:23]\n"
    "v_mfma_f32_16x16x32_bf16 v[28:31], v[112:115], v[252:255], v[28:31]\n"
    "s_barrier\n"
    "s_add_u32 m0, s74, 67584\n"
    "s_nop 0\n"
    "buffer_load_dwordx4 v126, s[56:59], s77 offen offset:0 lds\n"
    "buffer_load_dwordx4 v126, s[56:59], s78 offen offset:1056 lds\n"
    "buffer_load_dwordx4 v126, s[56:59], s79 offen offset:2112 lds\n"
    "buffer_load_dwordx4 v126, s[56:59], s80 offen offset:3168 lds\n"
    "s_add_u32 m0, s74, 71808\n"
    "s_nop 0\n"
    "buffer_load_dwordx4 v126, s[56:59], s81 offen offset:0 lds\n"
    "buffer_load_dwordx4 v126, s[56:59], s82 offen offset:1056 lds\n"
    "buffer_load_dwordx4 v126, s[56:59], s83 offen offset:2112 lds\n"
    "buffer_load_dwordx4 v126, s[56:59], s84 offen offset:3168 lds\n"
    "s_add_u32 s56, s56, 32768\n"
    "s_addc_u32 s57, s57, 0\n"
    "s_sub_u32 s58, s58, 32768\n"
    "s_nop 7\n"
    "v_max3_f32 v64, v0, v1, v2\n"
    "v_max3_f32 v64, v64, v3, v4\n"
    "v_max3_f32 v64, v64, v5, v6\n"
    "v_max_f32 v64, v64, v7\n"
    "v_max3_f32 v68, v8, v9, v10\n"
    "v_max3_f32 v68, v68, v11, v12\n"
    "v_max3_f32 v68, v68, v13, v14\n"
    "v_max_f32 v68, v68, v15\n"
    "v_max3_f32 v72, v16, v17, v18\n"
    "v_max3_f32 v72, v72, v19, v20\n"
    "v_max3_f32 v72, v72, v21, v22\n"
    "v_max_f32 v72, v72, v23\n"
    "v_max3_f32 v76, v24, v25, v26\n"
    "v_max3_f32 v76, v76, v27, v28\n"
    "v_max3_f32 v76, v76, v29, v30\n"
    "v_max_f32 v76, v76, v31\n"
    "v_mov_b32 v65, v64\n"
    "s_nop 1\n"
    "v_permlane32_swap_b32 v64, v65\n"
    "s_nop 1\n"
    "v_max_f32 v64, v64, v65\n"
    "v_mov_b32 v65, v64\n"
    "s_nop 1\n"
    "v_permlane16_swap_b32 v64, v65\n"
    "s_nop 1\n"
    "v_max_f32 v64, v64, v65\n"
    "v_mov_b32 v69, v68\n"
    "s_nop 1\n"
    "v_permlane32_swap_b32 v68, v69\n"
    "s_nop 1\n"
    "v_max_f32 v68, v68, v69\n"
    "v_mov_b32 v69, v68\n"
    "s_nop 1\n"
    "v_permlane16_swap_b32 v68, v69\n"
    "s_nop 1\n"
    "v_max_f32 v68, v68, v69\n"
    "v_mov_b32 v73, v72\n"
    "s_nop 1\n"
    "v_permlane32_swap_b32 v72, v73\n"
    "s_nop 1\n"
    "v_max_f32 v72, v72, v73\n"
    "v_mov_b32 v73, v72\n"
    "s_nop 1\n"
    "v_permlane16_swap_b32 v72, v73\n"
    "s_nop 1\n"
    "v_max_f32 v72, v72, v73\n"
    "v_mov_b32 v77, v76\n"
    "s_nop 1\n"
    "v_permlane32_swap_b32 v76, v77\n"
    "s_nop 1\n"
    "v_max_f32 v76, v76, v77\n"
    "v_mov_b32 v77, v76\n"
    "s_nop 1\n"
    "v_permlane16_swap_b32 v76, v77\n"
    "s_nop 1\n"
    "v_max_f32 v76, v76, v77\n"
    "v_mbcnt_lo_u32_b32 v127, -1, 0\n"
    "v_mbcnt_hi_u32_b32 v127, -1, v127\n"
    "v_and_b32 v127, 15, v127\n"
    "s_lshl_b32 s86, s69, 8\n"
    "s_add_u32 s86, s86, 135168\n"
    "v_lshl_add_u32 v120, v127, 2, s86\n"
    "ds_write_b32 v120, v64 offset:0\n"
    "ds_write_b32 v120, v68 offset:64\n"
    "ds_write_b32 v120, v72 offset:128\n"
    "ds_write_b32 v120, v76 offset:192\n"
    "s_waitcnt lgkmcnt(0)\n"
    "s_barrier\n"
    "s_xor_b32 s86, s69, 2\n"
    "s_lshl_b32 s86, s86, 8\n"
    "s_add_u32 s86, s86, 135168\n"
    "v_lshl_add_u32 v120, v127, 2, s86\n"
    "ds_read_b32 v65, v120 offset:128\n"
    "ds_read_b32 v69, v120 offset:192\n"
    "ds_read_b32 v73, v120 offset:0\n"
    "ds_read_b32 v77, v120 offset:64\n"
    "s_waitcnt lgkmcnt(3)\n"
    "v_max_f32 v64, v64, v65\n"
    "s_waitcnt lgkmcnt(2)\n"
    "v_max_f32 v68, v68, v69\n"
    "s_waitcnt lgkmcnt(1)\n"
    "v_max_f32 v72, v72, v73\n"
    "s_waitcnt lgkmcnt(0)\n"
    "v_max_f32 v76, v76, v77\n"
    "v_sub_f32 v0, v0, v64\n"
    "v_sub_f32 v1, v1, v64\n"
    "v_sub_f32 v2, v2, v64\n"
    "v_sub_f32 v3, v3, v64\n"
    "v_sub_f32 v4, v4, v64\n"
    "v_sub_f32 v5, v5, v64\n"
    "v_sub_f32 v6, v6, v64\n"
    "v_sub_f32 v7, v7, v64\n"
    "v_sub_f32 v64, 0, v64\n"
    "v_mov_b32 v65, v64\n"
    "v_mov_b32 v66, v64\n"
    "v_mov_b32 v67, v64\n"
    "v_mov_b32 v80, 0\n"
    "v_sub_f32 v8, v8, v68\n"
    "v_sub_f32 v9, v9, v68\n"
    "v_sub_f32 v10, v10, v68\n"
    "v_sub_f32 v11, v11, v68\n"
    "v_sub_f32 v12, v12, v68\n"
    "v_sub_f32 v13, v13, v68\n"
    "v_sub_f32 v14, v14, v68\n"
    "v_sub_f32 v15, v15, v68\n"
    "v_sub_f32 v68, 0, v68\n"
    "v_mov_b32 v69, v68\n"
    "v_mov_b32 v70, v68\n"
    "v_mov_b32 v71, v68\n"
    "v_mov_b32 v81, 0\n"
    "v_sub_f32 v16, v16, v72\n"
    "v_sub_f32 v17, v17, v72\n"
    "v_sub_f32 v18, v18, v72\n"
    "v_sub_f32 v19, v19, v72\n"
    "v_sub_f32 v20, v20, v72\n"
    "v_sub_f32 v21, v21, v72\n"
    "v_sub_f32 v22, v22, v72\n"
    "v_sub_f32 v23, v23, v72\n"
    "v_sub_f32 v72, 0, v72\n"
    "v_mov_b32 v73, v72\n"
    "v_mov_b32 v74, v72\n"
    "v_mov_b32 v75, v72\n"
    "v_mov_b32 v82, 0\n"
    "v_sub_f32 v24, v24, v76\n"
    "v_sub_f32 v25, v25, v76\n"
    "v_sub_f32 v26, v26, v76\n"
    "v_sub_f32 v27, v27, v76\n"
    "v_sub_f32 v28, v28, v76\n"
    "v_sub_f32 v29, v29, v76\n"
    "v_sub_f32 v30, v30, v76\n"
    "v_sub_f32 v31, v31, v76\n"
    "v_sub_f32 v76, 0, v76\n"
    "v_mov_b32 v77, v76\n"
    "v_mov_b32 v78, v76\n"
    "v_mov_b32 v79, v76\n"
    "v_mov_b32 v83, 0\n"
    "v_exp_f32 v0, v0\n"
    "s_nop 0\n"
    "v_add_f32 v80, v80, v0\n"
    "v_exp_f32 v1, v1\n"
    "s_nop 0\n"
    "v_add_f32 v80, v80, v1\n"
    "v_cvt_pk_bf16_f32 v0, v0, v1\n"
    "v_exp_f32 v8, v8\n"
    "s_nop 0\n"
    "v_add_f32 v81, v81, v8\n"
    "v_exp_f32 v9, v9\n"
    "s_nop 0\n"
    "v_exp_f32 v16, v16\n"
    "s_nop 0\n"
    "v_add_f32 v81, v81, v9\n"
    "v_cvt_pk_bf16_f32 v8, v8, v9\n"
    "v_add_f32 v82, v82, v16\n"
    "v_exp_f32 v17, v17\n"
    "s_nop 0\n"
    "v_add_f32 v82, v82, v17\n"
    "v_cvt_pk_bf16_f32 v16, v16, v17\n"
    "v_exp_f32 v24, v24\n"
    "s_nop 0\n"
    "v_add_f32 v83, v83, v24\n"
    "v_exp_f32 v25, v25\n"
    "s_nop 0\n"
    "v_add_f32 v83, v83, v25\n"
    "v_cvt_pk_bf16_f32 v24, v24, v25\n"
    "v_exp_f32 v2, v2\n"
    "s_nop 0\n"
    "v_add_f32 v80, v80, v2\n"
    "v_exp_f32 v3, v3\n"
    "s_nop 0\n"
    "v_exp_f32 v10, v10\n"
    "s_nop 0\n"
    "v_add_f32 v80, v80, v3\n"
    "v_cvt_pk_bf16_f32 v1, v2, v3\n"
    "v_add_f32 v81, v81, v10\n"
    "v_exp_f32 v11, v11\n"
    "s_nop 0\n"
    "v_add_f32 v81, v81, v11\n"
    "v_cvt_pk_bf16_f32 v9, v10, v11\n"
    "v_exp_f32 v18, v18\n"
    "s_nop 0\n"
    "v_add_f32 v82, v82, v18\n"
    "v_exp_f32 v19, v19\n"
    "s_nop 0\n"
    "v_add_f32 v82, v82, v19\n"
    "v_cvt_pk_bf16_f32 v17, v18, v19\n"
    "s_waitcnt vmcnt(0) lgkmcnt(0)\n"
    "s_barrier\n"
    "s_cmp_lg_u32 s69, 0\n"
    "s_cbranch_scc1 .Lnfy\n"
    "v_mbcnt_lo_u32_b32 v120, -1, 0\n"
    "v_mbcnt_hi_u32_b32 v120, -1, v120\n"
    "v_and_b32 v120, 7, v120\n"
    "v_lshlrev_b32 v120, 2, v120\n"
    "s_lshl_b32 s86, s71, 8\n"
    "v_add_u32 v120, s86, v120\n"
    "v_add_u32 v121, 0x9e3779b9, v117\n"
    "s_lshl_b64 exec, 1, s70\n"
    "global_store_dword v120, v121, s[36:37] offset:128 sc1\n"
    "s_mov_b64 exec, -1\n"
    ".Lnfy:\n"
    "ds_read_b128 v[84:87], v124 offset:33792\n"
    "ds_read_b128 v[88:91], v124 offset:33856\n"
    "s_add_u32 m0, s74, 0\n"
    "v_exp_f32 v26, v26\n"
    "s_nop 0\n"
    "buffer_load_dwordx4 v126, s[52:55], s77 offen offset:0 lds\n"
    "v_add_f32 v83, v83, v26\n"
    "ds_read_b128 v[92:95], v124 offset:33920\n"
    "v_exp_f32 v27, v27\n"
    "s_nop 0\n"
    "buffer_load_dwordx4 v126, s[52:55], s78 offen offset:1056 lds\n"
    "v_add_f32 v83, v83, v27\n"
    "v_cvt_pk_bf16_f32 v25, v26, v27\n"
    "ds_read_b128 v[96:99], v124 offset:33984\n"
    "v_exp_f32 v4, v4\n"
    "s_nop 0\n"
    "buffer_load_dwordx4 v126, s[52:55], s79 offen offset:2112 lds\n"
    "v_add_f32 v80, v80, v4\n"
    "ds_read_b128 v[100:103], v119 offset:34048\n"
    "v_exp_f32 v5, v5\n"
    "s_nop 0\n"
    "buffer_load_dwordx4 v126, s[52:55], s80 offen offset:3168 lds\n"
    "s_waitcnt lgkmcnt(3)\n"
    "v_add_f32 v80, v80, v5\n"
    "v_cvt_pk_bf16_f32 v2, v4, v5\n"
    "v_mfma_f32_16x16x32_bf16 v[32:35], v[84:87], v[128:131], v[64:67]\n"
    "ds_read_b128 v[104:107], v119 offset:34112\n"
    "v_mfma_f32_16x16x32_bf16 v[40:43], v[84:87], v[132:135], v[68:71]\n"
    "s_add_u32 m0, s74, 4224\n"
    "v_exp_f32 v12, v12\n"
    "v_mfma_f32_16x16x32_bf16 v[48:51], v[84:87], v[192:195], v[72:75]\n"
    "buffer_load_dwordx4 v126, s[52:55], s81 offen offset:0 lds\n"
    "v_mfma_f32_16x16x32_bf16 v[56:59], v[84:87], v[196:199], v[76:79]\n"
    "v_add_f32 v81, v81, v12\n"
    "v_mfma_f32_16x16x32_bf16 v[32:35], v[88:91], v[136:139], v[32:35]\n"
    "ds_read_b128 v[108:111], v119 offset:34176\n"
    "v_mfma_f32_16x16x32_bf16 v[40:43], v[88:91], v[140:143], v[40:43]\n"
    "v_exp_f32 v13, v13\n"
    "v_mfma_f32_16x16x32_bf16 v[48:51], v[88:91], v[200:203], v[48:51]\n"
    "buffer_load_dwordx4 v126, s[52:55], s82 offen offset:1056 lds\n"
    "v_mfma_f32_16x16x32_bf16 v[56:59], v[88:91], v[204:207], v[56:59]\n"
    "s_waitcnt lgkmcnt(3)\n"
    "v_add_f32 v81, v81, v13\n"
    "v_cvt_pk_bf16_f32 v10, v12, v13\n"
    "v_mfma_f32_16x16x32_bf16 v[32:35], v[92:95], v[144:147], v[32:35]\n"
    "ds_read_b128 v[112:115], v119 offset:34240\n"
    "v_mfma_f32_16x16x32_bf16 v[40:43], v[92:95], v[148:151], v[40:43]\n"
    "v_exp_f32 v20, v20\n"
    "v_mfma_f32_16x16x32_bf16 v[48:51], v[92:95], v[208:211], v[48:51]\n"
    "buffer_load_dwordx4 v126, s[52:55], s83 offen offset:2112 lds\n"
    "v_mfma_f32_16x16x32_bf16 v[56:59], v[92:95], v[212:215], v[56:59]\n"
    "v_add_f32 v82, v82, v20\n"
    "v_mfma_f32_16x16x32_bf16 v[32:35], v[96:99], v[152:155], v[32:35]\n"
    "ds_read_b128 v[84:87], v124 offset:50688\n"
    "v_mfma_f32_16x16x32_bf16 v[40:43], v[96:99], v[156:159], v[40:43]\n"
    "v_exp_f32 v21, v21\n"
    "v_mfma_f32_16x16x32_bf16 v[48:51], v[96:99], v[216:219], v[48:51]\n"
    "buffer_load_dwordx4 v126, s[52:55], s84 offen offset:3168 lds\n"
    "v_mfma_f32_16x16x32_bf16 v[56:59], v[96:99], v[220:223], v[56:59]\n"
    "s_waitcnt lgkmcnt(3)\n"
    "v_add_f32 v82, v82, v21\n"
    "v_cvt_pk_bf16_f32 v18, v20, v21\n"
    "v_mfma_f32_16x16x32_bf16 v[32:35], v[100:103], v[160:163], v[32:35]\n"
    "s_sub_u32 s54, s54, 32768\n"
    "ds_read_b128 v[88:91], v124 offset:50752\n"
    "v_mfma_f32_16x16x32_bf16 v[40:43], v[100:103], v[164:167], v[40:43]\n"
    "s_add_u32 m0, s74, 101376\n"
    "v_exp_f32 v28, v28\n"
    "v_mfma_f32_16x16x32_bf16 v[48:51], v[100:103], v[224:227], v[48:51]\n"
    "buffer_load_dwordx4 v126, s[56:59], s77 offen offset:0 lds\n"
    "v_mfma_f32_16x16x32_bf16 v[56:59], v[100:103], v[228:231], v[56:59]\n"
    "s_add_u32 s52, s52, 32768\n"
    "s_addc_u32 s53, s53, 0\n"
    "v_add_f32 v83, v83, v28\n"
    "v_mfma_f32_16x16x32_bf16 v[32:35], v[104:107], v[168:171], v[32:35]\n"
    "ds_read_b128 v[92:95], v124 offset:50816\n"
    "v_mfma_f32_16x16x32_bf16 v[40:43], v[104:107], v[172:175], v[40:43]\n"
    "v_exp_f32 v29, v29\n"
    "v_mfma_f32_16x16x32_bf16 v[48:51], v[104:107], v[232:235], v[48:51]\n"
    "buffer_load_dwordx4 v126, s[56:59], s78 offen offset:1056 lds\n"
    "v_mfma_f32_16x16x32_bf16 v[56:59], v[104:107], v[236:239], v[56:59]\n"
    "s_waitcnt lgkmcnt(3)\n"
    "v_add_f32 v83, v83, v29\n"
    "v_cvt_pk_bf16_f32 v26, v28, v29\n"
    "v_mfma_f32_16x16x32_bf16 v[32:35], v[108:111], v[176:179], v[32:35]\n"
    "ds_read_b128 v[96:99], v124 offset:50880\n"
    "v_mfma_f32_16x16x32_bf16 v[40:43], v[108:111], v[180:183], v[40:43]\n"
    "v_exp_f32 v6, v6\n"
    "v_mfma_f32_16x16x32_bf16 v[48:51], v[108:111], v[240:243], v[48:51]\n"
    "buffer_load_dwordx4 v126, s[56:59], s79 offen offset:2112 lds\n"
    "v_mfma_f32_16x16x32_bf16 v[56:59], v[108:111], v[244:247], v[56:59]\n"
    "v_add_f32 v80, v80, v6\n"
    "v_mfma_f32_16x16x32_bf16 v[32:35], v[112:115], v[184:187], v[32:35]\n"
    "ds_read_b128 v[100:103], v119 offset:50944\n"
    "v_mfma_f32_16x16x32_bf16 v[40:43], v[112:115], v[188:191], v[40:43]\n"
    "v_exp_f32 v7, v7\n"
    "v_mfma_f32_16x16x32_bf16 v[48:51], v[112:115], v[248:251], v[48:51]\n"
    "buffer_load_dwordx4 v126, s[56:59], s80 offen offset:3168 lds\n"
    "v_mfma_f32_16x16x32_bf16 v[56:59], v[112:115], v[252:255], v[56:59]\n"
    "s_waitcnt lgkmcnt(3)\n"
    "v_add_f32 v80, v80, v7\n"
    "v_cvt_pk_bf16_f32 v3, v6, v7\n"
    "v_mfma_f32_16x16x32_bf16 v[36:39], v[84:87], v[128:131], v[64:67]\n"
    "ds_read_b128 v[104:107], v119 offset:51008\n"
    "v_mfma_f32_16x16x32_bf16 v[44:47], v[84:87], v[132:135], v[68:71]\n"
    "s_add_u32 m0, s74, 105600\n"
    "v_exp_f32 v14, v14\n"
    "v_mfma_f32_16x16x32_bf16 v[52:55], v[84:87], v[192:195], v[72:75]\n"
    "buffer_load_dwordx4 v126, s[56:59], s81 offen offset:0 lds\n"
    "v_mfma_f32_16x16x32_bf16 v[60:63], v[84:87], v[196:199], v[76:79]\n"
    "v_add_f32 v81, v81, v14\n"
    "v_mfma_f32_16x16x32_bf16 v[36:39], v[88:91], v[136:139], v[36:39]\n"
    "ds_read_b128 v[108:111], v119 offset:51072\n"
    "v_mfma_f32_16x16x32_bf16 v[44:47], v[88:91], v[140:143], v[44:47]\n"
    "v_exp_f32 v15, v15\n"
    "v_mfma_f32_16x16x32_bf16 v[52:55], v[88:91], v[200:203], v[52:55]\n"
    "buffer_load_dwordx4 v126, s[56:59], s82 offen offset:1056 lds\n"
    "v_mfma_f32_16x16x32_bf16 v[60:63], v[88:91], v[204:207], v[60:63]\n"
    "s_waitcnt lgkmcnt(3)\n"
    "v_add_f32 v81, v81, v15\n"
    "v_cvt_pk_bf16_f32 v11, v14, v15\n"
    "v_mfma_f32_16x16x32_bf16 v[36:39], v[92:95], v[144:147], v[36:39]\n"
    "ds_read_b128 v[112:115], v119 offset:51136\n"
    "v_mfma_f32_16x16x32_bf16 v[44:47], v[92:95], v[148:151], v[44:47]\n"
    "v_exp_f32 v22, v22\n"
    "v_mfma_f32_16x16x32_bf16 v[52:55], v[92:95], v[208:211], v[52:55]\n"
    "buffer_load_dwordx4 v126, s[56:59], s83 offen offset:2112 lds\n"
    "v_mfma_f32_16x16x32_bf16 v[60:63], v[92:95], v[212:215], v[60:63]\n"
    "v_add_f32 v82, v82, v22\n"
    "v_mfma_f32_16x16x32_bf16 v[36:39], v[96:99], v[152:155], v[36:39]\n"
    "ds_read_b64_tr_b16 v[84:85], v125 offset:0\n"
    "ds_read_b64_tr_b16 v[86:87], v125 offset:16896\n"
    "v_mfma_f32_16x16x32_bf16 v[44:47], v[96:99], v[156:159], v[44:47]\n"
    "v_exp_f32 v23, v23\n"
    "v_mfma_f32_16x16x32_bf16 v[52:55], v[96:99], v[216:219], v[52:55]\n"
    "buffer_load_dwordx4 v126, s[56:59], s84 offen offset:3168 lds\n"
    "v_mfma_f32_16x16x32_bf16 v[60:63], v[96:99], v[220:223], v[60:63]\n"
    "s_waitcnt lgkmcnt(4)\n"
    "v_add_f32 v82, v82, v23\n"
    "v_cvt_pk_bf16_f32 v19, v22, v23\n"
    "v_mfma_f32_16x16x32_bf16 v[36:39], v[100:103], v[160:163], v[36:39]\n"
    "s_sub_u32 s58, s58, 32768\n"
    "ds_read_b64_tr_b16 v[88:89], v125 offset:32\n"
    "ds_read_b64_tr_b16 v[90:91], v125 offset:16928\n"
    "v_mfma_f32_16x16x32_bf16 v[44:47], v[100:103], v[164:167], v[44:47]\n"
    "v_exp_f32 v30, v30\n"
    "v_mfma_f32_16x16x32_bf16 v[52:55], v[100:103], v[224:227], v[52:55]\n"
    "buffer_load_dword v118, v116, s[52:55], 0 offen\n"
    "v_mfma_f32_16x16x32_bf16 v[60:63], v[100:103], v[228:231], v[60:63]\n"
    "s_add_u32 s56, s56, 32768\n"
    "s_addc_u32 s57, s57, 0\n"
    "v_add_f32 v83, v83, v30\n"
    "v_mfma_f32_16x16x32_bf16 v[36:39], v[104:107], v[168:171], v[36:39]\n"
    "ds_read_b64_tr_b16 v[92:93], v125 offset:64\n"
    "ds_read_b64_tr_b16 v[94:95], v125 offset:16960\n"
    "v_mfma_f32_16x16x32_bf16 v[44:47], v[104:107], v[172:175], v[44:47]\n"
    "v_exp_f32 v31, v31\n"
    "v_mfma_f32_16x16x32_bf16 v[52:55], v[104:107], v[232:235], v[52:55]\n"
    "buffer_load_dword v118, v116, s[56:59], 0 offen\n"
    "v_mfma_f32_16x16x32_bf16 v[60:63], v[104:107], v[236:239], v[60:63]\n"
    "s_waitcnt lgkmcnt(6)\n"
    "v_add_f32 v83, v83, v31\n"
    "v_cvt_pk_bf16_f32 v27, v30, v31\n"
    "v_mfma_f32_16x16x32_bf16 v[36:39], v[108:111], v[176:179], v[36:39]\n"
    "ds_read_b64_tr_b16 v[96:97], v125 offset:96\n"
    "ds_read_b64_tr_b16 v[98:99], v125 offset:16992\n"
    "v_mfma_f32_16x16x32_bf16 v[44:47], v[108:111], v[180:183], v[44:47]\n"
    "v_mfma_f32_16x16x32_bf16 v[52:55], v[108:111], v[240:243], v[52:55]\n"
    "v_mfma_f32_16x16x32_bf16 v[60:63], v[108:111], v[244:247], v[60:63]\n"
    "v_mfma_f32_16x16x32_bf16 v[36:39], v[112:115], v[184:187], v[36:39]\n"
    "ds_read_b64_tr_b16 v[100:101], v125 offset:128\n"
    "ds_read_b64_tr_b16 v[102:103], v125 offset:17024\n"
    "v_mfma_f32_16x16x32_bf16 v[44:47], v[112:115], v[188:191], v[44:47]\n"
    "v_mfma_f32_16x16x32_bf16 v[52:55], v[112:115], v[248:251], v[52:55]\n"
    "v_mfma_f32_16x16x32_bf16 v[60:63], v[112:115], v[252:255], v[60:63]\n"
    "s_waitcnt lgkmcnt(6)\n"
    "v_mfma_f32_16x16x32_bf16 a[0:3], v[84:87], v[0:3], 0\n"
    "ds_read_b64_tr_b16 v[104:105], v125 offset:160\n"
    "ds_read_b64_tr_b16 v[106:107], v125 offset:17056\n"
    "v_mfma_f32_16x16x32_bf16 a[4:7], v[84:87], v[8:11], 0\n"
    "v_exp_f32 v32, v32\n"
    "v_mfma_f32_16x16x32_bf16 a[128:131], v[84:87], v[16:19], 0\n"
    "v_add_f32 v80, v80, v32\n"
    "v_mfma_f32_16x16x32_bf16 a[132:135], v[84:87], v[24:27], 0\n"
    "v_mfma_f32_16x16x32_bf16 a[8:11], v[88:91], v[0:3], 0\n"
    "ds_read_b64_tr_b16 v[108:109], v125 offset:192\n"
    "ds_read_b64_tr_b16 v[110:111], v125 offset:17088\n"
    "v_mfma_f32_16x16x32_bf16 a[12:15], v[88:91], v[8:11], 0\n"
    "v_exp_f32 v33, v33\n"
    "v_mfma_f32_16x16x32_bf16 a[136:139], v[88:91], v[16:19], 0\n"
    "v_add_f32 v80, v80, v33\n"
    "v_cvt_pk_bf16_f32 v32, v32, v33\n"
    "v_mfma_f32_16x16x32_bf16 a[140:143], v[88:91], v[24:27], 0\n"
    "s_waitcnt lgkmcnt(6)\n"
    "v_mfma_f32_16x16x32_bf16 a[16:19], v[92:95], v[0:3], 0\n"
    "ds_read_b64_tr_b16 v[112:113], v125 offset:224\n"
    "ds_read_b64_tr_b16 v[114:115], v125 offset:17120\n"
    "v_mfma_f32_16x16x32_bf16 a[20:23], v[92:95], v[8:11], 0\n"
    "v_exp_f32 v40, v40\n"
    "v_mfma_f32_16x16x32_bf16 a[144:147], v[92:95], v[16:19], 0\n"
    "v_add_f32 v81, v81, v40\n"
    "v_mfma_f32_16x16x32_bf16 a[148:151], v[92:95], v[24:27], 0\n"
    "v_mfma_f32_16x16x32_bf16 a[24:27], v[96:99], v[0:3], 0\n"
    "ds_read_b64_tr_b16 v[84:85], v125 offset:256\n"
    "ds_read_b64_tr_b16 v[86:87], v125 offset:17152\n"
    "v_mfma_f32_16x16x32_bf16 a[28:31], v[96:99], v[8:11], 0\n"
    "v_exp_f32 v41, v41\n"
    "v_mfma_f32_16x16x32_bf16 a[152:155], v[96:99], v[16:19], 0\n"
    "v_exp_f32 v48, v48\n"
    "v_mfma_f32_16x16x32_bf16 a[156:159], v[96:99], v[24:27], 0\n"
    "s_waitcnt lgkmcnt(6)\n"
    "v_add_f32 v81, v81, v41\n"
    "v_cvt_pk_bf16_f32 v40, v40, v41\n"
    "v_mfma_f32_16x16x32_bf16 a[32:35], v[100:103], v[0:3], 0\n"
    "ds_read_b64_tr_b16 v[88:89], v125 offset:288\n"
    "ds_read_b64_tr_b16 v[90:91], v125 offset:17184\n"
    "v_mfma_f32_16x16x32_bf16 a[36:39], v[100:103], v[8:11], 0\n"
    "v_exp_f32 v49, v49\n"
    "v_mfma_f32_16x16x32_bf16 a[160:163], v[100:103], v[16:19], 0\n"
    "v_add_f32 v82, v82, v48\n"
    "v_add_f32 v82, v82, v49\n"
    "v_mfma_f32_16x16x32_bf16 a[164:167], v[100:103], v[24:27], 0\n"
    "v_cvt_pk_bf16_f32 v48, v48, v49\n"
    "v_mfma_f32_16x16x32_bf16 a[40:43], v[104:107], v[0:3], 0\n"
    "ds_read_b64_tr_b16 v[92:93], v125 offset:320\n"
    "ds_read_b64_tr_b16 v[94:95], v125 offset:17216\n"
    "v_mfma_f32_16x16x32_bf16 a[44:47], v[104:107], v[8:11], 0\n"
    "v_exp_f32 v56, v56\n"
    "v_mfma_f32_16x16x32_bf16 a[168:171], v[104:107], v[16:19], 0\n"
    "v_add_f32 v83, v83, v56\n"
    "v_mfma_f32_16x16x32_bf16 a[172:175], v[104:107], v[24:27], 0\n"
    "s_waitcnt lgkmcnt(6)\n"
    "v_mfma_f32_16x16x32_bf16 a[48:51], v[108:111], v[0:3], 0\n"
    "ds_read_b64_tr_b16 v[96:97], v125 offset:352\n"
    "ds_read_b64_tr_b16 v[98:99], v125 offset:17248\n"
    "v_mfma_f32_16x16x32_bf16 a[52:55], v[108:111], v[8:11], 0\n"
    "v_exp_f32 v57, v57\n"
    "v_mfma_f32_16x16x32_bf16 a[176:179], v[108:111], v[16:19], 0\n"
    "v_add_f32 v83, v83, v57\n"
    "v_cvt_pk_bf16_f32 v56, v56, v57\n"
    "v_mfma_f32_16x16x32_bf16 a[180:183], v[108:111], v[24:27], 0\n"
    "v_mfma_f32_16x16x32_bf16 a[56:59], v[112:115], v[0:3], 0\n"
    "ds_read_b64_tr_b16 v[100:101], v125 offset:384\n"
    "ds_read_b64_tr_b16 v[102:103], v125 offset:17280\n"
    "v_mfma_f32_16x16x32_bf16 a[60:63], v[112:115], v[8:11], 0\n"
    "v_exp_f32 v34, v34\n"
    "v_mfma_f32_16x16x32_bf16 a[184:187], v[112:115], v[16:19], 0\n"
    "v_add_f32 v80, v80, v34\n"
    "v_mfma_f32_16x16x32_bf16 a[188:191], v[112:115], v[24:27], 0\n"
    "s_waitcnt lgkmcnt(6)\n"
    "v_mfma_f32_16x16x32_bf16 a[64:67], v[84:87], v[0:3], 0\n"
    "ds_read_b64_tr_b16 v[104:105], v125 offset:416\n"
    "ds_read_b64_tr_b16 v[106:107], v125 offset:17312\n"
    "v_mfma_f32_16x16x32_bf16 a[68:71], v[84:87], v[8:11], 0\n"
    "v_exp_f32 v35, v35\n"
    "v_mfma_f32_16x16x32_bf16 a[192:195], v[84:87], v[16:19], 0\n"
    "v_exp_f32 v42, v42\n"
    "v_mfma_f32_16x16x32_bf16 a[196:199], v[84:87], v[24:27], 0\n"
    "v_add_f32 v80, v80, v35\n"
    "v_cvt_pk_bf16_f32 v33, v34, v35\n"
    "v_mfma_f32_16x16x32_bf16 a[72:75], v[88:91], v[0:3], 0\n"
    "ds_read_b64_tr_b16 v[108:109], v125 offset:448\n"
    "ds_read_b64_tr_b16 v[110:111], v125 offset:17344\n"
    "v_mfma_f32_16x16x32_bf16 a[76:79], v[88:91], v[8:11], 0\n"
    "v_exp_f32 v43, v43\n"
    "v_mfma_f32_16x16x32_bf16 a[200:203], v[88:91], v[16:19], 0\n"
    "v_add_f32 v81, v81, v42\n"
    "v_add_f32 v81, v81, v43\n"
    "v_mfma_f32_16x16x32_bf16 a[204:207], v[88:91], v[24:27], 0\n"
    "s_waitcnt lgkmcnt(6)\n"
    "v_cvt_pk_bf16_f32 v41, v42, v43\n"
    "v_mfma_f32_16x16x32_bf16 a[80:83], v[92:95], v[0:3], 0\n"
    "ds_read_b64_tr_b16 v[112:113], v125 offset:480\n"
    "ds_read_b64_tr_b16 v[114:115], v125 offset:17376\n"
    "v_mfma_f32_16x16x32_bf16 a[84:87], v[92:95], v[8:11], 0\n"
    "v_exp_f32 v50, v50\n"
    "v_mfma_f32_16x16x32_bf16 a[208:211], v[92:95], v[16:19], 0\n"
    "v_add_f32 v82, v82, v50\n"
    "v_mfma_f32_16x16x32_bf16 a[212:215], v[92:95], v[24:27], 0\n"
    "v_mfma_f32_16x16x32_bf16 a[88:91], v[96:99], v[0:3], 0\n"
    "v_mfma_f32_16x16x32_bf16 a[92:95], v[96:99], v[8:11], 0\n"
    "v_exp_f32 v51, v51\n"
    "v_mfma_f32_16x16x32_bf16 a[216:219], v[96:99], v[16:19], 0\n"
    "v_add_f32 v82, v82, v51\n"
    "v_cvt_pk_bf16_f32 v49, v50, v51\n"
    "v_mfma_f32_16x16x32_bf16 a[220:223], v[96:99], v[24:27], 0\n"
    "s_waitcnt vmcnt(2) lgkmcnt(0)\n"
    "s_barrier\n"
    "v_mfma_f32_16x16x32_bf16 a[96:99], v[100:103], v[0:3], 0\n"
    "ds_read_b128 v[84:87], v124 offset:0\n"
    "ds_read_b128 v[88:91], v124 offset:64\n"
    "v_mfma_f32_16x16x32_bf16 a[100:103], v[100:103], v[8:11], 0\n"
    "s_add_u32 m0, s74, 33792\n"
    "v_exp_f32 v58, v58\n"
    "v_mfma_f32_16x16x32_bf16 a[224:227], v[100:103], v[16:19], 0\n"
    "buffer_load_dwordx4 v126, s[52:55], s77 offen offset:0 lds\n"
    "v_mfma_f32_16x16x32_bf16 a[228:231], v[100:103], v[24:27], 0\n"
    "v_add_f32 v83, v83, v58\n"
    "v_mfma_f32_16x16x32_bf16 a[104:107], v[104:107], v[0:3], 0\n"
    "ds_read_b128 v[92:95], v124 offset:128\n"
    "v_mfma_f32_16x16x32_bf16 a[108:111], v[104:107], v[8:11], 0\n"
    "v_exp_f32 v59, v59\n"
    "v_mfma_f32_16x16x32_bf16 a[232:235], v[104:107], v[16:19], 0\n"
    "buffer_load_dwordx4 v126, s[52:55], s78 offen offset:1056 lds\n"
    "v_mfma_f32_16x16x32_bf16 a[236:239], v[104:107], v[24:27], 0\n"
    "v_add_f32 v83, v83, v59\n"
    "v_cvt_pk_bf16_f32 v57, v58, v59\n"
    "v_mfma_f32_16x16x32_bf16 a[112:115], v[108:111], v[0:3], 0\n"
    "ds_read_b128 v[96:99], v124 offset:192\n"
    "v_mfma_f32_16x16x32_bf16 a[116:119], v[108:111], v[8:11], 0\n"
    "v_exp_f32 v36, v36\n"
    "v_mfma_f32_16x16x32_bf16 a[240:243], v[108:111], v[16:19], 0\n"
    "buffer_load_dwordx4 v126, s[52:55], s79 offen offset:2112 lds\n"
    "v_mfma_f32_16x16x32_bf16 a[244:247], v[108:111], v[24:27], 0\n"
    "v_add_f32 v80, v80, v36\n"
    "v_mfma_f32_16x16x32_bf16 a[120:123], v[112:115], v[0:3], 0\n"
    "ds_read_b128 v[100:103], v119 offset:256\n"
    "v_mfma_f32_16x16x32_bf16 a[124:127], v[112:115], v[8:11], 0\n"
    "v_exp_f32 v37, v37\n"
    "v_mfma_f32_16x16x32_bf16 a[248:251], v[112:115], v[16:19], 0\n"
    "buffer_load_dwordx4 v126, s[52:55], s80 offen offset:3168 lds\n"
    "v_mfma_f32_16x16x32_bf16 a[252:255], v[112:115], v[24:27], 0\n"
    "s_waitcnt lgkmcnt(3)\n"
    "v_add_f32 v80, v80, v37\n"
    "v_cvt_pk_bf16_f32 v34, v36, v37\n"
    "s_mov_b32 s85, 14\n"
    ".Lloop:\n"
    "v_mfma_f32_16x16x32_bf16 v[0:3], v[84:87], v[128:131], v[64:67]\n"
    "ds_read_b128 v[104:107], v119 offset:320\n"
    "v_mfma_f32_16x16x32_bf16 v[8:11], v[84:87], v[132:135], v[68:71]\n"
    "s_add_u32 m0, s74, 38016\n"
    "v_exp_f32 v44, v44\n"
    "v_mfma_f32_16x16x32_bf16 v[16:19], v[84:87], v[192:195], v[72:75]\n"
    "buffer_load_dwordx4 v126, s[52:55], s81 offen offset:0 lds\n"
    "v_mfma_f32_16x16x32_bf16 v[24:27], v[84:87], v[196:199], v[76:79]\n"
    "v_add_f32 v81, v81, v44\n"
    "v_mfma_f32_16x16x32_bf16 v[0:3], v[88:91], v[136:139], v[0:3]\n"
    "ds_read_b128 v[108:111], v119 offset:384\n"
    "v_mfma_f32_16x16x32_bf16 v[8:11], v[88:91], v[140:143], v[8:11]\n"
    "v_exp_f32 v45, v45\n"
    "v_mfma_f32_16x16x32_bf16 v[16:19], v[88:91], v[200:203], v[16:19]\n"
    "buffer_load_dwordx4 v126, s[52:55], s82 offen offset:1056 lds\n"
    "v_mfma_f32_16x16x32_bf16 v[24:27], v[88:91], v[204:207], v[24:27]\n"
    "s_waitcnt lgkmcnt(3)\n"
    "v_add_f32 v81, v81, v45\n"
    "v_cvt_pk_bf16_f32 v42, v44, v45\n"
    "v_mfma_f32_16x16x32_bf16 v[0:3], v[92:95], v[144:147], v[0:3]\n"
    "ds_read_b128 v[112:115], v119 offset:448\n"
    "v_mfma_f32_16x16x32_bf16 v[8:11], v[92:95], v[148:151], v[8:11]\n"
    "v_exp_f32 v52, v52\n"
    "v_mfma_f32_16x16x32_bf16 v[16:19], v[92:95], v[208:211], v[16:19]\n"
    "buffer_load_dwordx4 v126, s[52:55], s83 offen offset:2112 lds\n"
    "v_mfma_f32_16x16x32_bf16 v[24:27], v[92:95], v[212:215], v[24:27]\n"
    "v_add_f32 v82, v82, v52\n"
    "v_mfma_f32_16x16x32_bf16 v[0:3], v[96:99], v[152:155], v[0:3]\n"
    "ds_read_b128 v[84:87], v124 offset:16896\n"
    "v_mfma_f32_16x16x32_bf16 v[8:11], v[96:99], v[156:159], v[8:11]\n"
    "v_exp_f32 v53, v53\n"
    "v_mfma_f32_16x16x32_bf16 v[16:19], v[96:99], v[216:219], v[16:19]\n"
    "buffer_load_dwordx4 v126, s[52:55], s84 offen offset:3168 lds\n"
    "v_mfma_f32_16x16x32_bf16 v[24:27], v[96:99], v[220:223], v[24:27]\n"
    "s_waitcnt lgkmcnt(3)\n"
    "v_add_f32 v82, v82, v53\n"
    "v_cvt_pk_bf16_f32 v50, v52, v53\n"
    "v_mfma_f32_16x16x32_bf16 v[0:3], v[100:103], v[160:163], v[0:3]\n"
    "s_sub_u32 s54, s54, 32768\n"
    "ds_read_b128 v[88:91], v124 offset:16960\n"
    "v_mfma_f32_16x16x32_bf16 v[8:11], v[100:103], v[164:167], v[8:11]\n"
    "s_add_u32 m0, s74, 67584\n"
    "v_exp_f32 v60, v60\n"
    "v_mfma_f32_16x16x32_bf16 v[16:19], v[100:103], v[224:227], v[16:19]\n"
    "buffer_load_dwordx4 v126, s[56:59], s77 offen offset:0 lds\n"
    "v_mfma_f32_16x16x32_bf16 v[24:27], v[100:103], v[228:231], v[24:27]\n"
    "s_add_u32 s52, s52, 32768\n"
    "s_addc_u32 s53, s53, 0\n"
    "v_add_f32 v83, v83, v60\n"
    "v_mfma_f32_16x16x32_bf16 v[0:3], v[104:107], v[168:171], v[0:3]\n"
    "ds_read_b128 v[92:95], v124 offset:17024\n"
    "v_mfma_f32_16x16x32_bf16 v[8:11], v[104:107], v[172:175], v[8:11]\n"
    "v_exp_f32 v61, v61\n"
    "v_mfma_f32_16x16x32_bf16 v[16:19], v[104:107], v[232:235], v[16:19]\n"
    "buffer_load_dwordx4 v126, s[56:59], s78 offen offset:1056 lds\n"
    "v_mfma_f32_16x16x32_bf16 v[24:27], v[104:107], v[236:239], v[24:27]\n"
    "s_waitcnt lgkmcnt(3)\n"
    "v_add_f32 v83, v83, v61\n"
    "v_cvt_pk_bf16_f32 v58, v60, v61\n"
    "v_mfma_f32_16x16x32_bf16 v[0:3], v[108:111], v[176:179], v[0:3]\n"
    "ds_read_b128 v[96:99], v124 offset:17088\n"
    "v_mfma_f32_16x16x32_bf16 v[8:11], v[108:111], v[180:183], v[8:11]\n"
    "v_exp_f32 v38, v38\n"
    "v_mfma_f32_16x16x32_bf16 v[16:19], v[108:111], v[240:243], v[16:19]\n"
    "buffer_load_dwordx4 v126, s[56:59], s79 offen offset:2112 lds\n"
    "v_mfma_f32_16x16x32_bf16 v[24:27], v[108:111], v[244:247], v[24:27]\n"
    "v_add_f32 v80, v80, v38\n"
    "v_mfma_f32_16x16x32_bf16 v[0:3], v[112:115], v[184:187], v[0:3]\n"
    "ds_read_b128 v[100:103], v119 offset:17152\n"
    "v_mfma_f32_16x16x32_bf16 v[8:11], v[112:115], v[188:191], v[8:11]\n"
    "v_exp_f32 v39, v39\n"
    "v_mfma_f32_16x16x32_bf16 v[16:19], v[112:115], v[248:251], v[16:19]\n"
    "buffer_load_dwordx4 v126, s[56:59], s80 offen offset:3168 lds\n"
    "v_mfma_f32_16x16x32_bf16 v[24:27], v[112:115], v[252:255], v[24:27]\n"
    "s_waitcnt lgkmcnt(3)\n"
    "v_add_f32 v80, v80, v39\n"
    "v_cvt_pk_bf16_f32 v35, v38, v39\n"
    "v_mfma_f32_16x16x32_bf16 v[4:7], v[84:87], v[128:131], v[64:67]\n"
    "ds_read_b128 v[104:107], v119 offset:17216\n"
    "v_mfma_f32_16x16x32_bf16 v[12:15], v[84:87], v[132:135], v[68:71]\n"
    "s_add_u32 m0, s74, 71808\n"
    "v_exp_f32 v46, v46\n"
    "v_mfma_f32_16x16x32_bf16 v[20:23], v[84:87], v[192:195], v[72:75]\n"
    "buffer_load_dwordx4 v126, s[56:59], s81 offen offset:0 lds\n"
    "v_mfma_f32_16x16x32_bf16 v[28:31], v[84:87], v[196:199], v[76:79]\n"
    "v_add_f32 v81, v81, v46\n"
    "v_mfma_f32_16x16x32_bf16 v[4:7], v[88:91], v[136:139], v[4:7]\n"
    "ds_read_b128 v[108:111], v119 offset:17280\n"
    "v_mfma_f32_16x16x32_bf16 v[12:15], v[88:91], v[140:143], v[12:15]\n"
    "v_exp_f32 v47, v47\n"
    "v_mfma_f32_16x16x32_bf16 v[20:23], v[88:91], v[200:203], v[20:23]\n"
    "buffer_load_dwordx4 v126, s[56:59], s82 offen offset:1056 lds\n"
    "v_mfma_f32_16x16x32_bf16 v[28:31], v[88:91], v[204:207], v[28:31]\n"
    "s_waitcnt lgkmcnt(3)\n"
    "v_add_f32 v81, v81, v47\n"
    "v_cvt_pk_bf16_f32 v43, v46, v47\n"
    "v_mfma_f32_16x16x32_bf16 v[4:7], v[92:95], v[144:147], v[4:7]\n"
    "ds_read_b128 v[112:115], v119 offset:17344\n"
    "v_mfma_f32_16x16x32_bf16 v[12:15], v[92:95], v[148:151], v[12:15]\n"
    "v_exp_f32 v54, v54\n"
    "v_mfma_f32_16x16x32_bf16 v[20:23], v[92:95], v[208:211], v[20:23]\n"
    "buffer_load_dwordx4 v126, s[56:59], s83 offen offset:2112 lds\n"
    "v_mfma_f32_16x16x32_bf16 v[28:31], v[92:95], v[212:215], v[28:31]\n"
    "v_add_f32 v82, v82, v54\n"
    "v_mfma_f32_16x16x32_bf16 v[4:7], v[96:99], v[152:155], v[4:7]\n"
    "ds_read_b64_tr_b16 v[84:85], v125 offset:33792\n"
    "ds_read_b64_tr_b16 v[86:87], v125 offset:50688\n"
    "v_mfma_f32_16x16x32_bf16 v[12:15], v[96:99], v[156:159], v[12:15]\n"
    "v_exp_f32 v55, v55\n"
    "v_mfma_f32_16x16x32_bf16 v[20:23], v[96:99], v[216:219], v[20:23]\n"
    "buffer_load_dwordx4 v126, s[56:59], s84 offen offset:3168 lds\n"
    "v_mfma_f32_16x16x32_bf16 v[28:31], v[96:99], v[220:223], v[28:31]\n"
    "s_waitcnt lgkmcnt(4)\n"
    "v_add_f32 v82, v82, v55\n"
    "v_cvt_pk_bf16_f32 v51, v54, v55\n"
    "v_mfma_f32_16x16x32_bf16 v[4:7], v[100:103], v[160:163], v[4:7]\n"
    "s_sub_u32 s58, s58, 32768\n"
    "ds_read_b64_tr_b16 v[88:89], v125 offset:33824\n"
    "ds_read_b64_tr_b16 v[90:91], v125 offset:50720\n"
    "v_mfma_f32_16x16x32_bf16 v[12:15], v[100:103], v[164:167], v[12:15]\n"
    "v_exp_f32 v62, v62\n"
    "v_mfma_f32_16x16x32_bf16 v[20:23], v[100:103], v[224:227], v[20:23]\n"
    "buffer_load_dword v118, v116, s[52:55], 0 offen\n"
    "v_mfma_f32_16x16x32_bf16 v[28:31], v[100:103], v[228:231], v[28:31]\n"
    "s_add_u32 s56, s56, 32768\n"
    "s_addc_u32 s57, s57, 0\n"
    "v_add_f32 v83, v83, v62\n"
    "v_mfma_f32_16x16x32_bf16 v[4:7], v[104:107], v[168:171], v[4:7]\n"
    "ds_read_b64_tr_b16 v[92:93], v125 offset:33856\n"
    "ds_read_b64_tr_b16 v[94:95], v125 offset:50752\n"
    "v_mfma_f32_16x16x32_bf16 v[12:15], v[104:107], v[172:175], v[12:15]\n"
    "v_exp_f32 v63, v63\n"
    "v_mfma_f32_16x16x32_bf16 v[20:23], v[104:107], v[232:235], v[20:23]\n"
    "buffer_load_dword v118, v116, s[56:59], 0 offen\n"
    "v_mfma_f32_16x16x32_bf16 v[28:31], v[104:107], v[236:239], v[28:31]\n"
    "s_waitcnt lgkmcnt(6)\n"
    "v_add_f32 v83, v83, v63\n"
    "v_cvt_pk_bf16_f32 v59, v62, v63\n"
    "v_mfma_f32_16x16x32_bf16 v[4:7], v[108:111], v[176:179], v[4:7]\n"
    "ds_read_b64_tr_b16 v[96:97], v125 offset:33888\n"
    "ds_read_b64_tr_b16 v[98:99], v125 offset:50784\n"
    "v_mfma_f32_16x16x32_bf16 v[12:15], v[108:111], v[180:183], v[12:15]\n"
    "v_mfma_f32_16x16x32_bf16 v[20:23], v[108:111], v[240:243], v[20:23]\n"
    "v_mfma_f32_16x16x32_bf16 v[28:31], v[108:111], v[244:247], v[28:31]\n"
    "v_mfma_f32_16x16x32_bf16 v[4:7], v[112:115], v[184:187], v[4:7]\n"
    "ds_read_b64_tr_b16 v[100:101], v125 offset:33920\n"
    "ds_read_b64_tr_b16 v[102:103], v125 offset:50816\n"
    "v_mfma_f32_16x16x32_bf16 v[12:15], v[112:115], v[188:191], v[12:15]\n"
    "v_mfma_f32_16x16x32_bf16 v[20:23], v[112:115], v[248:251], v[20:23]\n"
    "v_mfma_f32_16x16x32_bf16 v[28:31], v[112:115], v[252:255], v[28:31]\n"
    "s_waitcnt lgkmcnt(6)\n"
    "v_mfma_f32_16x16x32_bf16 a[0:3], v[84:87], v[32:35], a[0:3]\n"
    "ds_read_b64_tr_b16 v[104:105], v125 offset:33952\n"
    "ds_read_b64_tr_b16 v[106:107], v125 offset:50848\n"
    "v_mfma_f32_16x16x32_bf16 a[4:7], v[84:87], v[40:43], a[4:7]\n"
    "v_exp_f32 v0, v0\n"
    "v_mfma_f32_16x16x32_bf16 a[128:131], v[84:87], v[48:51], a[128:131]\n"
    "v_add_f32 v80, v80, v0\n"
    "v_mfma_f32_16x16x32_bf16 a[132:135], v[84:87], v[56:59], a[132:135]\n"
    "v_mfma_f32_16x16x32_bf16 a[8:11], v[88:91], v[32:35], a[8:11]\n"
    "ds_read_b64_tr_b16 v[108:109], v125 offset:33984\n"
    "ds_read_b64_tr_b16 v[110:111], v125 offset:50880\n"
    "v_mfma_f32_16x16x32_bf16 a[12:15], v[88:91], v[40:43], a[12:15]\n"
    "v_exp_f32 v1, v1\n"
    "v_mfma_f32_16x16x32_bf16 a[136:139], v[88:91], v[48:51], a[136:139]\n"
    "v_add_f32 v80, v80, v1\n"
    "v_cvt_pk_bf16_f32 v0, v0, v1\n"
    "v_mfma_f32_16x16x32_bf16 a[140:143], v[88:91], v[56:59], a[140:143]\n"
    "s_waitcnt lgkmcnt(6)\n"
    "v_mfma_f32_16x16x32_bf16 a[16:19], v[92:95], v[32:35], a[16:19]\n"
    "ds_read_b64_tr_b16 v[112:113], v125 offset:34016\n"
    "ds_read_b64_tr_b16 v[114:115], v125 offset:50912\n"
    "v_mfma_f32_16x16x32_bf16 a[20:23], v[92:95], v[40:43], a[20:23]\n"
    "v_exp_f32 v8, v8\n"
    "v_mfma_f32_16x16x32_bf16 a[144:147], v[92:95], v[48:51], a[144:147]\n"
    "v_add_f32 v81, v81, v8\n"
    "v_mfma_f32_16x16x32_bf16 a[148:151], v[92:95], v[56:59], a[148:151]\n"
    "v_mfma_f32_16x16x32_bf16 a[24:27], v[96:99], v[32:35], a[24:27]\n"
    "ds_read_b64_tr_b16 v[84:85], v125 offset:34048\n"
    "ds_read_b64_tr_b16 v[86:87], v125 offset:50944\n"
    "v_mfma_f32_16x16x32_bf16 a[28:31], v[96:99], v[40:43], a[28:31]\n"
    "v_exp_f32 v9, v9\n"
    "v_mfma_f32_16x16x32_bf16 a[152:155], v[96:99], v[48:51], a[152:155]\n"
    "v_exp_f32 v16, v16\n"
    "v_mfma_f32_16x16x32_bf16 a[156:159], v[96:99], v[56:59], a[156:159]\n"
    "s_waitcnt lgkmcnt(6)\n"
    "v_add_f32 v81, v81, v9\n"
    "v_cvt_pk_bf16_f32 v8, v8, v9\n"
    "v_mfma_f32_16x16x32_bf16 a[32:35], v[100:103], v[32:35], a[32:35]\n"
    "ds_read_b64_tr_b16 v[88:89], v125 offset:34080\n"
    "ds_read_b64_tr_b16 v[90:91], v125 offset:50976\n"
    "v_mfma_f32_16x16x32_bf16 a[36:39], v[100:103], v[40:43], a[36:39]\n"
    "v_exp_f32 v17, v17\n"
    "v_mfma_f32_16x16x32_bf16 a[160:163], v[100:103], v[48:51], a[160:163]\n"
    "v_add_f32 v82, v82, v16\n"
    "v_add_f32 v82, v82, v17\n"
    "v_mfma_f32_16x16x32_bf16 a[164:167], v[100:103], v[56:59], a[164:167]\n"
    "v_cvt_pk_bf16_f32 v16, v16, v17\n"
    "v_mfma_f32_16x16x32_bf16 a[40:43], v[104:107], v[32:35], a[40:43]\n"
    "ds_read_b64_tr_b16 v[92:93], v125 offset:34112\n"
    "ds_read_b64_tr_b16 v[94:95], v125 offset:51008\n"
    "v_mfma_f32_16x16x32_bf16 a[44:47], v[104:107], v[40:43], a[44:47]\n"
    "v_exp_f32 v24, v24\n"
    "v_mfma_f32_16x16x32_bf16 a[168:171], v[104:107], v[48:51], a[168:171]\n"
    "v_add_f32 v83, v83, v24\n"
    "v_mfma_f32_16x16x32_bf16 a[172:175], v[104:107], v[56:59], a[172:175]\n"
    "s_waitcnt lgkmcnt(6)\n"
    "v_mfma_f32_16x16x32_bf16 a[48:51], v[108:111], v[32:35], a[48:51]\n"
    "ds_read_b64_tr_b16 v[96:97], v125 offset:34144\n"
    "ds_read_b64_tr_b16 v[98:99], v125 offset:51040\n"
    "v_mfma_f32_16x16x32_bf16 a[52:55], v[108:111], v[40:43], a[52:55]\n"
    "v_exp_f32 v25, v25\n"
    "v_mfma_f32_16x16x32_bf16 a[176:179], v[108:111], v[48:51], a[176:179]\n"
    "v_add_f32 v83, v83, v25\n"
    "v_cvt_pk_bf16_f32 v24, v24, v25\n"
    "v_mfma_f32_16x16x32_bf16 a[180:183], v[108:111], v[56:59], a[180:183]\n"
    "v_mfma_f32_16x16x32_bf16 a[56:59], v[112:115], v[32:35], a[56:59]\n"
    "ds_read_b64_tr_b16 v[100:101], v125 offset:34176\n"
    "ds_read_b64_tr_b16 v[102:103], v125 offset:51072\n"
    "v_mfma_f32_16x16x32_bf16 a[60:63], v[112:115], v[40:43], a[60:63]\n"
    "v_exp_f32 v2, v2\n"
    "v_mfma_f32_16x16x32_bf16 a[184:187], v[112:115], v[48:51], a[184:187]\n"
    "v_add_f32 v80, v80, v2\n"
    "v_mfma_f32_16x16x32_bf16 a[188:191], v[112:115], v[56:59], a[188:191]\n"
    "s_waitcnt lgkmcnt(6)\n"
    "v_mfma_f32_16x16x32_bf16 a[64:67], v[84:87], v[32:35], a[64:67]\n"
    "ds_read_b64_tr_b16 v[104:105], v125 offset:34208\n"
    "ds_read_b64_tr_b16 v[106:107], v125 offset:51104\n"
    "v_mfma_f32_16x16x32_bf16 a[68:71], v[84:87], v[40:43], a[68:71]\n"
    "v_exp_f32 v3, v3\n"
    "v_mfma_f32_16x16x32_bf16 a[192:195], v[84:87], v[48:51], a[192:195]\n"
    "v_exp_f32 v10, v10\n"
    "v_mfma_f32_16x16x32_bf16 a[196:199], v[84:87], v[56:59], a[196:199]\n"
    "v_add_f32 v80, v80, v3\n"
    "v_cvt_pk_bf16_f32 v1, v2, v3\n"
    "v_mfma_f32_16x16x32_bf16 a[72:75], v[88:91], v[32:35], a[72:75]\n"
    "ds_read_b64_tr_b16 v[108:109], v125 offset:34240\n"
    "ds_read_b64_tr_b16 v[110:111], v125 offset:51136\n"
    "v_mfma_f32_16x16x32_bf16 a[76:79], v[88:91], v[40:43], a[76:79]\n"
    "v_exp_f32 v11, v11\n"
    "v_mfma_f32_16x16x32_bf16 a[200:203], v[88:91], v[48:51], a[200:203]\n"
    "v_add_f32 v81, v81, v10\n"
    "v_add_f32 v81, v81, v11\n"
    "v_mfma_f32_16x16x32_bf16 a[204:207], v[88:91], v[56:59], a[204:207]\n"
    "s_waitcnt lgkmcnt(6)\n"
    "v_cvt_pk_bf16_f32 v9, v10, v11\n"
    "v_mfma_f32_16x16x32_bf16 a[80:83], v[92:95], v[32:35], a[80:83]\n"
    "ds_read_b64_tr_b16 v[112:113], v125 offset:34272\n"
    "ds_read_b64_tr_b16 v[114:115], v125 offset:51168\n"
    "v_mfma_f32_16x16x32_bf16 a[84:87], v[92:95], v[40:43], a[84:87]\n"
    "v_exp_f32 v18, v18\n"
    "v_mfma_f32_16x16x32_bf16 a[208:211], v[92:95], v[48:51], a[208:211]\n"
    "v_add_f32 v82, v82, v18\n"
    "v_mfma_f32_16x16x32_bf16 a[212:215], v[92:95], v[56:59], a[212:215]\n"
    "v_mfma_f32_16x16x32_bf16 a[88:91], v[96:99], v[32:35], a[88:91]\n"
    "v_mfma_f32_16x16x32_bf16 a[92:95], v[96:99], v[40:43], a[92:95]\n"
    "v_exp_f32 v19, v19\n"
    "v_mfma_f32_16x16x32_bf16 a[216:219], v[96:99], v[48:51], a[216:219]\n"
    "v_add_f32 v82, v82, v19\n"
    "v_cvt_pk_bf16_f32 v17, v18, v19\n"
    "v_mfma_f32_16x16x32_bf16 a[220:223], v[96:99], v[56:59], a[220:223]\n"
    "s_waitcnt vmcnt(2) lgkmcnt(0)\n"
    "s_barrier\n"
    "v_mfma_f32_16x16x32_bf16 a[96:99], v[100:103], v[32:35], a[96:99]\n"
    "ds_read_b128 v[84:87], v124 offset:33792\n"
    "ds_read_b128 v[88:91], v124 offset:33856\n"
    "v_mfma_f32_16x16x32_bf16 a[100:103], v[100:103], v[40:43], a[100:103]\n"
    "s_add_u32 m0, s74, 0\n"
    "v_exp_f32 v26, v26\n"
    "v_mfma_f32_16x16x32_bf16 a[224:227], v[100:103], v[48:51], a[224:227]\n"
    "buffer_load_dwordx4 v126, s[52:55], s77 offen offset:0 lds\n"
    "v_mfma_f32_16x16x32_bf16 a[228:231], v[100:103], v[56:59], a[228:231]\n"
    "v_add_f32 v83, v83, v26\n"
    "v_mfma_f32_16x16x32_bf16 a[104:107], v[104:107], v[32:35], a[104:107]\n"
    "ds_read_b128 v[92:95], v124 offset:33920\n"
    "v_mfma_f32_16x16x32_bf16 a[108:111], v[104:107], v[40:43], a[108:111]\n"
    "v_exp_f32 v27, v27\n"
    "v_mfma_f32_16x16x32_bf16 a[232:235], v[104:107], v[48:51], a[232:235]\n"
    "buffer_load_dwordx4 v126, s[52:55], s78 offen offset:1056 lds\n"
    "v_mfma_f32_16x16x32_bf16 a[236:239], v[104:107], v[56:59], a[236:239]\n"
    "v_add_f32 v83, v83, v27\n"
    "v_cvt_pk_bf16_f32 v25, v26, v27\n"
    "v_mfma_f32_16x16x32_bf16 a[112:115], v[108:111], v[32:35], a[112:115]\n"
    "ds_read_b128 v[96:99], v124 offset:33984\n"
    "v_mfma_f32_16x16x32_bf16 a[116:119], v[108:111], v[40:43], a[116:119]\n"
    "v_exp_f32 v4, v4\n"
    "v_mfma_f32_16x16x32_bf16 a[240:243], v[108:111], v[48:51], a[240:243]\n"
    "buffer_load_dwordx4 v126, s[52:55], s79 offen offset:2112 lds\n"
    "v_mfma_f32_16x16x32_bf16 a[244:247], v[108:111], v[56:59], a[244:247]\n"
    "v_add_f32 v80, v80, v4\n"
    "v_mfma_f32_16x16x32_bf16 a[120:123], v[112:115], v[32:35], a[120:123]\n"
    "ds_read_b128 v[100:103], v119 offset:34048\n"
    "v_mfma_f32_16x16x32_bf16 a[124:127], v[112:115], v[40:43], a[124:127]\n"
    "v_exp_f32 v5, v5\n"
    "v_mfma_f32_16x16x32_bf16 a[248:251], v[112:115], v[48:51], a[248:251]\n"
    "buffer_load_dwordx4 v126, s[52:55], s80 offen offset:3168 lds\n"
    "v_mfma_f32_16x16x32_bf16 a[252:255], v[112:115], v[56:59], a[252:255]\n"
    "s_waitcnt lgkmcnt(3)\n"
    "v_add_f32 v80, v80, v5\n"
    "v_cvt_pk_bf16_f32 v2, v4, v5\n"
    "v_mfma_f32_16x16x32_bf16 v[32:35], v[84:87], v[128:131], v[64:67]\n"
    "ds_read_b128 v[104:107], v119 offset:34112\n"
    "v_mfma_f32_16x16x32_bf16 v[40:43], v[84:87], v[132:135], v[68:71]\n"
    "s_add_u32 m0, s74, 4224\n"
    "v_exp_f32 v12, v12\n"
    "v_mfma_f32_16x16x32_bf16 v[48:51], v[84:87], v[192:195], v[72:75]\n"
    "buffer_load_dwordx4 v126, s[52:55], s81 offen offset:0 lds\n"
    "v_mfma_f32_16x16x32_bf16 v[56:59], v[84:87], v[196:199], v[76:79]\n"
    "v_add_f32 v81, v81, v12\n"
    "v_mfma_f32_16x16x32_bf16 v[32:35], v[88:91], v[136:139], v[32:35]\n"
    "ds_read_b128 v[108:111], v119 offset:34176\n"
    "v_mfma_f32_16x16x32_bf16 v[40:43], v[88:91], v[140:143], v[40:43]\n"
    "v_exp_f32 v13, v13\n"
    "v_mfma_f32_16x16x32_bf16 v[48:51], v[88:91], v[200:203], v[48:51]\n"
    "buffer_load_dwordx4 v126, s[52:55], s82 offen offset:1056 lds\n"
    "v_mfma_f32_16x16x32_bf16 v[56:59], v[88:91], v[204:207], v[56:59]\n"
    "s_waitcnt lgkmcnt(3)\n"
    "v_add_f32 v81, v81, v13\n"
    "v_cvt_pk_bf16_f32 v10, v12, v13\n"
    "v_mfma_f32_16x16x32_bf16 v[32:35], v[92:95], v[144:147], v[32:35]\n"
    "ds_read_b128 v[112:115], v119 offset:34240\n"
    "v_mfma_f32_16x16x32_bf16 v[40:43], v[92:95], v[148:151], v[40:43]\n"
    "v_exp_f32 v20, v20\n"
    "v_mfma_f32_16x16x32_bf16 v[48:51], v[92:95], v[208:211], v[48:51]\n"
    "buffer_load_dwordx4 v126, s[52:55], s83 offen offset:2112 lds\n"
    "v_mfma_f32_16x16x32_bf16 v[56:59], v[92:95], v[212:215], v[56:59]\n"
    "v_add_f32 v82, v82, v20\n"
    "v_mfma_f32_16x16x32_bf16 v[32:35], v[96:99], v[152:155], v[32:35]\n"
    "ds_read_b128 v[84:87], v124 offset:50688\n"
    "v_mfma_f32_16x16x32_bf16 v[40:43], v[96:99], v[156:159], v[40:43]\n"
    "v_exp_f32 v21, v21\n"
    "v_mfma_f32_16x16x32_bf16 v[48:51], v[96:99], v[216:219], v[48:51]\n"
    "buffer_load_dwordx4 v126, s[52:55], s84 offen offset:3168 lds\n"
    "v_mfma_f32_16x16x32_bf16 v[56:59], v[96:99], v[220:223], v[56:59]\n"
    "s_waitcnt lgkmcnt(3)\n"
    "v_add_f32 v82, v82, v21\n"
    "v_cvt_pk_bf16_f32 v18, v20, v21\n"
    "v_mfma_f32_16x16x32_bf16 v[32:35], v[100:103], v[160:163], v[32:35]\n"
    "s_sub_u32 s54, s54, 32768\n"
    "ds_read_b128 v[88:91], v124 offset:50752\n"
    "v_mfma_f32_16x16x32_bf16 v[40:43], v[100:103], v[164:167], v[40:43]\n"
    "s_add_u32 m0, s74, 101376\n"
    "v_exp_f32 v28, v28\n"
    "v_mfma_f32_16x16x32_bf16 v[48:51], v[100:103], v[224:227], v[48:51]\n"
    "buffer_load_dwordx4 v126, s[56:59], s77 offen offset:0 lds\n"
    "v_mfma_f32_16x16x32_bf16 v[56:59], v[100:103], v[228:231], v[56:59]\n"
    "s_add_u32 s52, s52, 32768\n"
    "s_addc_u32 s53, s53, 0\n"
    "v_add_f32 v83, v83, v28\n"
    "v_mfma_f32_16x16x32_bf16 v[32:35], v[104:107], v[168:171], v[32:35]\n"
    "ds_read_b128 v[92:95], v124 offset:50816\n"
    "v_mfma_f32_16x16x32_bf16 v[40:43], v[104:107], v[172:175], v[40:43]\n"
    "v_exp_f32 v29, v29\n"
    "v_mfma_f32_16x16x32_bf16 v[48:51], v[104:107], v[232:235], v[48:51]\n"
    "buffer_load_dwordx4 v126, s[56:59], s78 offen offset:1056 lds\n"
    "v_mfma_f32_16x16x32_bf16 v[56:59], v[104:107], v[236:239], v[56:59]\n"
    "s_waitcnt lgkmcnt(3)\n"
    "v_add_f32 v83, v83, v29\n"
    "v_cvt_pk_bf16_f32 v26, v28, v29\n"
    "v_mfma_f32_16x16x32_bf16 v[32:35], v[108:111], v[176:179], v[32:35]\n"
    "ds_read_b128 v[96:99], v124 offset:50880\n"
    "v_mfma_f32_16x16x32_bf16 v[40:43], v[108:111], v[180:183], v[40:43]\n"
    "v_exp_f32 v6, v6\n"
    "v_mfma_f32_16x16x32_bf16 v[48:51], v[108:111], v[240:243], v[48:51]\n"
    "buffer_load_dwordx4 v126, s[56:59], s79 offen offset:2112 lds\n"
    "v_mfma_f32_16x16x32_bf16 v[56:59], v[108:111], v[244:247], v[56:59]\n"
    "v_add_f32 v80, v80, v6\n"
    "v_mfma_f32_16x16x32_bf16 v[32:35], v[112:115], v[184:187], v[32:35]\n"
    "ds_read_b128 v[100:103], v119 offset:50944\n"
    "v_mfma_f32_16x16x32_bf16 v[40:43], v[112:115], v[188:191], v[40:43]\n"
    "v_exp_f32 v7, v7\n"
    "v_mfma_f32_16x16x32_bf16 v[48:51], v[112:115], v[248:251], v[48:51]\n"
    "buffer_load_dwordx4 v126, s[56:59], s80 offen offset:3168 lds\n"
    "v_mfma_f32_16x16x32_bf16 v[56:59], v[112:115], v[252:255], v[56:59]\n"
    "s_waitcnt lgkmcnt(3)\n"
    "v_add_f32 v80, v80, v7\n"
    "v_cvt_pk_bf16_f32 v3, v6, v7\n"
    "v_mfma_f32_16x16x32_bf16 v[36:39], v[84:87], v[128:131], v[64:67]\n"
    "ds_read_b128 v[104:107], v119 offset:51008\n"
    "v_mfma_f32_16x16x32_bf16 v[44:47], v[84:87], v[132:135], v[68:71]\n"
    "s_add_u32 m0, s74, 105600\n"
    "v_exp_f32 v14, v14\n"
    "v_mfma_f32_16x16x32_bf16 v[52:55], v[84:87], v[192:195], v[72:75]\n"
    "buffer_load_dwordx4 v126, s[56:59], s81 offen offset:0 lds\n"
    "v_mfma_f32_16x16x32_bf16 v[60:63], v[84:87], v[196:199], v[76:79]\n"
    "v_add_f32 v81, v81, v14\n"
    "v_mfma_f32_16x16x32_bf16 v[36:39], v[88:91], v[136:139], v[36:39]\n"
    "ds_read_b128 v[108:111], v119 offset:51072\n"
    "v_mfma_f32_16x16x32_bf16 v[44:47], v[88:91], v[140:143], v[44:47]\n"
    "v_exp_f32 v15, v15\n"
    "v_mfma_f32_16x16x32_bf16 v[52:55], v[88:91], v[200:203], v[52:55]\n"
    "buffer_load_dwordx4 v126, s[56:59], s82 offen offset:1056 lds\n"
    "v_mfma_f32_16x16x32_bf16 v[60:63], v[88:91], v[204:207], v[60:63]\n"
    "s_waitcnt lgkmcnt(3)\n"
    "v_add_f32 v81, v81, v15\n"
    "v_cvt_pk_bf16_f32 v11, v14, v15\n"
    "v_mfma_f32_16x16x32_bf16 v[36:39], v[92:95], v[144:147], v[36:39]\n"
    "ds_read_b128 v[112:115], v119 offset:51136\n"
    "v_mfma_f32_16x16x32_bf16 v[44:47], v[92:95], v[148:151], v[44:47]\n"
    "v_exp_f32 v22, v22\n"
    "v_mfma_f32_16x16x32_bf16 v[52:55], v[92:95], v[208:211], v[52:55]\n"
    "buffer_load_dwordx4 v126, s[56:59], s83 offen offset:2112 lds\n"
    "v_mfma_f32_16x16x32_bf16 v[60:63], v[92:95], v[212:215], v[60:63]\n"
    "v_add_f32 v82, v82, v22\n"
    "v_mfma_f32_16x16x32_bf16 v[36:39], v[96:99], v[152:155], v[36:39]\n"
    "ds_read_b64_tr_b16 v[84:85], v125 offset:0\n"
    "ds_read_b64_tr_b16 v[86:87], v125 offset:16896\n"
    "v_mfma_f32_16x16x32_bf16 v[44:47], v[96:99], v[156:159], v[44:47]\n"
    "v_exp_f32 v23, v23\n"
    "v_mfma_f32_16x16x32_bf16 v[52:55], v[96:99], v[216:219], v[52:55]\n"
    "buffer_load_dwordx4 v126, s[56:59], s84 offen offset:3168 lds\n"
    "v_mfma_f32_16x16x32_bf16 v[60:63], v[96:99], v[220:223], v[60:63]\n"
    "s_waitcnt lgkmcnt(4)\n"
    "v_add_f32 v82, v82, v23\n"
    "v_cvt_pk_bf16_f32 v19, v22, v23\n"
    "v_mfma_f32_16x16x32_bf16 v[36:39], v[100:103], v[160:163], v[36:39]\n"
    "s_sub_u32 s58, s58, 32768\n"
    "ds_read_b64_tr_b16 v[88:89], v125 offset:32\n"
    "ds_read_b64_tr_b16 v[90:91], v125 offset:16928\n"
    "v_mfma_f32_16x16x32_bf16 v[44:47], v[100:103], v[164:167], v[44:47]\n"
    "v_exp_f32 v30, v30\n"
    "v_mfma_f32_16x16x32_bf16 v[52:55], v[100:103], v[224:227], v[52:55]\n"
    "buffer_load_dword v118, v116, s[52:55], 0 offen\n"
    "v_mfma_f32_16x16x32_bf16 v[60:63], v[100:103], v[228:231], v[60:63]\n"
    "s_add_u32 s56, s56, 32768\n"
    "s_addc_u32 s57, s57, 0\n"
    "v_add_f32 v83, v83, v30\n"
    "v_mfma_f32_16x16x32_bf16 v[36:39], v[104:107], v[168:171], v[36:39]\n"
    "ds_read_b64_tr_b16 v[92:93], v125 offset:64\n"
    "ds_read_b64_tr_b16 v[94:95], v125 offset:16960\n"
    "v_mfma_f32_16x16x32_bf16 v[44:47], v[104:107], v[172:175], v[44:47]\n"
    "v_exp_f32 v31, v31\n"
    "v_mfma_f32_16x16x32_bf16 v[52:55], v[104:107], v[232:235], v[52:55]\n"
    "buffer_load_dword v118, v116, s[56:59], 0 offen\n"
    "v_mfma_f32_16x16x32_bf16 v[60:63], v[104:107], v[236:239], v[60:63]\n"
    "s_waitcnt lgkmcnt(6)\n"
    "v_add_f32 v83, v83, v31\n"
    "v_cvt_pk_bf16_f32 v27, v30, v31\n"
    "v_mfma_f32_16x16x32_bf16 v[36:39], v[108:111], v[176:179], v[36:39]\n"
    "ds_read_b64_tr_b16 v[96:97], v125 offset:96\n"
    "ds_read_b64_tr_b16 v[98:99], v125 offset:16992\n"
    "v_mfma_f32_16x16x32_bf16 v[44:47], v[108:111], v[180:183], v[44:47]\n"
    "v_mfma_f32_16x16x32_bf16 v[52:55], v[108:111], v[240:243], v[52:55]\n"
    "v_mfma_f32_16x16x32_bf16 v[60:63], v[108:111], v[244:247], v[60:63]\n"
    "v_mfma_f32_16x16x32_bf16 v[36:39], v[112:115], v[184:187], v[36:39]\n"
    "ds_read_b64_tr_b16 v[100:101], v125 offset:128\n"
    "ds_read_b64_tr_b16 v[102:103], v125 offset:17024\n"
    "v_mfma_f32_16x16x32_bf16 v[44:47], v[112:115], v[188:191], v[44:47]\n"
    "v_mfma_f32_16x16x32_bf16 v[52:55], v[112:115], v[248:251], v[52:55]\n"
    "v_mfma_f32_16x16x32_bf16 v[60:63], v[112:115], v[252:255], v[60:63]\n"
    "s_waitcnt lgkmcnt(6)\n"
    "v_mfma_f32_16x16x32_bf16 a[0:3], v[84:87], v[0:3], a[0:3]\n"
    "ds_read_b64_tr_b16 v[104:105], v125 offset:160\n"
    "ds_read_b64_tr_b16 v[106:107], v125 offset:17056\n"
    "v_mfma_f32_16x16x32_bf16 a[4:7], v[84:87], v[8:11], a[4:7]\n"
    "v_exp_f32 v32, v32\n"
    "v_mfma_f32_16x16x32_bf16 a[128:131], v[84:87], v[16:19], a[128:131]\n"
    "v_add_f32 v80, v80, v32\n"
    "v_mfma_f32_16x16x32_bf16 a[132:135], v[84:87], v[24:27], a[132:135]\n"
    "v_mfma_f32_16x16x32_bf16 a[8:11], v[88:91], v[0:3], a[8:11]\n"
    "ds_read_b64_tr_b16 v[108:109], v125 offset:192\n"
    "ds_read_b64_tr_b16 v[110:111], v125 offset:17088\n"
    "v_mfma_f32_16x16x32_bf16 a[12:15], v[88:91], v[8:11], a[12:15]\n"
    "v_exp_f32 v33, v33\n"
    "v_mfma_f32_16x16x32_bf16 a[136:139], v[88:91], v[16:19], a[136:139]\n"
    "v_add_f32 v80, v80, v33\n"
    "v_cvt_pk_bf16_f32 v32, v32, v33\n"
    "v_mfma_f32_16x16x32_bf16 a[140:143], v[88:91], v[24:27], a[140:143]\n"
    "s_waitcnt lgkmcnt(6)\n"
    "v_mfma_f32_16x16x32_bf16 a[16:19], v[92:95], v[0:3], a[16:19]\n"
    "ds_read_b64_tr_b16 v[112:113], v125 offset:224\n"
    "ds_read_b64_tr_b16 v[114:115], v125 offset:17120\n"
    "v_mfma_f32_16x16x32_bf16 a[20:23], v[92:95], v[8:11], a[20:23]\n"
    "v_exp_f32 v40, v40\n"
    "v_mfma_f32_16x16x32_bf16 a[144:147], v[92:95], v[16:19], a[144:147]\n"
    "v_add_f32 v81, v81, v40\n"
    "v_mfma_f32_16x16x32_bf16 a[148:151], v[92:95], v[24:27], a[148:151]\n"
    "v_mfma_f32_16x16x32_bf16 a[24:27], v[96:99], v[0:3], a[24:27]\n"
    "ds_read_b64_tr_b16 v[84:85], v125 offset:256\n"
    "ds_read_b64_tr_b16 v[86:87], v125 offset:17152\n"
    "v_mfma_f32_16x16x32_bf16 a[28:31], v[96:99], v[8:11], a[28:31]\n"
    "v_exp_f32 v41, v41\n"
    "v_mfma_f32_16x16x32_bf16 a[152:155], v[96:99], v[16:19], a[152:155]\n"
    "v_exp_f32 v48, v48\n"
    "v_mfma_f32_16x16x32_bf16 a[156:159], v[96:99], v[24:27], a[156:159]\n"
    "s_waitcnt lgkmcnt(6)\n"
    "v_add_f32 v81, v81, v41\n"
    "v_cvt_pk_bf16_f32 v40, v40, v41\n"
    "v_mfma_f32_16x16x32_bf16 a[32:35], v[100:103], v[0:3], a[32:35]\n"
    "ds_read_b64_tr_b16 v[88:89], v125 offset:288\n"
    "ds_read_b64_tr_b16 v[90:91], v125 offset:17184\n"
    "v_mfma_f32_16x16x32_bf16 a[36:39], v[100:103], v[8:11], a[36:39]\n"
    "v_exp_f32 v49, v49\n"
    "v_mfma_f32_16x16x32_bf16 a[160:163], v[100:103], v[16:19], a[160:163]\n"
    "v_add_f32 v82, v82, v48\n"
    "v_add_f32 v82, v82, v49\n"
    "v_mfma_f32_16x16x32_bf16 a[164:167], v[100:103], v[24:27], a[164:167]\n"
    "v_cvt_pk_bf16_f32 v48, v48, v49\n"
    "v_mfma_f32_16x16x32_bf16 a[40:43], v[104:107], v[0:3], a[40:43]\n"
    "ds_read_b64_tr_b16 v[92:93], v125 offset:320\n"
    "ds_read_b64_tr_b16 v[94:95], v125 offset:17216\n"
    "v_mfma_f32_16x16x32_bf16 a[44:47], v[104:107], v[8:11], a[44:47]\n"
    "v_exp_f32 v56, v56\n"
    "v_mfma_f32_16x16x32_bf16 a[168:171], v[104:107], v[16:19], a[168:171]\n"
    "v_add_f32 v83, v83, v56\n"
    "v_mfma_f32_16x16x32_bf16 a[172:175], v[104:107], v[24:27], a[172:175]\n"
    "s_waitcnt lgkmcnt(6)\n"
    "v_mfma_f32_16x16x32_bf16 a[48:51], v[108:111], v[0:3], a[48:51]\n"
    "ds_read_b64_tr_b16 v[96:97], v125 offset:352\n"
    "ds_read_b64_tr_b16 v[98:99], v125 offset:17248\n"
    "v_mfma_f32_16x16x32_bf16 a[52:55], v[108:111], v[8:11], a[52:55]\n"
    "v_exp_f32 v57, v57\n"
    "v_mfma_f32_16x16x32_bf16 a[176:179], v[108:111], v[16:19], a[176:179]\n"
    "v_add_f32 v83, v83, v57\n"
    "v_cvt_pk_bf16_f32 v56, v56, v57\n"
    "v_mfma_f32_16x16x32_bf16 a[180:183], v[108:111], v[24:27], a[180:183]\n"
    "v_mfma_f32_16x16x32_bf16 a[56:59], v[112:115], v[0:3], a[56:59]\n"
    "ds_read_b64_tr_b16 v[100:101], v125 offset:384\n"
    "ds_read_b64_tr_b16 v[102:103], v125 offset:17280\n"
    "v_mfma_f32_16x16x32_bf16 a[60:63], v[112:115], v[8:11], a[60:63]\n"
    "v_exp_f32 v34, v34\n"
    "v_mfma_f32_16x16x32_bf16 a[184:187], v[112:115], v[16:19], a[184:187]\n"
    "v_add_f32 v80, v80, v34\n"
    "v_mfma_f32_16x16x32_bf16 a[188:191], v[112:115], v[24:27], a[188:191]\n"
    "s_waitcnt lgkmcnt(6)\n"
    "v_mfma_f32_16x16x32_bf16 a[64:67], v[84:87], v[0:3], a[64:67]\n"
    "ds_read_b64_tr_b16 v[104:105], v125 offset:416\n"
    "ds_read_b64_tr_b16 v[106:107], v125 offset:17312\n"
    "v_mfma_f32_16x16x32_bf16 a[68:71], v[84:87], v[8:11], a[68:71]\n"
    "v_exp_f32 v35, v35\n"
    "v_mfma_f32_16x16x32_bf16 a[192:195], v[84:87], v[16:19], a[192:195]\n"
    "v_exp_f32 v42, v42\n"
    "v_mfma_f32_16x16x32_bf16 a[196:199], v[84:87], v[24:27], a[196:199]\n"
    "v_add_f32 v80, v80, v35\n"
    "v_cvt_pk_bf16_f32 v33, v34, v35\n"
    "v_mfma_f32_16x16x32_bf16 a[72:75], v[88:91], v[0:3], a[72:75]\n"
    "ds_read_b64_tr_b16 v[108:109], v125 offset:448\n"
    "ds_read_b64_tr_b16 v[110:111], v125 offset:17344\n"
    "v_mfma_f32_16x16x32_bf16 a[76:79], v[88:91], v[8:11], a[76:79]\n"
    "v_exp_f32 v43, v43\n"
    "v_mfma_f32_16x16x32_bf16 a[200:203], v[88:91], v[16:19], a[200:203]\n"
    "v_add_f32 v81, v81, v42\n"
    "v_add_f32 v81, v81, v43\n"
    "v_mfma_f32_16x16x32_bf16 a[204:207], v[88:91], v[24:27], a[204:207]\n"
    "s_waitcnt lgkmcnt(6)\n"
    "v_cvt_pk_bf16_f32 v41, v42, v43\n"
    "v_mfma_f32_16x16x32_bf16 a[80:83], v[92:95], v[0:3], a[80:83]\n"
    "ds_read_b64_tr_b16 v[112:113], v125 offset:480\n"
    "ds_read_b64_tr_b16 v[114:115], v125 offset:17376\n"
    "v_mfma_f32_16x16x32_bf16 a[84:87], v[92:95], v[8:11], a[84:87]\n"
    "v_exp_f32 v50, v50\n"
    "v_mfma_f32_16x16x32_bf16 a[208:211], v[92:95], v[16:19], a[208:211]\n"
    "v_add_f32 v82, v82, v50\n"
    "v_mfma_f32_16x16x32_bf16 a[212:215], v[92:95], v[24:27], a[212:215]\n"
    "v_mfma_f32_16x16x32_bf16 a[88:91], v[96:99], v[0:3], a[88:91]\n"
    "v_mfma_f32_16x16x32_bf16 a[92:95], v[96:99], v[8:11], a[92:95]\n"
    "v_exp_f32 v51, v51\n"
    "v_mfma_f32_16x16x32_bf16 a[216:219], v[96:99], v[16:19], a[216:219]\n"
    "v_add_f32 v82, v82, v51\n"
    "v_cvt_pk_bf16_f32 v49, v50, v51\n"
    "v_mfma_f32_16x16x32_bf16 a[220:223], v[96:99], v[24:27], a[220:223]\n"
    "s_waitcnt vmcnt(2) lgkmcnt(0)\n"
    "s_barrier\n"
    "v_mfma_f32_16x16x32_bf16 a[96:99], v[100:103], v[0:3], a[96:99]\n"
    "ds_read_b128 v[84:87], v124 offset:0\n"
    "ds_read_b128 v[88:91], v124 offset:64\n"
    "v_mfma_f32_16x16x32_bf16 a[100:103], v[100:103], v[8:11], a[100:103]\n"
    "s_add_u32 m0, s74, 33792\n"
    "v_exp_f32 v58, v58\n"
    "v_mfma_f32_16x16x32_bf16 a[224:227], v[100:103], v[16:19], a[224:227]\n"
    "buffer_load_dwordx4 v126, s[52:55], s77 offen offset:0 lds\n"
    "v_mfma_f32_16x16x32_bf16 a[228:231], v[100:103], v[24:27], a[228:231]\n"
    "v_add_f32 v83, v83, v58\n"
    "v_mfma_f32_16x16x32_bf16 a[104:107], v[104:107], v[0:3], a[104:107]\n"
    "ds_read_b128 v[92:95], v124 offset:128\n"
    "v_mfma_f32_16x16x32_bf16 a[108:111], v[104:107], v[8:11], a[108:111]\n"
    "v_exp_f32 v59, v59\n"
    "v_mfma_f32_16x16x32_bf16 a[232:235], v[104:107], v[16:19], a[232:235]\n"
    "buffer_load_dwordx4 v126, s[52:55], s78 offen offset:1056 lds\n"
    "v_mfma_f32_16x16x32_bf16 a[236:239], v[104:107], v[24:27], a[236:239]\n"
    "v_add_f32 v83, v83, v59\n"
    "v_cvt_pk_bf16_f32 v57, v58, v59\n"
    "v_mfma_f32_16x16x32_bf16 a[112:115], v[108:111], v[0:3], a[112:115]\n"
    "ds_read_b128 v[96:99], v124 offset:192\n"
    "v_mfma_f32_16x16x32_bf16 a[116:119], v[108:111], v[8:11], a[116:119]\n"
    "s_sub_u32 s85, s85, 1\n"
    "v_exp_f32 v36, v36\n"
    "v_mfma_f32_16x16x32_bf16 a[240:243], v[108:111], v[16:19], a[240:243]\n"
    "buffer_load_dwordx4 v126, s[52:55], s79 offen offset:2112 lds\n"
    "v_mfma_f32_16x16x32_bf16 a[244:247], v[108:111], v[24:27], a[244:247]\n"
    "v_add_f32 v80, v80, v36\n"
    "v_mfma_f32_16x16x32_bf16 a[120:123], v[112:115], v[0:3], a[120:123]\n"
    "ds_read_b128 v[100:103], v119 offset:256\n"
    "v_mfma_f32_16x16x32_bf16 a[124:127], v[112:115], v[8:11], a[124:127]\n"
    "s_cmp_lg_u32 s85, 0\n"
    "v_exp_f32 v37, v37\n"
    "v_mfma_f32_16x16x32_bf16 a[248:251], v[112:115], v[16:19], a[248:251]\n"
    "buffer_load_dwordx4 v126, s[52:55], s80 offen offset:3168 lds\n"
    "v_mfma_f32_16x16x32_bf16 a[252:255], v[112:115], v[24:27], a[252:255]\n"
    "s_waitcnt lgkmcnt(3)\n"
    "v_add_f32 v80, v80, v37\n"
    "v_cvt_pk_bf16_f32 v34, v36, v37\n"
    "s_cbranch_scc1 .Lloop\n"
    "s_getpc_b64 s[86:87]\n"
    ".Lpc2:\n"
    "s_lshr_b32 s88, s68, 3\n"
    "s_lshl_b32 s88, s88, 2\n"
    "s_add_u32 s88, s88, s69\n"
    "s_mul_i32 s88, s88, 256\n"
    "s_add_u32 s88, s88, .Ltail-.Lpc2\n"
    "v_mbcnt_lo_u32_b32 v120, -1, 0\n"
    "v_mbcnt_hi_u32_b32 v120, -1, v120\n"
    "v_min_u32 v120, 1, v120\n"
    "v_lshl_add_u32 v120, v120, 7, s88\n"
    "s_mov_b32 s88, .Lmainend-.Lpc2-4\n"
    "v_min_u32 v120, s88, v120\n"
    "v_and_b32 v120, -4, v120\n"
    "global_load_dword v118, v120, s[86:87]\n"
    "v_mbcnt_lo_u32_b32 v127, -1, 0\n"
    "v_mbcnt_hi_u32_b32 v127, -1, v127\n"
    "v_mov_b32 v122, 0x80000000\n"
    "s_and_b32 s86, s71, 15\n"
    "s_mul_i32 s86, s86, 224\n"
    "s_mul_i32 s87, s69, 56\n"
    "s_add_u32 s86, s86, s87\n"
    "v_add_u32 v120, s86, v127\n"
    "v_lshlrev_b32 v120, 7, v120\n"
    "v_add_u32 v120, 3168, v120\n"
    "v_cmp_gt_u32 vcc, 56, v127\n"
    "s_nop 1\n"
    "v_cndmask_b32 v120, v122, v120, vcc\n"
    "buffer_load_dword v118, v120, s[60:63], 0 offen\n"
    "v_mfma_f32_16x16x32_bf16 v[0:3], v[84:87], v[128:131], v[64:67]\n"
    "ds_read_b128 v[104:107], v119 offset:320\n"
    "v_mfma_f32_16x16x32_bf16 v[8:11], v[84:87], v[132:135], v[68:71]\n"
    "s_add_u32 m0, s74, 38016\n"
    "v_exp_f32 v44, v44\n"
    "v_mfma_f32_16x16x32_bf16 v[16:19], v[84:87], v[192:195], v[72:75]\n"
    "buffer_load_dwordx4 v126, s[52:55], s81 offen offset:0 lds\n"
    "v_mfma_f32_16x16x32_bf16 v[24:27], v[84:87], v[196:199], v[76:79]\n"
    "v_add_f32 v81, v81, v44\n"
    "v_mfma_f32_16x16x32_bf16 v[0:3], v[88:91], v[136:139], v[0:3]\n"
    "ds_read_b128 v[108:111], v119 offset:384\n"
    "v_mfma_f32_16x16x32_bf16 v[8:11], v[88:91], v[140:143], v[8:11]\n"
    "v_exp_f32 v45, v45\n"
    "v_mfma_f32_16x16x32_bf16 v[16:19], v[88:91], v[200:203], v[16:19]\n"
    "buffer_load_dwordx4 v126, s[52:55], s82 offen offset:1056 lds\n"
    "v_mfma_f32_16x16x32_bf16 v[24:27], v[88:91], v[204:207], v[24:27]\n"
    "s_waitcnt lgkmcnt(3)\n"
    "v_add_f32 v81, v81, v45\n"
    "v_cvt_pk_bf16_f32 v42, v44, v45\n"
    "v_mfma_f32_16x16x32_bf16 v[0:3], v[92:95], v[144:147], v[0:3]\n"
    "ds_read_b128 v[112:115], v119 offset:448\n"
    "v_mfma_f32_16x16x32_bf16 v[8:11], v[92:95], v[148:151], v[8:11]\n"
    "v_exp_f32 v52, v52\n"
    "v_mfma_f32_16x16x32_bf16 v[16:19], v[92:95], v[208:211], v[16:19]\n"
    "buffer_load_dwordx4 v126, s[52:55], s83 offen offset:2112 lds\n"
    "v_mfma_f32_16x16x32_bf16 v[24:27], v[92:95], v[212:215], v[24:27]\n"
    "v_add_f32 v82, v82, v52\n"
    "v_mfma_f32_16x16x32_bf16 v[0:3], v[96:99], v[152:155], v[0:3]\n"
    "ds_read_b128 v[84:87], v124 offset:16896\n"
    "v_mfma_f32_16x16x32_bf16 v[8:11], v[96:99], v[156:159], v[8:11]\n"
    "v_exp_f32 v53, v53\n"
    "v_mfma_f32_16x16x32_bf16 v[16:19], v[96:99], v[216:219], v[16:19]\n"
    "buffer_load_dwordx4 v126, s[52:55], s84 offen offset:3168 lds\n"
    "v_mfma_f32_16x16x32_bf16 v[24:27], v[96:99], v[220:223], v[24:27]\n"
    "s_waitcnt lgkmcnt(3)\n"
    "v_add_f32 v82, v82, v53\n"
    "v_cvt_pk_bf16_f32 v50, v52, v53\n"
    "v_mfma_f32_16x16x32_bf16 v[0:3], v[100:103], v[160:163], v[0:3]\n"
    "s_sub_u32 s54, s54, 32768\n"
    "ds_read_b128 v[88:91], v124 offset:16960\n"
    "v_mfma_f32_16x16x32_bf16 v[8:11], v[100:103], v[164:167], v[8:11]\n"
    "s_add_u32 m0, s74, 67584\n"
    "v_exp_f32 v60, v60\n"
    "v_mfma_f32_16x16x32_bf16 v[16:19], v[100:103], v[224:227], v[16:19]\n"
    "buffer_load_dwordx4 v126, s[56:59], s77 offen offset:0 lds\n"
    "v_mfma_f32_16x16x32_bf16 v[24:27], v[100:103], v[228:231], v[24:27]\n"
    "s_add_u32 s52, s52, 32768\n"
    "s_addc_u32 s53, s53, 0\n"
    "v_add_f32 v83, v83, v60\n"
    "v_mfma_f32_16x16x32_bf16 v[0:3], v[104:107], v[168:171], v[0:3]\n"
    "ds_read_b128 v[92:95], v124 offset:17024\n"
    "v_mfma_f32_16x16x32_bf16 v[8:11], v[104:107], v[172:175], v[8:11]\n"
    "v_exp_f32 v61, v61\n"
    "v_mfma_f32_16x16x32_bf16 v[16:19], v[104:107], v[232:235], v[16:19]\n"
    "buffer_load_dwordx4 v126, s[56:59], s78 offen offset:1056 lds\n"
    "v_mfma_f32_16x16x32_bf16 v[24:27], v[104:107], v[236:239], v[24:27]\n"
    "s_waitcnt lgkmcnt(3)\n"
    "v_add_f32 v83, v83, v61\n"
    "v_cvt_pk_bf16_f32 v58, v60, v61\n"
    "v_mfma_f32_16x16x32_bf16 v[0:3], v[108:111], v[176:179], v[0:3]\n"
    "ds_read_b128 v[96:99], v124 offset:17088\n"
    "v_mfma_f32_16x16x32_bf16 v[8:11], v[108:111], v[180:183], v[8:11]\n"
    "v_exp_f32 v38, v38\n"
    "v_mfma_f32_16x16x32_bf16 v[16:19], v[108:111], v[240:243], v[16:19]\n"
    "buffer_load_dwordx4 v126, s[56:59], s79 offen offset:2112 lds\n"
    "v_mfma_f32_16x16x32_bf16 v[24:27], v[108:111], v[244:247], v[24:27]\n"
    "v_add_f32 v80, v80, v38\n"
    "v_mfma_f32_16x16x32_bf16 v[0:3], v[112:115], v[184:187], v[0:3]\n"
    "ds_read_b128 v[100:103], v119 offset:17152\n"
    "v_mfma_f32_16x16x32_bf16 v[8:11], v[112:115], v[188:191], v[8:11]\n"
    "v_exp_f32 v39, v39\n"
    "v_mfma_f32_16x16x32_bf16 v[16:19], v[112:115], v[248:251], v[16:19]\n"
    "buffer_load_dwordx4 v126, s[56:59], s80 offen offset:3168 lds\n"
    "v_mfma_f32_16x16x32_bf16 v[24:27], v[112:115], v[252:255], v[24:27]\n"
    "s_waitcnt lgkmcnt(3)\n"
    "v_add_f32 v80, v80, v39\n"
    "v_cvt_pk_bf16_f32 v35, v38, v39\n"
    "v_mfma_f32_16x16x32_bf16 v[4:7], v[84:87], v[128:131], v[64:67]\n"
    "ds_read_b128 v[104:107], v119 offset:17216\n"
    "v_mfma_f32_16x16x32_bf16 v[12:15], v[84:87], v[132:135], v[68:71]\n"
    "s_add_u32 m0, s74, 71808\n"
    "v_exp_f32 v46, v46\n"
    "v_mfma_f32_16x16x32_bf16 v[20:23], v[84:87], v[192:195], v[72:75]\n"
    "buffer_load_dwordx4 v126, s[56:59], s81 offen offset:0 lds\n"
    "v_mfma_f32_16x16x32_bf16 v[28:31], v[84:87], v[196:199], v[76:79]\n"
    "v_add_f32 v81, v81, v46\n"
    "v_mfma_f32_16x16x32_bf16 v[4:7], v[88:91], v[136:139], v[4:7]\n"
    "ds_read_b128 v[108:111], v119 offset:17280\n"
    "v_mfma_f32_16x16x32_bf16 v[12:15], v[88:91], v[140:143], v[12:15]\n"
    "v_exp_f32 v47, v47\n"
    "v_mfma_f32_16x16x32_bf16 v[20:23], v[88:91], v[200:203], v[20:23]\n"
    "buffer_load_dwordx4 v126, s[56:59], s82 offen offset:1056 lds\n"
    "v_mfma_f32_16x16x32_bf16 v[28:31], v[88:91], v[204:207], v[28:31]\n"
    "s_waitcnt lgkmcnt(3)\n"
    "v_add_f32 v81, v81, v47\n"
    "v_cvt_pk_bf16_f32 v43, v46, v47\n"
    "v_mfma_f32_16x16x32_bf16 v[4:7], v[92:95], v[144:147], v[4:7]\n"
    "ds_read_b128 v[112:115], v119 offset:17344\n"
    "v_mfma_f32_16x16x32_bf16 v[12:15], v[92:95], v[148:151], v[12:15]\n"
    "v_exp_f32 v54, v54\n"
    "v_mfma_f32_16x16x32_bf16 v[20:23], v[92:95], v[208:211], v[20:23]\n"
    "buffer_load_dwordx4 v126, s[56:59], s83 offen offset:2112 lds\n"
    "v_mfma_f32_16x16x32_bf16 v[28:31], v[92:95], v[212:215], v[28:31]\n"
    "v_add_f32 v82, v82, v54\n"
    "v_mfma_f32_16x16x32_bf16 v[4:7], v[96:99], v[152:155], v[4:7]\n"
    "ds_read_b64_tr_b16 v[84:85], v125 offset:33792\n"
    "ds_read_b64_tr_b16 v[86:87], v125 offset:50688\n"
    "v_mfma_f32_16x16x32_bf16 v[12:15], v[96:99], v[156:159], v[12:15]\n"
    "v_exp_f32 v55, v55\n"
    "v_mfma_f32_16x16x32_bf16 v[20:23], v[96:99], v[216:219], v[20:23]\n"
    "buffer_load_dwordx4 v126, s[56:59], s84 offen offset:3168 lds\n"
    "v_mfma_f32_16x16x32_bf16 v[28:31], v[96:99], v[220:223], v[28:31]\n"
    "s_waitcnt lgkmcnt(4)\n"
    "v_add_f32 v82, v82, v55\n"
    "v_cvt_pk_bf16_f32 v51, v54, v55\n"
    "v_mfma_f32_16x16x32_bf16 v[4:7], v[100:103], v[160:163], v[4:7]\n"
    "s_sub_u32 s58, s58, 32768\n"
    "ds_read_b64_tr_b16 v[88:89], v125 offset:33824\n"
    "ds_read_b64_tr_b16 v[90:91], v125 offset:50720\n"
    "v_mfma_f32_16x16x32_bf16 v[12:15], v[100:103], v[164:167], v[12:15]\n"
    "v_exp_f32 v62, v62\n"
    "v_mfma_f32_16x16x32_bf16 v[20:23], v[100:103], v[224:227], v[20:23]\n"
    "buffer_load_dword v118, v116, s[52:55], 0 offen\n"
    "v_mfma_f32_16x16x32_bf16 v[28:31], v[100:103], v[228:231], v[28:31]\n"
    "s_add_u32 s56, s56, 32768\n"
    "s_addc_u32 s57, s57, 0\n"
    "v_add_f32 v83, v83, v62\n"
    "v_mfma_f32_16x16x32_bf16 v[4:7], v[104:107], v[168:171], v[4:7]\n"
    "ds_read_b64_tr_b16 v[92:93], v125 offset:33856\n"
    "ds_read_b64_tr_b16 v[94:95], v125 offset:50752\n"
    "v_mfma_f32_16x16x32_bf16 v[12:15], v[104:107], v[172:175], v[12:15]\n"
    "v_exp_f32 v63, v63\n"
    "v_mfma_f32_16x16x32_bf16 v[20:23], v[104:107], v[232:235], v[20:23]\n"
    "buffer_load_dword v118, v116, s[56:59], 0 offen\n"
    "v_mfma_f32_16x16x32_bf16 v[28:31], v[104:107], v[236:239], v[28:31]\n"
    "s_waitcnt lgkmcnt(6)\n"
    "v_add_f32 v83, v83, v63\n"
    "v_cvt_pk_bf16_f32 v59, v62, v63\n"
    "v_mfma_f32_16x16x32_bf16 v[4:7], v[108:111], v[176:179], v[4:7]\n"
    "ds_read_b64_tr_b16 v[96:97], v125 offset:33888\n"
    "ds_read_b64_tr_b16 v[98:99], v125 offset:50784\n"
    "v_mfma_f32_16x16x32_bf16 v[12:15], v[108:111], v[180:183], v[12:15]\n"
    "v_mfma_f32_16x16x32_bf16 v[20:23], v[108:111], v[240:243], v[20:23]\n"
    "v_mfma_f32_16x16x32_bf16 v[28:31], v[108:111], v[244:247], v[28:31]\n"
    "v_mfma_f32_16x16x32_bf16 v[4:7], v[112:115], v[184:187], v[4:7]\n"
    "ds_read_b64_tr_b16 v[100:101], v125 offset:33920\n"
    "ds_read_b64_tr_b16 v[102:103], v125 offset:50816\n"
    "v_mfma_f32_16x16x32_bf16 v[12:15], v[112:115], v[188:191], v[12:15]\n"
    "v_mfma_f32_16x16x32_bf16 v[20:23], v[112:115], v[248:251], v[20:23]\n"
    "v_mfma_f32_16x16x32_bf16 v[28:31], v[112:115], v[252:255], v[28:31]\n"
    "s_waitcnt lgkmcnt(6)\n"
    "v_mfma_f32_16x16x32_bf16 a[0:3], v[84:87], v[32:35], a[0:3]\n"
    "ds_read_b64_tr_b16 v[104:105], v125 offset:33952\n"
    "ds_read_b64_tr_b16 v[106:107], v125 offset:50848\n"
    "v_mfma_f32_16x16x32_bf16 a[4:7], v[84:87], v[40:43], a[4:7]\n"
    "v_exp_f32 v0, v0\n"
    "v_mfma_f32_16x16x32_bf16 a[128:131], v[84:87], v[48:51], a[128:131]\n"
    "v_add_f32 v80, v80, v0\n"
    "v_mfma_f32_16x16x32_bf16 a[132:135], v[84:87], v[56:59], a[132:135]\n"
    "v_mfma_f32_16x16x32_bf16 a[8:11], v[88:91], v[32:35], a[8:11]\n"
    "ds_read_b64_tr_b16 v[108:109], v125 offset:33984\n"
    "ds_read_b64_tr_b16 v[110:111], v125 offset:50880\n"
    "v_mfma_f32_16x16x32_bf16 a[12:15], v[88:91], v[40:43], a[12:15]\n"
    "v_exp_f32 v1, v1\n"
    "v_mfma_f32_16x16x32_bf16 a[136:139], v[88:91], v[48:51], a[136:139]\n"
    "v_add_f32 v80, v80, v1\n"
    "v_cvt_pk_bf16_f32 v0, v0, v1\n"
    "v_mfma_f32_16x16x32_bf16 a[140:143], v[88:91], v[56:59], a[140:143]\n"
    "s_waitcnt lgkmcnt(6)\n"
    "v_mfma_f32_16x16x32_bf16 a[16:19], v[92:95], v[32:35], a[16:19]\n"
    "ds_read_b64_tr_b16 v[112:113], v125 offset:34016\n"
    "ds_read_b64_tr_b16 v[114:115], v125 offset:50912\n"
    "v_mfma_f32_16x16x32_bf16 a[20:23], v[92:95], v[40:43], a[20:23]\n"
    "v_exp_f32 v8, v8\n"
    "v_mfma_f32_16x16x32_bf16 a[144:147], v[92:95], v[48:51], a[144:147]\n"
    "v_add_f32 v81, v81, v8\n"
    "v_mfma_f32_16x16x32_bf16 a[148:151], v[92:95], v[56:59], a[148:151]\n"
    "v_mfma_f32_16x16x32_bf16 a[24:27], v[96:99], v[32:35], a[24:27]\n"
    "ds_read_b64_tr_b16 v[84:85], v125 offset:34048\n"
    "ds_read_b64_tr_b16 v[86:87], v125 offset:50944\n"
    "v_mfma_f32_16x16x32_bf16 a[28:31], v[96:99], v[40:43], a[28:31]\n"
    "v_exp_f32 v9, v9\n"
    "v_mfma_f32_16x16x32_bf16 a[152:155], v[96:99], v[48:51], a[152:155]\n"
    "v_exp_f32 v16, v16\n"
    "v_mfma_f32_16x16x32_bf16 a[156:159], v[96:99], v[56:59], a[156:159]\n"
    "s_waitcnt lgkmcnt(6)\n"
    "v_add_f32 v81, v81, v9\n"
    "v_cvt_pk_bf16_f32 v8, v8, v9\n"
    "v_mfma_f32_16x16x32_bf16 a[32:35], v[100:103], v[32:35], a[32:35]\n"
    "ds_read_b64_tr_b16 v[88:89], v125 offset:34080\n"
    "ds_read_b64_tr_b16 v[90:91], v125 offset:50976\n"
    "v_mfma_f32_16x16x32_bf16 a[36:39], v[100:103], v[40:43], a[36:39]\n"
    "v_exp_f32 v17, v17\n"
    "v_mfma_f32_16x16x32_bf16 a[160:163], v[100:103], v[48:51], a[160:163]\n"
    "v_add_f32 v82, v82, v16\n"
    "v_add_f32 v82, v82, v17\n"
    "v_mfma_f32_16x16x32_bf16 a[164:167], v[100:103], v[56:59], a[164:167]\n"
    "v_cvt_pk_bf16_f32 v16, v16, v17\n"
    "v_mfma_f32_16x16x32_bf16 a[40:43], v[104:107], v[32:35], a[40:43]\n"
    "ds_read_b64_tr_b16 v[92:93], v125 offset:34112\n"
    "ds_read_b64_tr_b16 v[94:95], v125 offset:51008\n"
    "v_mfma_f32_16x16x32_bf16 a[44:47], v[104:107], v[40:43], a[44:47]\n"
    "v_exp_f32 v24, v24\n"
    "v_mfma_f32_16x16x32_bf16 a[168:171], v[104:107], v[48:51], a[168:171]\n"
    "v_add_f32 v83, v83, v24\n"
    "v_mfma_f32_16x16x32_bf16 a[172:175], v[104:107], v[56:59], a[172:175]\n"
    "s_waitcnt lgkmcnt(6)\n"
    "v_mfma_f32_16x16x32_bf16 a[48:51], v[108:111], v[32:35], a[48:51]\n"
    "ds_read_b64_tr_b16 v[96:97], v125 offset:34144\n"
    "ds_read_b64_tr_b16 v[98:99], v125 offset:51040\n"
    "v_mfma_f32_16x16x32_bf16 a[52:55], v[108:111], v[40:43], a[52:55]\n"
    "v_exp_f32 v25, v25\n"
    "v_mfma_f32_16x16x32_bf16 a[176:179], v[108:111], v[48:51], a[176:179]\n"
    "v_add_f32 v83, v83, v25\n"
    "v_cvt_pk_bf16_f32 v24, v24, v25\n"
    "v_mfma_f32_16x16x32_bf16 a[180:183], v[108:111], v[56:59], a[180:183]\n"
    "v_mfma_f32_16x16x32_bf16 a[56:59], v[112:115], v[32:35], a[56:59]\n"
    "ds_read_b64_tr_b16 v[100:101], v125 offset:34176\n"
    "ds_read_b64_tr_b16 v[102:103], v125 offset:51072\n"
    "v_mfma_f32_16x16x32_bf16 a[60:63], v[112:115], v[40:43], a[60:63]\n"
    "v_exp_f32 v2, v2\n"
    "v_mfma_f32_16x16x32_bf16 a[184:187], v[112:115], v[48:51], a[184:187]\n"
    "v_add_f32 v80, v80, v2\n"
    "v_mfma_f32_16x16x32_bf16 a[188:191], v[112:115], v[56:59], a[188:191]\n"
    "s_waitcnt lgkmcnt(6)\n"
    "v_mfma_f32_16x16x32_bf16 a[64:67], v[84:87], v[32:35], a[64:67]\n"
    "ds_read_b64_tr_b16 v[104:105], v125 offset:34208\n"
    "ds_read_b64_tr_b16 v[106:107], v125 offset:51104\n"
    "v_mfma_f32_16x16x32_bf16 a[68:71], v[84:87], v[40:43], a[68:71]\n"
    "v_exp_f32 v3, v3\n"
    "v_mfma_f32_16x16x32_bf16 a[192:195], v[84:87], v[48:51], a[192:195]\n"
    "v_exp_f32 v10, v10\n"
    "v_mfma_f32_16x16x32_bf16 a[196:199], v[84:87], v[56:59], a[196:199]\n"
    "v_add_f32 v80, v80, v3\n"
    "v_cvt_pk_bf16_f32 v1, v2, v3\n"
    "v_mfma_f32_16x16x32_bf16 a[72:75], v[88:91], v[32:35], a[72:75]\n"
    "ds_read_b64_tr_b16 v[108:109], v125 offset:34240\n"
    "ds_read_b64_tr_b16 v[110:111], v125 offset:51136\n"
    "v_mfma_f32_16x16x32_bf16 a[76:79], v[88:91], v[40:43], a[76:79]\n"
    "v_exp_f32 v11, v11\n"
    "v_mfma_f32_16x16x32_bf16 a[200:203], v[88:91], v[48:51], a[200:203]\n"
    "v_add_f32 v81, v81, v10\n"
    "v_add_f32 v81, v81, v11\n"
    "v_mfma_f32_16x16x32_bf16 a[204:207], v[88:91], v[56:59], a[204:207]\n"
    "s_waitcnt lgkmcnt(6)\n"
    "v_cvt_pk_bf16_f32 v9, v10, v11\n"
    "v_mfma_f32_16x16x32_bf16 a[80:83], v[92:95], v[32:35], a[80:83]\n"
    "ds_read_b64_tr_b16 v[112:113], v125 offset:34272\n"
    "ds_read_b64_tr_b16 v[114:115], v125 offset:51168\n"
    "v_mfma_f32_16x16x32_bf16 a[84:87], v[92:95], v[40:43], a[84:87]\n"
    "v_exp_f32 v18, v18\n"
    "v_mfma_f32_16x16x32_bf16 a[208:211], v[92:95], v[48:51], a[208:211]\n"
    "v_add_f32 v82, v82, v18\n"
    "v_mfma_f32_16x16x32_bf16 a[212:215], v[92:95], v[56:59], a[212:215]\n"
    "v_mfma_f32_16x16x32_bf16 a[88:91], v[96:99], v[32:35], a[88:91]\n"
    "v_mfma_f32_16x16x32_bf16 a[92:95], v[96:99], v[40:43], a[92:95]\n"
    "v_exp_f32 v19, v19\n"
    "v_mfma_f32_16x16x32_bf16 a[216:219], v[96:99], v[48:51], a[216:219]\n"
    "v_add_f32 v82, v82, v19\n"
    "v_cvt_pk_bf16_f32 v17, v18, v19\n"
    "v_mfma_f32_16x16x32_bf16 a[220:223], v[96:99], v[56:59], a[220:223]\n"
    "s_waitcnt vmcnt(2) lgkmcnt(0)\n"
    "s_barrier\n"
    "v_mfma_f32_16x16x32_bf16 a[96:99], v[100:103], v[32:35], a[96:99]\n"
    "ds_read_b128 v[84:87], v124 offset:33792\n"
    "ds_read_b128 v[88:91], v124 offset:33856\n"
    "v_mfma_f32_16x16x32_bf16 a[100:103], v[100:103], v[40:43], a[100:103]\n"
    "s_add_u32 m0, s74, 101376\n"
    "v_exp_f32 v26, v26\n"
    "v_mfma_f32_16x16x32_bf16 a[224:227], v[100:103], v[48:51], a[224:227]\n"
    "buffer_load_dwordx4 v126, s[56:59], s77 offen offset:0 lds\n"
    "v_mfma_f32_16x16x32_bf16 a[228:231], v[100:103], v[56:59], a[228:231]\n"
    "v_add_f32 v83, v83, v26\n"
    "v_mfma_f32_16x16x32_bf16 a[104:107], v[104:107], v[32:35], a[104:107]\n"
    "ds_read_b128 v[92:95], v124 offset:33920\n"
    "v_mfma_f32_16x16x32_bf16 a[108:111], v[104:107], v[40:43], a[108:111]\n"
    "v_exp_f32 v27, v27\n"
    "v_mfma_f32_16x16x32_bf16 a[232:235], v[104:107], v[48:51], a[232:235]\n"
    "buffer_load_dwordx4 v126, s[56:59], s78 offen offset:1056 lds\n"
    "v_mfma_f32_16x16x32_bf16 a[236:239], v[104:107], v[56:59], a[236:239]\n"
    "v_add_f32 v83, v83, v27\n"
    "v_cvt_pk_bf16_f32 v25, v26, v27\n"
    "v_mfma_f32_16x16x32_bf16 a[112:115], v[108:111], v[32:35], a[112:115]\n"
    "ds_read_b128 v[96:99], v124 offset:33984\n"
    "v_mfma_f32_16x16x32_bf16 a[116:119], v[108:111], v[40:43], a[116:119]\n"
    "v_exp_f32 v4, v4\n"
    "v_mfma_f32_16x16x32_bf16 a[240:243], v[108:111], v[48:51], a[240:243]\n"
    "buffer_load_dwordx4 v126, s[56:59], s79 offen offset:2112 lds\n"
    "v_mfma_f32_16x16x32_bf16 a[244:247], v[108:111], v[56:59], a[244:247]\n"
    "v_add_f32 v80, v80, v4\n"
    "v_mfma_f32_16x16x32_bf16 a[120:123], v[112:115], v[32:35], a[120:123]\n"
    "ds_read_b128 v[100:103], v119 offset:34048\n"
    "v_mfma_f32_16x16x32_bf16 a[124:127], v[112:115], v[40:43], a[124:127]\n"
    "v_exp_f32 v5, v5\n"
    "v_mfma_f32_16x16x32_bf16 a[248:251], v[112:115], v[48:51], a[248:251]\n"
    "buffer_load_dwordx4 v126, s[56:59], s80 offen offset:3168 lds\n"
    "v_mfma_f32_16x16x32_bf16 a[252:255], v[112:115], v[56:59], a[252:255]\n"
    "s_waitcnt lgkmcnt(3)\n"
    "v_add_f32 v80, v80, v5\n"
    "v_cvt_pk_bf16_f32 v2, v4, v5\n"
    "v_mfma_f32_16x16x32_bf16 v[32:35], v[84:87], v[128:131], v[64:67]\n"
    "ds_read_b128 v[104:107], v119 offset:34112\n"
    "v_mfma_f32_16x16x32_bf16 v[40:43], v[84:87], v[132:135], v[68:71]\n"
    "s_add_u32 m0, s74, 105600\n"
    "v_exp_f32 v12, v12\n"
    "v_mfma_f32_16x16x32_bf16 v[48:51], v[84:87], v[192:195], v[72:75]\n"
    "buffer_load_dwordx4 v126, s[56:59], s81 offen offset:0 lds\n"
    "v_mfma_f32_16x16x32_bf16 v[56:59], v[84:87], v[196:199], v[76:79]\n"
    "v_add_f32 v81, v81, v12\n"
    "v_mfma_f32_16x16x32_bf16 v[32:35], v[88:91], v[136:139], v[32:35]\n"
    "ds_read_b128 v[108:111], v119 offset:34176\n"
    "v_mfma_f32_16x16x32_bf16 v[40:43], v[88:91], v[140:143], v[40:43]\n"
    "v_exp_f32 v13, v13\n"
    "v_mfma_f32_16x16x32_bf16 v[48:51], v[88:91], v[200:203], v[48:51]\n"
    "buffer_load_dwordx4 v126, s[56:59], s82 offen offset:1056 lds\n"
    "v_mfma_f32_16x16x32_bf16 v[56:59], v[88:91], v[204:207], v[56:59]\n"
    "s_waitcnt lgkmcnt(3)\n"
    "v_add_f32 v81, v81, v13\n"
    "v_cvt_pk_bf16_f32 v10, v12, v13\n"
    "v_mfma_f32_16x16x32_bf16 v[32:35], v[92:95], v[144:147], v[32:35]\n"
    "ds_read_b128 v[112:115], v119 offset:34240\n"
    "v_mfma_f32_16x16x32_bf16 v[40:43], v[92:95], v[148:151], v[40:43]\n"
    "v_exp_f32 v20, v20\n"
    "v_mfma_f32_16x16x32_bf16 v[48:51], v[92:95], v[208:211], v[48:51]\n"
    "buffer_load_dwordx4 v126, s[56:59], s83 offen offset:2112 lds\n"
    "v_mfma_f32_16x16x32_bf16 v[56:59], v[92:95], v[212:215], v[56:59]\n"
    "v_add_f32 v82, v82, v20\n"
    "v_mfma_f32_16x16x32_bf16 v[32:35], v[96:99], v[152:155], v[32:35]\n"
    "ds_read_b128 v[84:87], v124 offset:50688\n"
    "v_mfma_f32_16x16x32_bf16 v[40:43], v[96:99], v[156:159], v[40:43]\n"
    "v_exp_f32 v21, v21\n"
    "v_mfma_f32_16x16x32_bf16 v[48:51], v[96:99], v[216:219], v[48:51]\n"
    "buffer_load_dwordx4 v126, s[56:59], s84 offen offset:3168 lds\n"
    "v_mfma_f32_16x16x32_bf16 v[56:59], v[96:99], v[220:223], v[56:59]\n"
    "s_waitcnt lgkmcnt(3)\n"
    "v_add_f32 v82, v82, v21\n"
    "v_cvt_pk_bf16_f32 v18, v20, v21\n"
    "v_mfma_f32_16x16x32_bf16 v[32:35], v[100:103], v[160:163], v[32:35]\n"
    "s_sub_u32 s58, s58, 32768\n"
    "ds_read_b128 v[88:91], v124 offset:50752\n"
    "v_mfma_f32_16x16x32_bf16 v[40:43], v[100:103], v[164:167], v[40:43]\n"
    "v_exp_f32 v28, v28\n"
    "v_mfma_f32_16x16x32_bf16 v[48:51], v[100:103], v[224:227], v[48:51]\n"
    "s_add_u32 s56, s56, 32768\n"
    "s_addc_u32 s57, s57, 0\n"
    "v_add_f32 v83, v83, v28\n"
    "v_mfma_f32_16x16x32_bf16 v[56:59], v[100:103], v[228:231], v[56:59]\n"
    "v_mfma_f32_16x16x32_bf16 v[32:35], v[104:107], v[168:171], v[32:35]\n"
    "ds_read_b128 v[92:95], v124 offset:50816\n"
    "v_mfma_f32_16x16x32_bf16 v[40:43], v[104:107], v[172:175], v[40:43]\n"
    "v_exp_f32 v29, v29\n"
    "v_mfma_f32_16x16x32_bf16 v[48:51], v[104:107], v[232:235], v[48:51]\n"
    "v_add_f32 v83, v83, v29\n"
    "v_cvt_pk_bf16_f32 v26, v28, v29\n"
    "v_mfma_f32_16x16x32_bf16 v[56:59], v[104:107], v[236:239], v[56:59]\n"
    "s_waitcnt lgkmcnt(3)\n"
    "v_mfma_f32_16x16x32_bf16 v[32:35], v[108:111], v[176:179], v[32:35]\n"
    "ds_read_b128 v[96:99], v124 offset:50880\n"
    "v_mfma_f32_16x16x32_bf16 v[40:43], v[108:111], v[180:183], v[40:43]\n"
    "v_exp_f32 v6, v6\n"
    "v_mfma_f32_16x16x32_bf16 v[48:51], v[108:111], v[240:243], v[48:51]\n"
    "v_add_f32 v80, v80, v6\n"
    "v_mfma_f32_16x16x32_bf16 v[56:59], v[108:111], v[244:247], v[56:59]\n"
    "v_mfma_f32_16x16x32_bf16 v[32:35], v[112:115], v[184:187], v[32:35]\n"
    "ds_read_b128 v[100:103], v119 offset:50944\n"
    "v_mfma_f32_16x16x32_bf16 v[40:43], v[112:115], v[188:191], v[40:43]\n"
    "v_exp_f32 v7, v7\n"
    "v_mfma_f32_16x16x32_bf16 v[48:51], v[112:115], v[248:251], v[48:51]\n"
    "v_add_f32 v80, v80, v7\n"
    "v_cvt_pk_bf16_f32 v3, v6, v7\n"
    "v_mfma_f32_16x16x32_bf16 v[56:59], v[112:115], v[252:255], v[56:59]\n"
    "s_waitcnt lgkmcnt(3)\n"
    "v_mfma_f32_16x16x32_bf16 v[36:39], v[84:87], v[128:131], v[64:67]\n"
    "ds_read_b128 v[104:107], v119 offset:51008\n"
    "v_mfma_f32_16x16x32_bf16 v[44:47], v[84:87], v[132:135], v[68:71]\n"
    "v_exp_f32 v14, v14\n"
    "v_mfma_f32_16x16x32_bf16 v[52:55], v[84:87], v[192:195], v[72:75]\n"
    "v_add_f32 v81, v81, v14\n"
    "v_mfma_f32_16x16x32_bf16 v[60:63], v[84:87], v[196:199], v[76:79]\n"
    "v_mfma_f32_16x16x32_bf16 v[36:39], v[88:91], v[136:139], v[36:39]\n"
    "ds_read_b128 v[108:111], v119 offset:51072\n"
    "v_mfma_f32_16x16x32_bf16 v[44:47], v[88:91], v[140:143], v[44:47]\n"
    "v_exp_f32 v15, v15\n"
    "v_mfma_f32_16x16x32_bf16 v[52:55], v[88:91], v[200:203], v[52:55]\n"
    "v_add_f32 v81, v81, v15\n"
    "v_cvt_pk_bf16_f32 v11, v14, v15\n"
    "v_mfma_f32_16x16x32_bf16 v[60:63], v[88:91], v[204:207], v[60:63]\n"
    "s_waitcnt lgkmcnt(3)\n"
    "v_mfma_f32_16x16x32_bf16 v[36:39], v[92:95], v[144:147], v[36:39]\n"
    "ds_read_b128 v[112:115], v119 offset:51136\n"
    "v_mfma_f32_16x16x32_bf16 v[44:47], v[92:95], v[148:151], v[44:47]\n"
    "v_exp_f32 v22, v22\n"
    "v_mfma_f32_16x16x32_bf16 v[52:55], v[92:95], v[208:211], v[52:55]\n"
    "v_add_f32 v82, v82, v22\n"
    "v_mfma_f32_16x16x32_bf16 v[60:63], v[92:95], v[212:215], v[60:63]\n"
    "v_mfma_f32_16x16x32_bf16 v[36:39], v[96:99], v[152:155], v[36:39]\n"
    "ds_read_b64_tr_b16 v[84:85], v125 offset:0\n"
    "ds_read_b64_tr_b16 v[86:87], v125 offset:16896\n"
    "v_mfma_f32_16x16x32_bf16 v[44:47], v[96:99], v[156:159], v[44:47]\n"
    "v_exp_f32 v23, v23\n"
    "v_mfma_f32_16x16x32_bf16 v[52:55], v[96:99], v[216:219], v[52:55]\n"
    "v_add_f32 v82, v82, v23\n"
    "v_cvt_pk_bf16_f32 v19, v22, v23\n"
    "v_mfma_f32_16x16x32_bf16 v[60:63], v[96:99], v[220:223], v[60:63]\n"
    "s_waitcnt lgkmcnt(4)\n"
    "v_mfma_f32_16x16x32_bf16 v[36:39], v[100:103], v[160:163], v[36:39]\n"
    "ds_read_b64_tr_b16 v[88:89], v125 offset:32\n"
    "ds_read_b64_tr_b16 v[90:91], v125 offset:16928\n"
    "v_mfma_f32_16x16x32_bf16 v[44:47], v[100:103], v[164:167], v[44:47]\n"
    "v_exp_f32 v30, v30\n"
    "v_mfma_f32_16x16x32_bf16 v[52:55], v[100:103], v[224:227], v[52:55]\n"
    "v_add_f32 v83, v83, v30\n"
    "v_mfma_f32_16x16x32_bf16 v[60:63], v[100:103], v[228:231], v[60:63]\n"
    "v_mfma_f32_16x16x32_bf16 v[36:39], v[104:107], v[168:171], v[36:39]\n"
    "ds_read_b64_tr_b16 v[92:93], v125 offset:64\n"
    "ds_read_b64_tr_b16 v[94:95], v125 offset:16960\n"
    "v_mfma_f32_16x16x32_bf16 v[44:47], v[104:107], v[172:175], v[44:47]\n"
    "v_exp_f32 v31, v31\n"
    "v_mfma_f32_16x16x32_bf16 v[52:55], v[104:107], v[232:235], v[52:55]\n"
    "v_add_f32 v83, v83, v31\n"
    "v_cvt_pk_bf16_f32 v27, v30, v31\n"
    "v_mfma_f32_16x16x32_bf16 v[60:63], v[104:107], v[236:239], v[60:63]\n"
    "s_waitcnt lgkmcnt(6)\n"
    "v_mfma_f32_16x16x32_bf16 v[36:39], v[108:111], v[176:179], v[36:39]\n"
    "ds_read_b64_tr_b16 v[96:97], v125 offset:96\n"
    "ds_read_b64_tr_b16 v[98:99], v125 offset:16992\n"
    "v_mfma_f32_16x16x32_bf16 v[44:47], v[108:111], v[180:183], v[44:47]\n"
    "v_mfma_f32_16x16x32_bf16 v[52:55], v[108:111], v[240:243], v[52:55]\n"
    "v_mfma_f32_16x16x32_bf16 v[60:63], v[108:111], v[244:247], v[60:63]\n"
    "v_mfma_f32_16x16x32_bf16 v[36:39], v[112:115], v[184:187], v[36:39]\n"
    "ds_read_b64_tr_b16 v[100:101], v125 offset:128\n"
    "ds_read_b64_tr_b16 v[102:103], v125 offset:17024\n"
    "v_mfma_f32_16x16x32_bf16 v[44:47], v[112:115], v[188:191], v[44:47]\n"
    "v_mfma_f32_16x16x32_bf16 v[52:55], v[112:115], v[248:251], v[52:55]\n"
    "v_mfma_f32_16x16x32_bf16 v[60:63], v[112:115], v[252:255], v[60:63]\n"
    "s_waitcnt lgkmcnt(6)\n"
    ".Ltail:\n"
    "v_mbcnt_lo_u32_b32 v127, -1, 0\n"
    "v_mbcnt_hi_u32_b32 v127, -1, v127\n"
    "v_lshrrev_b32 v120, 3, v127\n"
    "v_mul_u32_u24 v64, 896, v120\n"
    "v_bfe_u32 v120, v127, 4, 2\n"
    "v_lshlrev_b32 v120, 1, v120\n"
    "v_and_b32 v121, 7, v127\n"
    "v_xor_b32 v120, v120, v121\n"
    "v_lshl_add_u32 v64, v120, 4, v64\n"
    "s_mul_i32 s86, s69, 7168\n"
    "s_add_u32 s86, s86, 3168\n"
    "v_add_u32 v64, s86, v64\n"
    "v_add_u32 v65, 0xfffffc80, v64\n"
    "v_add_u32 v66, 0xfffff900, v64\n"
    "v_add_u32 v67, 0xfffff580, v64\n"
    "v_add_u32 v68, 0x200, v64\n"
    "v_add_u32 v69, 0xfffffe80, v64\n"
    "v_add_u32 v70, 0xfffffb00, v64\n"
    "s_mul_i32 s100, s69, 7168\n"
    "v_mbcnt_lo_u32_b32 v76, -1, 0\n"
    "v_mbcnt_hi_u32_b32 v76, -1, v76\n"
    "v_and_b32 v76, 7, v76\n"
    "v_lshlrev_b32 v76, 2, v76\n"
    "s_lshl_b32 s86, s71, 8\n"
    "v_add_u32 v76, s86, v76\n"
    "global_load_dword v78, v76, s[36:37] offset:128 sc1\n"
    "v_mov_b32 v79, v117\n"
    "v_mfma_f32_16x16x32_bf16 a[0:3], v[84:87], v[0:3], a[0:3]\n"
    "ds_read_b64_tr_b16 v[104:105], v125 offset:160\n"
    "ds_read_b64_tr_b16 v[106:107], v125 offset:17056\n"
    "v_mfma_f32_16x16x32_bf16 a[4:7], v[84:87], v[8:11], a[4:7]\n"
    "v_exp_f32 v32, v32\n"
    "v_mfma_f32_16x16x32_bf16 a[128:131], v[84:87], v[16:19], a[128:131]\n"
    "v_add_f32 v80, v80, v32\n"
    "v_mfma_f32_16x16x32_bf16 a[132:135], v[84:87], v[24:27], a[132:135]\n"
    "v_mfma_f32_16x16x32_bf16 a[8:11], v[88:91], v[0:3], a[8:11]\n"
    "ds_read_b64_tr_b16 v[108:109], v125 offset:192\n"
    "ds_read_b64_tr_b16 v[110:111], v125 offset:17088\n"
    "v_mfma_f32_16x16x32_bf16 a[12:15], v[88:91], v[8:11], a[12:15]\n"
    "v_exp_f32 v33, v33\n"
    "v_mfma_f32_16x16x32_bf16 a[136:139], v[88:91], v[16:19], a[136:139]\n"
    "v_add_f32 v80, v80, v33\n"
    "v_cvt_pk_bf16_f32 v32, v32, v33\n"
    "v_mfma_f32_16x16x32_bf16 a[140:143], v[88:91], v[24:27], a[140:143]\n"
    "s_waitcnt lgkmcnt(6)\n"
    "v_mfma_f32_16x16x32_bf16 a[16:19], v[92:95], v[0:3], a[16:19]\n"
    "ds_read_b64_tr_b16 v[112:113], v125 offset:224\n"
    "ds_read_b64_tr_b16 v[114:115], v125 offset:17120\n"
    "v_mfma_f32_16x16x32_bf16 a[20:23], v[92:95], v[8:11], a[20:23]\n"
    "v_exp_f32 v40, v40\n"
    "v_mfma_f32_16x16x32_bf16 a[144:147], v[92:95], v[16:19], a[144:147]\n"
    "v_add_f32 v81, v81, v40\n"
    "v_mfma_f32_16x16x32_bf16 a[148:151], v[92:95], v[24:27], a[148:151]\n"
    "v_mfma_f32_16x16x32_bf16 a[24:27], v[96:99], v[0:3], a[24:27]\n"
    "ds_read_b64_tr_b16 v[84:85], v125 offset:256\n"
    "ds_read_b64_tr_b16 v[86:87], v125 offset:17152\n"
    "v_mfma_f32_16x16x32_bf16 a[28:31], v[96:99], v[8:11], a[28:31]\n"
    "v_exp_f32 v41, v41\n"
    "v_mfma_f32_16x16x32_bf16 a[152:155], v[96:99], v[16:19], a[152:155]\n"
    "v_exp_f32 v48, v48\n"
    "v_mfma_f32_16x16x32_bf16 a[156:159], v[96:99], v[24:27], a[156:159]\n"
    "s_waitcnt lgkmcnt(6)\n"
    "v_add_f32 v81, v81, v41\n"
    "v_cvt_pk_bf16_f32 v40, v40, v41\n"
    "v_mfma_f32_16x16x32_bf16 a[32:35], v[100:103], v[0:3], a[32:35]\n"
    "ds_read_b64_tr_b16 v[88:89], v125 offset:288\n"
    "ds_read_b64_tr_b16 v[90:91], v125 offset:17184\n"
    "v_mfma_f32_16x16x32_bf16 a[36:39], v[100:103], v[8:11], a[36:39]\n"
    "v_exp_f32 v49, v49\n"
    "v_mfma_f32_16x16x32_bf16 a[160:163], v[100:103], v[16:19], a[160:163]\n"
    "v_add_f32 v82, v82, v48\n"
    "v_add_f32 v82, v82, v49\n"
    "v_mfma_f32_16x16x32_bf16 a[164:167], v[100:103], v[24:27], a[164:167]\n"
    "v_cvt_pk_bf16_f32 v48, v48, v49\n"
    "v_mfma_f32_16x16x32_bf16 a[40:43], v[104:107], v[0:3], a[40:43]\n"
    "ds_read_b64_tr_b16 v[92:93], v125 offset:320\n"
    "ds_read_b64_tr_b16 v[94:95], v125 offset:17216\n"
    "v_mfma_f32_16x16x32_bf16 a[44:47], v[104:107], v[8:11], a[44:47]\n"
    "v_exp_f32 v56, v56\n"
    "v_mfma_f32_16x16x32_bf16 a[168:171], v[104:107], v[16:19], a[168:171]\n"
    "v_add_f32 v83, v83, v56\n"
    "v_mfma_f32_16x16x32_bf16 a[172:175], v[104:107], v[24:27], a[172:175]\n"
    "s_waitcnt lgkmcnt(6)\n"
    "v_mfma_f32_16x16x32_bf16 a[48:51], v[108:111], v[0:3], a[48:51]\n"
    "ds_read_b64_tr_b16 v[96:97], v125 offset:352\n"
    "ds_read_b64_tr_b16 v[98:99], v125 offset:17248\n"
    "v_mfma_f32_16x16x32_bf16 a[52:55], v[108:111], v[8:11], a[52:55]\n"
    "v_exp_f32 v57, v57\n"
    "v_mfma_f32_16x16x32_bf16 a[176:179], v[108:111], v[16:19], a[176:179]\n"
    "v_add_f32 v83, v83, v57\n"
    "v_cvt_pk_bf16_f32 v56, v56, v57\n"
    "v_mfma_f32_16x16x32_bf16 a[180:183], v[108:111], v[24:27], a[180:183]\n"
    "v_mfma_f32_16x16x32_bf16 a[56:59], v[112:115], v[0:3], a[56:59]\n"
    "ds_read_b64_tr_b16 v[100:101], v125 offset:384\n"
    "ds_read_b64_tr_b16 v[102:103], v125 offset:17280\n"
    "v_mfma_f32_16x16x32_bf16 a[60:63], v[112:115], v[8:11], a[60:63]\n"
    "v_exp_f32 v34, v34\n"
    "v_mfma_f32_16x16x32_bf16 a[184:187], v[112:115], v[16:19], a[184:187]\n"
    "v_add_f32 v80, v80, v34\n"
    "v_mfma_f32_16x16x32_bf16 a[188:191], v[112:115], v[24:27], a[188:191]\n"
    "s_waitcnt lgkmcnt(6)\n"
    "v_mfma_f32_16x16x32_bf16 a[64:67], v[84:87], v[0:3], a[64:67]\n"
    "ds_read_b64_tr_b16 v[104:105], v125 offset:416\n"
    "ds_read_b64_tr_b16 v[106:107], v125 offset:17312\n"
    "v_mfma_f32_16x16x32_bf16 a[68:71], v[84:87], v[8:11], a[68:71]\n"
    "v_exp_f32 v35, v35\n"
    "v_mfma_f32_16x16x32_bf16 a[192:195], v[84:87], v[16:19], a[192:195]\n"
    "v_exp_f32 v42, v42\n"
    "v_mfma_f32_16x16x32_bf16 a[196:199], v[84:87], v[24:27], a[196:199]\n"
    "v_add_f32 v80, v80, v35\n"
    "v_cvt_pk_bf16_f32 v33, v34, v35\n"
    "v_mfma_f32_16x16x32_bf16 a[72:75], v[88:91], v[0:3], a[72:75]\n"
    "ds_read_b64_tr_b16 v[108:109], v125 offset:448\n"
    "ds_read_b64_tr_b16 v[110:111], v125 offset:17344\n"
    "v_mfma_f32_16x16x32_bf16 a[76:79], v[88:91], v[8:11], a[76:79]\n"
    "v_exp_f32 v43, v43\n"
    "v_mfma_f32_16x16x32_bf16 a[200:203], v[88:91], v[16:19], a[200:203]\n"
    "v_add_f32 v81, v81, v42\n"
    "v_add_f32 v81, v81, v43\n"
    "v_mfma_f32_16x16x32_bf16 a[204:207], v[88:91], v[24:27], a[204:207]\n"
    "s_waitcnt lgkmcnt(6)\n"
    "v_cvt_pk_bf16_f32 v41, v42, v43\n"
    "v_mfma_f32_16x16x32_bf16 a[80:83], v[92:95], v[0:3], a[80:83]\n"
    "ds_read_b64_tr_b16 v[112:113], v125 offset:480\n"
    "ds_read_b64_tr_b16 v[114:115], v125 offset:17376\n"
    "v_mfma_f32_16x16x32_bf16 a[84:87], v[92:95], v[8:11], a[84:87]\n"
    "v_exp_f32 v50, v50\n"
    "v_mfma_f32_16x16x32_bf16 a[208:211], v[92:95], v[16:19], a[208:211]\n"
    "v_add_f32 v82, v82, v50\n"
    "v_mfma_f32_16x16x32_bf16 a[212:215], v[92:95], v[24:27], a[212:215]\n"
    "v_mfma_f32_16x16x32_bf16 a[88:91], v[96:99], v[0:3], a[88:91]\n"
    "v_mfma_f32_16x16x32_bf16 a[92:95], v[96:99], v[8:11], a[92:95]\n"
    "v_exp_f32 v51, v51\n"
    "v_mfma_f32_16x16x32_bf16 a[216:219], v[96:99], v[16:19], a[216:219]\n"
    "v_add_f32 v82, v82, v51\n"
    "v_cvt_pk_bf16_f32 v49, v50, v51\n"
    "v_mfma_f32_16x16x32_bf16 a[220:223], v[96:99], v[24:27], a[220:223]\n"
    "s_waitcnt vmcnt(1) lgkmcnt(0)\n"
    "s_barrier\n"
    "v_mfma_f32_16x16x32_bf16 a[96:99], v[100:103], v[0:3], a[96:99]\n"
    "v_mfma_f32_16x16x32_bf16 a[100:103], v[100:103], v[8:11], a[100:103]\n"
    "s_add_u32 m0, s100, 0\n"
    "s_mov_b32 s75, 0\n"
    "v_exp_f32 v58, v58\n"
    "v_mfma_f32_16x16x32_bf16 a[224:227], v[100:103], v[16:19], a[224:227]\n"
    "buffer_load_dwordx4 v64, s[60:63], s75 offen offset:0 lds\n"
    "v_mfma_f32_16x16x32_bf16 a[228:231], v[100:103], v[24:27], a[228:231]\n"
    "v_add_f32 v83, v83, v58\n"
    "v_mfma_f32_16x16x32_bf16 a[104:107], v[104:107], v[0:3], a[104:107]\n"
    "v_mfma_f32_16x16x32_bf16 a[108:111], v[104:107], v[8:11], a[108:111]\n"
    "v_exp_f32 v59, v59\n"
    "v_mfma_f32_16x16x32_bf16 a[232:235], v[104:107], v[16:19], a[232:235]\n"
    "buffer_load_dwordx4 v65, s[60:63], s75 offen offset:1024 lds\n"
    "v_mfma_f32_16x16x32_bf16 a[236:239], v[104:107], v[24:27], a[236:239]\n"
    "v_add_f32 v83, v83, v59\n"
    "v_cvt_pk_bf16_f32 v57, v58, v59\n"
    "v_mfma_f32_16x16x32_bf16 a[112:115], v[108:111], v[0:3], a[112:115]\n"
    "v_mfma_f32_16x16x32_bf16 a[116:119], v[108:111], v[8:11], a[116:119]\n"
    "v_exp_f32 v36, v36\n"
    "v_mfma_f32_16x16x32_bf16 a[240:243], v[108:111], v[16:19], a[240:243]\n"
    "buffer_load_dwordx4 v66, s[60:63], s75 offen offset:2048 lds\n"
    "v_mfma_f32_16x16x32_bf16 a[244:247], v[108:111], v[24:27], a[244:247]\n"
    "v_add_f32 v80, v80, v36\n"
    "v_mfma_f32_16x16x32_bf16 a[120:123], v[112:115], v[0:3], a[120:123]\n"
    "v_mfma_f32_16x16x32_bf16 a[124:127], v[112:115], v[8:11], a[124:127]\n"
    "v_exp_f32 v37, v37\n"
    "v_mfma_f32_16x16x32_bf16 a[248:251], v[112:115], v[16:19], a[248:251]\n"
    "buffer_load_dwordx4 v67, s[60:63], s75 offen offset:3072 lds\n"
    "v_mfma_f32_16x16x32_bf16 a[252:255], v[112:115], v[24:27], a[252:255]\n"
    "v_add_f32 v80, v80, v37\n"
    "v_cvt_pk_bf16_f32 v34, v36, v37\n"
    "v_exp_f32 v44, v44\n"
    "s_nop 0\n"
    "v_add_f32 v81, v81, v44\n"
    "v_exp_f32 v45, v45\n"
    "s_nop 0\n"
    "v_add_f32 v81, v81, v45\n"
    "v_cvt_pk_bf16_f32 v42, v44, v45\n"
    "v_exp_f32 v52, v52\n"
    "s_nop 0\n"
    "v_add_f32 v82, v82, v52\n"
    "v_exp_f32 v53, v53\n"
    "s_nop 0\n"
    "v_add_f32 v82, v82, v53\n"
    "v_cvt_pk_bf16_f32 v50, v52, v53\n"
    "v_exp_f32 v60, v60\n"
    "s_nop 0\n"
    "v_add_f32 v83, v83, v60\n"
    "v_exp_f32 v61, v61\n"
    "s_nop 0\n"
    "v_add_f32 v83, v83, v61\n"
    "v_cvt_pk_bf16_f32 v58, v60, v61\n"
    "v_exp_f32 v38, v38\n"
    "s_nop 0\n"
    "v_add_f32 v80, v80, v38\n"
    "v_exp_f32 v39, v39\n"
    "s_nop 0\n"
    "v_add_f32 v80, v80, v39\n"
    "v_cvt_pk_bf16_f32 v35, v38, v39\n"
    "v_exp_f32 v46, v46\n"
    "s_nop 0\n"
    "v_add_f32 v81, v81, v46\n"
    "v_exp_f32 v47, v47\n"
    "s_nop 0\n"
    "v_add_f32 v81, v81, v47\n"
    "v_cvt_pk_bf16_f32 v43, v46, v47\n"
    "v_exp_f32 v54, v54\n"
    "s_nop 0\n"
    "v_add_f32 v82, v82, v54\n"
    "ds_read_b64_tr_b16 v[84:85], v125 offset:33792\n"
    "ds_read_b64_tr_b16 v[86:87], v125 offset:50688\n"
    "v_exp_f32 v55, v55\n"
    "s_nop 0\n"
    "v_add_f32 v82, v82, v55\n"
    "v_cvt_pk_bf16_f32 v51, v54, v55\n"
    "ds_read_b64_tr_b16 v[88:89], v125 offset:33824\n"
    "ds_read_b64_tr_b16 v[90:91], v125 offset:50720\n"
    "v_exp_f32 v62, v62\n"
    "s_nop 0\n"
    "v_add_f32 v83, v83, v62\n"
    "ds_read_b64_tr_b16 v[92:93], v125 offset:33856\n"
    "ds_read_b64_tr_b16 v[94:95], v125 offset:50752\n"
    "v_exp_f32 v63, v63\n"
    "s_nop 0\n"
    "v_add_f32 v83, v83, v63\n"
    "v_cvt_pk_bf16_f32 v59, v62, v63\n"
    "ds_read_b64_tr_b16 v[96:97], v125 offset:33888\n"
    "ds_read_b64_tr_b16 v[98:99], v125 offset:50784\n"
    "ds_read_b64_tr_b16 v[100:101], v125 offset:33920\n"
    "ds_read_b64_tr_b16 v[102:103], v125 offset:50816\n"
    "s_waitcnt lgkmcnt(6)\n"
    "v_mfma_f32_16x16x32_bf16 a[0:3], v[84:87], v[32:35], a[0:3]\n"
    "ds_read_b64_tr_b16 v[104:105], v125 offset:33952\n"
    "ds_read_b64_tr_b16 v[106:107], v125 offset:50848\n"
    "v_mfma_f32_16x16x32_bf16 a[4:7], v[84:87], v[40:43], a[4:7]\n"
    "s_add_u32 m0, s100, 4096\n"
    "v_mov_b32 v120, v80\n"
    "v_mov_b32 v121, v81\n"
    "v_mfma_f32_16x16x32_bf16 a[128:131], v[84:87], v[48:51], a[128:131]\n"
    "buffer_load_dwordx4 v68, s[60:63], s75 offen offset:0 lds\n"
    "v_mov_b32 v122, v82\n"
    "v_mfma_f32_16x16x32_bf16 a[132:135], v[84:87], v[56:59], a[132:135]\n"
    "v_mov_b32 v123, v83\n"
    "v_mfma_f32_16x16x32_bf16 a[8:11], v[88:91], v[32:35], a[8:11]\n"
    "ds_read_b64_tr_b16 v[108:109], v125 offset:33984\n"
    "ds_read_b64_tr_b16 v[110:111], v125 offset:50880\n"
    "v_permlane32_swap_b32 v80, v120\n"
    "v_mfma_f32_16x16x32_bf16 a[12:15], v[88:91], v[40:43], a[12:15]\n"
    "v_permlane32_swap_b32 v81, v121\n"
    "v_mfma_f32_16x16x32_bf16 a[136:139], v[88:91], v[48:51], a[136:139]\n"
    "buffer_load_dwordx4 v69, s[60:63], s75 offen offset:1024 lds\n"
    "v_mfma_f32_16x16x32_bf16 a[140:143], v[88:91], v[56:59], a[140:143]\n"
    "s_waitcnt lgkmcnt(6)\n"
    "v_permlane32_swap_b32 v82, v122\n"
    "v_mfma_f32_16x16x32_bf16 a[16:19], v[92:95], v[32:35], a[16:19]\n"
    "ds_read_b64_tr_b16 v[112:113], v125 offset:34016\n"
    "ds_read_b64_tr_b16 v[114:115], v125 offset:50912\n"
    "v_mfma_f32_16x16x32_bf16 a[20:23], v[92:95], v[40:43], a[20:23]\n"
    "v_permlane32_swap_b32 v83, v123\n"
    "v_add_f32 v80, v80, v120\n"
    "v_mfma_f32_16x16x32_bf16 a[144:147], v[92:95], v[48:51], a[144:147]\n"
    "buffer_load_dwordx4 v70, s[60:63], s75 offen offset:2048 lds\n"
    "v_mfma_f32_16x16x32_bf16 a[148:151], v[92:95], v[56:59], a[148:151]\n"
    "v_add_f32 v81, v81, v121\n"
    "v_add_f32 v82, v82, v122\n"
    "v_mfma_f32_16x16x32_bf16 a[24:27], v[96:99], v[32:35], a[24:27]\n"
    "ds_read_b64_tr_b16 v[84:85], v125 offset:34048\n"
    "ds_read_b64_tr_b16 v[86:87], v125 offset:50944\n"
    "v_add_f32 v83, v83, v123\n"
    "v_mfma_f32_16x16x32_bf16 a[28:31], v[96:99], v[40:43], a[28:31]\n"
    "s_add_u32 m0, s100, 28672\n"
    "s_mov_b32 s76, 28672\n"
    "v_mov_b32 v120, v80\n"
    "v_mfma_f32_16x16x32_bf16 a[152:155], v[96:99], v[48:51], a[152:155]\n"
    "buffer_load_dwordx4 v64, s[60:63], s76 offen offset:0 lds\n"
    "v_mfma_f32_16x16x32_bf16 a[156:159], v[96:99], v[56:59], a[156:159]\n"
    "s_waitcnt lgkmcnt(6)\n"
    "v_mov_b32 v121, v81\n"
    "v_mov_b32 v122, v82\n"
    "v_mfma_f32_16x16x32_bf16 a[32:35], v[100:103], v[32:35], a[32:35]\n"
    "ds_read_b64_tr_b16 v[88:89], v125 offset:34080\n"
    "ds_read_b64_tr_b16 v[90:91], v125 offset:50976\n"
    "v_mov_b32 v123, v83\n"
    "v_mfma_f32_16x16x32_bf16 a[36:39], v[100:103], v[40:43], a[36:39]\n"
    "v_permlane16_swap_b32 v80, v120\n"
    "v_mfma_f32_16x16x32_bf16 a[160:163], v[100:103], v[48:51], a[160:163]\n"
    "buffer_load_dwordx4 v65, s[60:63], s76 offen offset:1024 lds\n"
    "v_mfma_f32_16x16x32_bf16 a[164:167], v[100:103], v[56:59], a[164:167]\n"
    "v_permlane16_swap_b32 v81, v121\n"
    "v_mfma_f32_16x16x32_bf16 a[40:43], v[104:107], v[32:35], a[40:43]\n"
    "ds_read_b64_tr_b16 v[92:93], v125 offset:34112\n"
    "ds_read_b64_tr_b16 v[94:95], v125 offset:51008\n"
    "v_mfma_f32_16x16x32_bf16 a[44:47], v[104:107], v[40:43], a[44:47]\n"
    "v_permlane16_swap_b32 v82, v122\n"
    "v_permlane16_swap_b32 v83, v123\n"
    "v_mfma_f32_16x16x32_bf16 a[168:171], v[104:107], v[48:51], a[168:171]\n"
    "buffer_load_dwordx4 v66, s[60:63], s76 offen offset:2048 lds\n"
    "v_mfma_f32_16x16x32_bf16 a[172:175], v[104:107], v[56:59], a[172:175]\n"
    "s_waitcnt lgkmcnt(6)\n"
    "v_add_f32 v80, v80, v120\n"
    "v_add_f32 v81, v81, v121\n"
    "v_mfma_f32_16x16x32_bf16 a[48:51], v[108:111], v[32:35], a[48:51]\n"
    "ds_read_b64_tr_b16 v[96:97], v125 offset:34144\n"
    "ds_read_b64_tr_b16 v[98:99], v125 offset:51040\n"
    "v_mfma_f32_16x16x32_bf16 a[52:55], v[108:111], v[40:43], a[52:55]\n"
    "v_add_f32 v82, v82, v122\n"
    "v_add_f32 v83, v83, v123\n"
    "v_mfma_f32_16x16x32_bf16 a[176:179], v[108:111], v[48:51], a[176:179]\n"
    "buffer_load_dwordx4 v67, s[60:63], s76 offen offset:3072 lds\n"
    "v_mbcnt_lo_u32_b32 v71, -1, 0\n"
    "v_mfma_f32_16x16x32_bf16 a[180:183], v[108:111], v[56:59], a[180:183]\n"
    "v_mbcnt_hi_u32_b32 v71, -1, v71\n"
    "v_and_b32 v71, 15, v71\n"
    "v_mfma_f32_16x16x32_bf16 a[56:59], v[112:115], v[32:35], a[56:59]\n"
    "ds_read_b64_tr_b16 v[100:101], v125 offset:34176\n"
    "ds_read_b64_tr_b16 v[102:103], v125 offset:51072\n"
    "v_mfma_f32_16x16x32_bf16 a[60:63], v[112:115], v[40:43], a[60:63]\n"
    "s_add_u32 m0, s100, 32768\n"
    "s_lshl_b32 s86, s69, 8\n"
    "s_add_u32 s86, s86, 136192\n"
    "v_lshl_add_u32 v71, v71, 2, s86\n"
    "v_mfma_f32_16x16x32_bf16 a[184:187], v[112:115], v[48:51], a[184:187]\n"
    "buffer_load_dwordx4 v68, s[60:63], s76 offen offset:0 lds\n"
    "v_mfma_f32_16x16x32_bf16 a[188:191], v[112:115], v[56:59], a[188:191]\n"
    "s_waitcnt lgkmcnt(6)\n"
    "ds_write_b32 v71, v80 offset:0\n"
    "ds_write_b32 v71, v81 offset:64\n"
    "v_mfma_f32_16x16x32_bf16 a[64:67], v[84:87], v[32:35], a[64:67]\n"
    "ds_read_b64_tr_b16 v[104:105], v125 offset:34208\n"
    "ds_read_b64_tr_b16 v[106:107], v125 offset:51104\n"
    "v_mfma_f32_16x16x32_bf16 a[68:71], v[84:87], v[40:43], a[68:71]\n"
    "ds_write_b32 v71, v82 offset:128\n"
    "v_mfma_f32_16x16x32_bf16 a[192:195], v[84:87], v[48:51], a[192:195]\n"
    "buffer_load_dwordx4 v69, s[60:63], s76 offen offset:1024 lds\n"
    "v_mfma_f32_16x16x32_bf16 a[196:199], v[84:87], v[56:59], a[196:199]\n"
    "ds_write_b32 v71, v83 offset:192\n"
    "v_mov_b32 v122, 0x5f800000\n"
    "v_mfma_f32_16x16x32_bf16 a[72:75], v[88:91], v[32:35], a[72:75]\n"
    "ds_read_b64_tr_b16 v[108:109], v125 offset:34240\n"
    "ds_read_b64_tr_b16 v[110:111], v125 offset:51136\n"
    "v_cmp_nlt_f32 vcc, v80, v122\n"
    "s_mov_b64 s[86:87], vcc\n"
    "v_mfma_f32_16x16x32_bf16 a[76:79], v[88:91], v[40:43], a[76:79]\n"
    "v_cmp_nlt_f32 vcc, v81, v122\n"
    "s_or_b64 s[86:87], s[86:87], vcc\n"
    "v_cmp_nlt_f32 vcc, v82, v122\n"
    "s_or_b64 s[86:87], s[86:87], vcc\n"
    "v_mfma_f32_16x16x32_bf16 a[200:203], v[88:91], v[48:51], a[200:203]\n"
    "buffer_load_dwordx4 v70, s[60:63], s76 offen offset:2048 lds\n"
    "v_mfma_f32_16x16x32_bf16 a[204:207], v[88:91], v[56:59], a[204:207]\n"
    "s_waitcnt lgkmcnt(10)\n"
    "v_cmp_nlt_f32 vcc, v83, v122\n"
    "s_or_b64 s[86:87], s[86:87], vcc\n"
    "s_cmp_lg_u64 s[86:87], 0\n"
    "s_cselect_b32 s86, 1, 0\n"
    "v_mov_b32 v122, s86\n"
    "s_lshl_b32 s87, s69, 2\n"
    "s_add_u32 s87, s87, 137216\n"
    "v_mov_b32 v123, s87\n"
    "v_mfma_f32_16x16x32_bf16 a[80:83], v[92:95], v[32:35], a[80:83]\n"
    "ds_read_b64_tr_b16 v[112:113], v125 offset:34272\n"
    "ds_read_b64_tr_b16 v[114:115], v125 offset:51168\n"
    "v_mfma_f32_16x16x32_bf16 a[84:87], v[92:95], v[40:43], a[84:87]\n"
    "ds_write_b32 v123, v122\n"
    "v_mfma_f32_16x16x32_bf16 a[208:211], v[92:95], v[48:51], a[208:211]\n"
    "v_mbcnt_lo_u32_b32 v77, -1, 0\n"
    "v_mbcnt_hi_u32_b32 v77, -1, v77\n"
    "v_mfma_f32_16x16x32_bf16 a[212:215], v[92:95], v[56:59], a[212:215]\n"
    "s_lshl_b32 s88, s69, 13\n"
    "s_add_u32 s88, s88, 57344\n"
    "v_lshl_add_u32 v74, v77, 4, s88\n"
    "s_xor_b32 s89, s69, 2\n"
    "s_lshl_b32 s89, s89, 13\n"
    "s_add_u32 s89, s89, 57344\n"
    "v_mfma_f32_16x16x32_bf16 a[88:91], v[96:99], v[32:35], a[88:91]\n"
    "v_lshl_add_u32 v75, v77, 4, s89\n"
    "v_mfma_f32_16x16x32_bf16 a[92:95], v[96:99], v[40:43], a[92:95]\n"
    "ds_write_b128 v74, a[128:131] offset:0\n"
    "v_mfma_f32_16x16x32_bf16 a[216:219], v[96:99], v[48:51], a[216:219]\n"
    "v_mfma_f32_16x16x32_bf16 a[220:223], v[96:99], v[56:59], a[220:223]\n"
    "s_waitcnt lgkmcnt(8)\n"
    "v_mfma_f32_16x16x32_bf16 a[96:99], v[100:103], v[32:35], a[96:99]\n"
    "ds_write_b128 v74, a[132:135] offset:1024\n"
    "v_mfma_f32_16x16x32_bf16 a[100:103], v[100:103], v[40:43], a[100:103]\n"
    "v_mfma_f32_16x16x32_bf16 a[224:227], v[100:103], v[48:51], a[224:227]\n"
    "ds_write_b128 v74, a[136:139] offset:2048\n"
    "v_mfma_f32_16x16x32_bf16 a[228:231], v[100:103], v[56:59], a[228:231]\n"
    "v_mfma_f32_16x16x32_bf16 a[104:107], v[104:107], v[32:35], a[104:107]\n"
    "ds_write_b128 v74, a[140:143] offset:3072\n"
    "v_mfma_f32_16x16x32_bf16 a[108:111], v[104:107], v[40:43], a[108:111]\n"
    "v_mfma_f32_16x16x32_bf16 a[232:235], v[104:107], v[48:51], a[232:235]\n"
    "v_mfma_f32_16x16x32_bf16 a[236:239], v[104:107], v[56:59], a[236:239]\n"
    "s_waitcnt lgkmcnt(5)\n"
    "ds_write_b128 v74, a[144:147] offset:4096\n"
    "v_mfma_f32_16x16x32_bf16 a[112:115], v[108:111], v[32:35], a[112:115]\n"
    "v_mfma_f32_16x16x32_bf16 a[116:119], v[108:111], v[40:43], a[116:119]\n"
    "ds_write_b128 v74, a[148:151] offset:5120\n"
    "v_mfma_f32_16x16x32_bf16 a[240:243], v[108:111], v[48:51], a[240:243]\n"
    "v_mfma_f32_16x16x32_bf16 a[244:247], v[108:111], v[56:59], a[244:247]\n"
    "v_mfma_f32_16x16x32_bf16 a[120:123], v[112:115], v[32:35], a[120:123]\n"
    "ds_write_b128 v74, a[152:155] offset:6144\n"
    "v_mfma_f32_16x16x32_bf16 a[124:127], v[112:115], v[40:43], a[124:127]\n"
    "v_mfma_f32_16x16x32_bf16 a[248:251], v[112:115], v[48:51], a[248:251]\n"
    "ds_write_b128 v74, a[156:159] offset:7168\n"
    "v_mfma_f32_16x16x32_bf16 a[252:255], v[112:115], v[56:59], a[252:255]\n"
    ".Lejoin:\n"
    "s_waitcnt lgkmcnt(0)\n"
    "s_barrier\n"
    "v_mov_b32 v123, 137216\n"
    "ds_read_b128 v[36:39], v123\n"
    "v_xor_b32 v120, 512, v71\n"
    "ds_read_b32 v72, v120 offset:128\n"
    "ds_read_b32 v73, v120 offset:192\n"
    "s_waitcnt lgkmcnt(2)\n"
    "v_or_b32 v36, v36, v37\n"
    "v_or3_b32 v36, v36, v38, v39\n"
    "s_nop 1\n"
    "v_readfirstlane_b32 s86, v36\n"
    "s_waitcnt lgkmcnt(1)\n"
    "v_add_f32 v72, v72, v80\n"
    "s_waitcnt lgkmcnt(0)\n"
    "v_add_f32 v73, v73, v81\n"
    "s_andn2_b32 s86, s86, s91\n"
    "s_cmp_lg_u32 s86, 0\n"
    "s_cbranch_scc1 .Lslow\n"
    "v_rcp_f32 v72, v72\n"
    "v_rcp_f32 v73, v73\n"
    "s_nop 0\n"
    "v_mov_b32 v62, v72\n"
    "v_mov_b32 v63, v72\n"
    "v_mov_b32 v176, v73\n"
    "v_mov_b32 v177, v73\n"
    "ds_read_b128 v[0:3], v75 offset:0\n"
    "ds_read_b128 v[4:7], v75 offset:1024\n"
    "ds_read_b128 v[8:11], v75 offset:2048\n"
    "ds_read_b128 v[12:15], v75 offset:3072\n"
    "ds_read_b128 v[16:19], v75 offset:4096\n"
    "ds_read_b128 v[20:23], v75 offset:5120\n"
    "ds_read_b128 v[24:27], v75 offset:6144\n"
    "ds_read_b128 v[28:31], v75 offset:7168\n"
    "ds_write_b128 v74, a[160:163] offset:32768\n"
    "v_accvgpr_read_b32 v144, a0\n"
    "v_accvgpr_read_b32 v145, a1\n"
    "v_accvgpr_read_b32 v146, a2\n"
    "s_mul_i32 s86, s72, 17408\n"
    "s_lshl_b32 s87, s73, 11\n"
    "s_add_u32 s86, s86, s87\n"
    "s_add_u32 s86, s86, 131072\n"
    "v_lshl_add_u32 v56, v77, 4, s86\n"
    "v_xor_b32 v57, 1024, v56\n"
    "v_accvgpr_read_b32 v147, a3\n"
    "ds_write_b128 v74, a[164:167] offset:33792\n"
    "v_accvgpr_read_b32 v148, a4\n"
    "v_accvgpr_read_b32 v149, a5\n"
    "s_mov_b32 s52, 57344\n"
    "s_mov_b32 s53, 114688\n"
    "s_mov_b32 s54, 172032\n"
    "s_mov_b32 s55, 229376\n"
    "v_accvgpr_read_b32 v150, a6\n"
    "v_accvgpr_read_b32 v151, a7\n"
    "ds_write_b128 v74, a[168:171] offset:34816\n"
    "v_accvgpr_read_b32 v152, a8\n"
    "v_accvgpr_read_b32 v153, a9\n"
    "s_mov_b32 s77, 286720\n"
    "s_mov_b32 s84, 344064\n"
    "s_mov_b32 s74, 401408\n"
    "s_and_b32 s86, s70, 1\n"
    "v_accvgpr_read_b32 v154, a10\n"
    "v_accvgpr_read_b32 v155, a11\n"
    "ds_write_b128 v74, a[172:175] offset:35840\n"
    "v_accvgpr_read_b32 v156, a12\n"
    "s_mul_i32 s86, s86, 7\n"
    "s_add_u32 s86, s86, s69\n"
    "s_lshl_b32 s87, s86, 10\n"
    "v_lshl_add_u32 v34, v77, 4, s87\n"
    "v_accvgpr_read_b32 v157, a13\n"
    "v_accvgpr_read_b32 v158, a14\n"
    "v_accvgpr_read_b32 v159, a15\n"
    "ds_write_b128 v74, a[176:179] offset:36864\n"
    "v_accvgpr_read_b32 v160, a16\n"
    "v_add_u32 v35, 4096, v34\n"
    "s_lshr_b32 s88, s70, 1\n"
    "s_and_b32 s88, s88, 1\n"
    "s_mul_i32 s89, s88, 448\n"
    "v_accvgpr_read_b32 v161, a17\n"
    "v_accvgpr_read_b32 v162, a18\n"
    "v_accvgpr_read_b32 v163, a19\n"
    "ds_write_b128 v74, a[180:183] offset:37888\n"
    "s_lshl_b32 s87, s86, 5\n"
    "s_add_u32 s89, s89, s87\n"
    "v_lshrrev_b32 v42, 4, v77\n"
    "v_lshl_add_u32 v40, v42, 3, s89\n"
    "v_accvgpr_read_b32 v164, a20\n"
    "v_accvgpr_read_b32 v165, a21\n"
    "v_accvgpr_read_b32 v166, a22\n"
    "v_accvgpr_read_b32 v167, a23\n"
    "v_add_u32 v41, 128, v40\n"
    "s_lshl_b32 s87, s71, 6\n"
    "s_lshr_b32 s89, s70, 2\n"
    "s_lshl_b32 s89, s89, 5\n"
    "ds_write_b128 v74, a[184:187] offset:38912\n"
    "v_accvgpr_read_b32 v168, a24\n"
    "v_accvgpr_read_b32 v169, a25\n"
    "v_accvgpr_read_b32 v170, a26\n"
    "v_accvgpr_read_b32 v171, a27\n"
    "s_add_u32 s87, s87, s89\n"
    "s_lshl_b32 s89, s88, 4\n"
    "s_add_u32 s86, s87, s89\n"
    "s_xor_b32 s89, s89, 16\n"
    "ds_write_b128 v74, a[188:191] offset:39936\n"
    "v_accvgpr_read_b32 v172, a28\n"
    "v_accvgpr_read_b32 v173, a29\n"
    "v_accvgpr_read_b32 v174, a30\n"
    "s_add_u32 s87, s87, s89\n"
    "v_and_b32 v42, 15, v77\n"
    "v_add_u32 v36, s86, v42\n"
    "v_add_u32 v37, s87, v42\n"
    "v_accvgpr_read_b32 v175, a31\n"
    "s_waitcnt lgkmcnt(15)\n"
    "v_pk_add_f32 v[144:145], v[144:145], v[0:1]\n"
    "v_pk_add_f32 v[146:147], v[146:147], v[2:3]\n"
    "s_waitcnt lgkmcnt(14)\n"
    "v_pk_add_f32 v[148:149], v[148:149], v[4:5]\n"
    "v_pk_add_f32 v[150:151], v[150:151], v[6:7]\n"
    "v_mul_u32_u24 v36, 896, v36\n"
    "v_mul_u32_u24 v37, 896, v37\n"
    "v_add_u32 v36, v36, v40\n"
    "v_add_u32 v37, v37, v40\n"
    "s_waitcnt lgkmcnt(13)\n"
    "v_pk_add_f32 v[152:153], v[152:153], v[8:9]\n"
    "v_pk_add_f32 v[154:155], v[154:155], v[10:11]\n"
    "s_waitcnt lgkmcnt(12)\n"
    "v_pk_add_f32 v[156:157], v[156:157], v[12:13]\n"
    "v_pk_add_f32 v[158:159], v[158:159], v[14:15]\n"
    "v_add_u32 v38, 128, v36\n"
    "v_add_u32 v39, 128, v37\n"
    "s_cmp_eq_u32 s69, 3\n"
    "s_cselect_b32 s87, 0x80000000, 0\n"
    "v_pk_mul_f32 v[144:145], v[144:145], v[62:63]\n"
    "v_pk_mul_f32 v[146:147], v[146:147], v[62:63]\n"
    "v_pk_mul_f32 v[148:149], v[148:149], v[176:177]\n"
    "v_pk_mul_f32 v[150:151], v[150:151], v[176:177]\n"
    "v_or_b32 v35, s87, v35\n"
    "v_or_b32 v41, s87, v41\n"
    "v_or_b32 v38, s87, v38\n"
    "v_or_b32 v39, s87, v39\n"
    "v_pk_mul_f32 v[152:153], v[152:153], v[62:63]\n"
    "v_pk_mul_f32 v[154:155], v[154:155], v[62:63]\n"
    "v_pk_mul_f32 v[156:157], v[156:157], v[176:177]\n"
    "v_pk_mul_f32 v[158:159], v[158:159], v[176:177]\n"
    "v_cvt_pk_bf16_f32 v80, v144, v145\n"
    "s_lshl_b32 s86, s71, 5\n"
    "s_lshr_b32 s87, s70, 1\n"
    "s_add_u32 s86, s86, s87\n"
    "s_mul_i32 s86, s86, 14336\n"
    "v_cvt_pk_bf16_f32 v81, v146, v147\n"
    "v_cvt_pk_bf16_f32 v84, v148, v149\n"
    "v_cvt_pk_bf16_f32 v85, v150, v151\n"
    "v_cvt_pk_bf16_f32 v82, v152, v153\n"
    "s_add_u32 s40, s36, s86\n"
    "s_addc_u32 s41, s37, 0\n"
    "s_add_u32 s40, s40, 8192\n"
    "s_addc_u32 s41, s41, 0\n"
    "v_cvt_pk_bf16_f32 v83, v154, v155\n"
    "v_cvt_pk_bf16_f32 v86, v156, v157\n"
    "v_cvt_pk_bf16_f32 v87, v158, v159\n"
    "s_waitcnt lgkmcnt(11)\n"
    "v_pk_add_f32 v[160:161], v[160:161], v[16:17]\n"
    "v_pk_add_f32 v[162:163], v[162:163], v[18:19]\n"
    "s_and_b32 s41, s41, 0xffff\n"
    "s_mov_b32 s42, 268435456\n"
    "s_mov_b32 s43, 0x20000\n"
    "s_mov_b32 s44, s16\n"
    "s_waitcnt lgkmcnt(10)\n"
    "v_pk_add_f32 v[164:165], v[164:165], v[20:21]\n"
    "v_pk_add_f32 v[166:167], v[166:167], v[22:23]\n"
    "s_waitcnt lgkmcnt(9)\n"
    "v_pk_add_f32 v[168:169], v[168:169], v[24:25]\n"
    "v_pk_add_f32 v[170:171], v[170:171], v[26:27]\n"
    "s_mov_b32 s45, s17\n"
    "s_and_b32 s45, s45, 0xffff\n"
    "s_mov_b32 s46, 1835008\n"
    "s_mov_b32 s47, 0x20000\n"
    "s_waitcnt lgkmcnt(8)\n"
    "v_pk_add_f32 v[172:173], v[172:173], v[28:29]\n"
    "v_pk_add_f32 v[174:175], v[174:175], v[30:31]\n"
    "v_pk_mul_f32 v[160:161], v[160:161], v[62:63]\n"
    "v_pk_mul_f32 v[162:163], v[162:163], v[62:63]\n"
    "s_mov_b32 s48, s34\n"
    "s_mov_b32 s49, s35\n"
    "s_and_b32 s49, s49, 0xffff\n"
    "s_mov_b32 s50, 1835008\n"
    "v_pk_mul_f32 v[164:165], v[164:165], v[176:177]\n"
    "v_pk_mul_f32 v[166:167], v[166:167], v[176:177]\n"
    "v_pk_mul_f32 v[168:169], v[168:169], v[62:63]\n"
    "v_pk_mul_f32 v[170:171], v[170:171], v[62:63]\n"
    "v_pk_mul_f32 v[172:173], v[172:173], v[176:177]\n"
    "s_mov_b32 s51, 0x20000\n"
    "s_mov_b32 s56, s32\n"
    "s_mov_b32 s57, s33\n"
    "s_and_b32 s57, s57, 0xffff\n"
    "v_pk_mul_f32 v[174:175], v[174:175], v[176:177]\n"
    "v_cvt_pk_bf16_f32 v88, v160, v161\n"
    "v_cvt_pk_bf16_f32 v89, v162, v163\n"
    "v_cvt_pk_bf16_f32 v92, v164, v165\n"
    "s_mov_b32 s58, 896\n"
    "s_mov_b32 s59, 0x20000\n"
    "v_cvt_pk_bf16_f32 v93, v166, v167\n"
    "v_cvt_pk_bf16_f32 v90, v168, v169\n"
    "v_cvt_pk_bf16_f32 v91, v170, v171\n"
    "v_cvt_pk_bf16_f32 v94, v172, v173\n"
    "v_cvt_pk_bf16_f32 v95, v174, v175\n"
    "s_waitcnt lgkmcnt(0)\n"
    "s_barrier\n"
    "ds_read_b128 v[0:3], v75 offset:32768\n"
    "ds_read_b128 v[4:7], v75 offset:33792\n"
    "ds_read_b128 v[8:11], v75 offset:34816\n"
    "ds_read_b128 v[12:15], v75 offset:35840\n"
    "ds_read_b128 v[16:19], v75 offset:36864\n"
    "ds_read_b128 v[20:23], v75 offset:37888\n"
    "ds_read_b128 v[24:27], v75 offset:38912\n"
    "ds_read_b128 v[28:31], v75 offset:39936\n"
    "ds_write_b128 v56, v[80:83] offset:0\n"
    "v_accvgpr_read_b32 v144, a32\n"
    "v_accvgpr_read_b32 v145, a33\n"
    "v_add_u32 v42, 0x9e3779b9, v79\n"
    "s_waitcnt vmcnt(14)\n"
    "v_cmp_eq_u32 vcc, v78, v42\n"
    "s_cmp_eq_u64 vcc, -1\n"
    "s_cbranch_scc0 .Lspiny\n"
    ".Lrety:\n"
    "ds_write_b128 v57, v[84:87] offset:0\n"
    "v_accvgpr_read_b32 v146, a34\n"
    "v_accvgpr_read_b32 v147, a35\n"
    "buffer_load_dwordx4 a[128:131], v34, s[40:43], 0 offen sc1\n"
    "ds_write_b128 v56, v[88:91] offset:4096\n"
    "v_accvgpr_read_b32 v148, a36\n"
    "v_accvgpr_read_b32 v149, a37\n"
    "buffer_load_dwordx4 a[132:135], v34, s[40:43], s52 offen sc1\n"
    "ds_write_b128 v57, v[92:95] offset:4096\n"
    "v_accvgpr_read_b32 v150, a38\n"
    "v_accvgpr_read_b32 v151, a39\n"
    "buffer_load_dwordx4 a[136:139], v34, s[40:43], s53 offen sc1\n"
    "ds_write_b128 v74, a[192:195] offset:0\n"
    "v_accvgpr_read_b32 v152, a40\n"
    "v_accvgpr_read_b32 v153, a41\n"
    "ds_write_b128 v74, a[196:199] offset:1024\n"
    "buffer_load_dwordx4 a[140:143], v34, s[40:43], s54 offen sc1\n"
    "v_accvgpr_read_b32 v154, a42\n"
    "v_accvgpr_read_b32 v155, a43\n"
    "ds_write_b128 v74, a[200:203] offset:2048\n"
    "buffer_load_dwordx4 a[144:147], v34, s[40:43], s55 offen sc1\n"
    "v_accvgpr_read_b32 v156, a44\n"
    "v_accvgpr_read_b32 v157, a45\n"
    "v_accvgpr_read_b32 v158, a46\n"
    "buffer_load_dwordx4 a[148:151], v34, s[40:43], s77 offen sc1\n"
    "v_accvgpr_read_b32 v159, a47\n"
    "ds_write_b128 v74, a[204:207] offset:3072\n"
    "v_accvgpr_read_b32 v160, a48\n"
    "buffer_load_dwordx4 a[152:155], v34, s[40:43], s84 offen sc1\n"
    "v_accvgpr_read_b32 v161, a49\n"
    "v_accvgpr_read_b32 v162, a50\n"
    "v_accvgpr_read_b32 v163, a51\n"
    "buffer_load_dwordx4 a[156:159], v34, s[40:43], s74 offen sc1\n"
    "ds_write_b128 v74, a[208:211] offset:4096\n"
    "v_accvgpr_read_b32 v164, a52\n"
    "v_accvgpr_read_b32 v165, a53\n"
    "v_accvgpr_read_b32 v166, a54\n"
    "buffer_load_dwordx4 a[160:163], v35, s[40:43], 0 offen sc1\n"
    "v_accvgpr_read_b32 v167, a55\n"
    "ds_write_b128 v74, a[212:215] offset:5120\n"
    "v_accvgpr_read_b32 v168, a56\n"
    "buffer_load_dwordx4 a[164:167], v35, s[40:43], s52 offen sc1\n"
    "v_accvgpr_read_b32 v169, a57\n"
    "v_accvgpr_read_b32 v170, a58\n"
    "v_accvgpr_read_b32 v171, a59\n"
    "buffer_load_dwordx4 a[168:171], v35, s[40:43], s53 offen sc1\n"
    "ds_write_b128 v74, a[216:219] offset:6144\n"
    "v_accvgpr_read_b32 v172, a60\n"
    "v_accvgpr_read_b32 v173, a61\n"
    "buffer_load_dwordx4 a[172:175], v35, s[40:43], s54 offen sc1\n"
    "v_accvgpr_read_b32 v174, a62\n"
    "v_accvgpr_read_b32 v175, a63\n"
    "ds_write_b128 v74, a[220:223] offset:7168\n"
    "buffer_load_dwordx4 a[176:179], v35, s[40:43], s55 offen sc1\n"
    "s_waitcnt lgkmcnt(15)\n"
    "v_pk_add_f32 v[144:145], v[144:145], v[0:1]\n"
    "v_pk_add_f32 v[146:147], v[146:147], v[2:3]\n"
    "v_pk_add_f32 v[148:149], v[148:149], v[4:5]\n"
    "v_pk_add_f32 v[150:151], v[150:151], v[6:7]\n"
    "buffer_load_dwordx4 a[180:183], v35, s[40:43], s77 offen sc1\n"
    "v_pk_add_f32 v[152:153], v[152:153], v[8:9]\n"
    "v_pk_add_f32 v[154:155], v[154:155], v[10:11]\n"
    "v_pk_add_f32 v[156:157], v[156:157], v[12:13]\n"
    "buffer_load_dwordx4 a[184:187], v35, s[40:43], s84 offen sc1\n"
    "v_pk_add_f32 v[158:159], v[158:159], v[14:15]\n"
    "v_pk_mul_f32 v[144:145], v[144:145], v[62:63]\n"
    "v_pk_mul_f32 v[146:147], v[146:147], v[62:63]\n"
    "buffer_load_dwordx4 a[188:191], v35, s[40:43], s74 offen sc1\n"
    "v_pk_mul_f32 v[148:149], v[148:149], v[176:177]\n"
    "v_pk_mul_f32 v[150:151], v[150:151], v[176:177]\n"
    "v_pk_mul_f32 v[152:153], v[152:153], v[62:63]\n"
    "v_lshrrev_b32 v58, 5, v77\n"
    "v_pk_mul_f32 v[154:155], v[154:155], v[62:63]\n"
    "v_pk_mul_f32 v[156:157], v[156:157], v[176:177]\n"
    "v_pk_mul_f32 v[158:159], v[158:159], v[176:177]\n"
    "v_mul_u32_u24 v58, 7168, v58\n"
    "v_cvt_pk_bf16_f32 v96, v144, v145\n"
    "v_cvt_pk_bf16_f32 v97, v146, v147\n"
    "v_cvt_pk_bf16_f32 v100, v148, v149\n"
    "v_cvt_pk_bf16_f32 v101, v150, v151\n"
    "v_bfe_u32 v60, v77, 2, 3\n"
    "v_cvt_pk_bf16_f32 v98, v152, v153\n"
    "v_cvt_pk_bf16_f32 v99, v154, v155\n"
    "v_cvt_pk_bf16_f32 v102, v156, v157\n"
    "v_lshl_add_u32 v58, v60, 7, v58\n"
    "v_cvt_pk_bf16_f32 v103, v158, v159\n"
    "ds_write_b128 v56, v[96:99] offset:8192\n"
    "ds_write_b128 v57, v[100:103] offset:8192\n"
    "v_and_b32 v60, 3, v77\n"
    "v_pk_add_f32 v[160:161], v[160:161], v[16:17]\n"
    "v_pk_add_f32 v[162:163], v[162:163], v[18:19]\n"
    "s_waitcnt lgkmcnt(15)\n"
    "v_pk_add_f32 v[164:165], v[164:165], v[20:21]\n"
    "v_lshl_add_u32 v58, v60, 3, v58\n"
    "v_pk_add_f32 v[166:167], v[166:167], v[22:23]\n"
    "v_pk_add_f32 v[168:169], v[168:169], v[24:25]\n"
    "v_pk_add_f32 v[170:171], v[170:171], v[26:27]\n"
    "v_bfe_u32 v59, v77, 3, 2\n"
    "s_waitcnt lgkmcnt(14)\n"
    "v_pk_add_f32 v[172:173], v[172:173], v[28:29]\n"
    "v_pk_add_f32 v[174:175], v[174:175], v[30:31]\n"
    "v_pk_mul_f32 v[160:161], v[160:161], v[62:63]\n"
    "v_pk_mul_f32 v[162:163], v[162:163], v[62:63]\n"
    "s_mul_i32 s86, s69, 7\n"
    "s_add_u32 s86, s86, 0\n"
    "s_and_b32 s87, s86, 3\n"
    "s_lshr_b32 s86, s86, 2\n"
    "s_lshl_b32 s86, s86, 10\n"
    "v_xor_b32 v60, s87, v59\n"
    "v_lshl_add_u32 v46, v60, 5, v58\n"
    "v_add_u32 v46, s86, v46\n"
    "v_add_u32 v50, 57344, v46\n"
    "v_pk_mul_f32 v[164:165], v[164:165], v[176:177]\n"
    "v_pk_mul_f32 v[166:167], v[166:167], v[176:177]\n"
    "v_pk_mul_f32 v[168:169], v[168:169], v[62:63]\n"
    "s_mul_i32 s86, s69, 7\n"
    "s_add_u32 s86, s86, 1\n"
    "s_and_b32 s87, s86, 3\n"
    "s_lshr_b32 s86, s86, 2\n"
    "s_lshl_b32 s86, s86, 10\n"
    "v_xor_b32 v60, s87, v59\n"
    "v_lshl_add_u32 v47, v60, 5, v58\n"
    "v_add_u32 v47, s86, v47\n"
    "v_add_u32 v51, 57344, v47\n"
    "v_pk_mul_f32 v[170:171], v[170:171], v[62:63]\n"
    "v_pk_mul_f32 v[172:173], v[172:173], v[176:177]\n"
    "v_pk_mul_f32 v[174:175], v[174:175], v[176:177]\n"
    "s_mul_i32 s86, s69, 7\n"
    "s_add_u32 s86, s86, 2\n"
    "s_and_b32 s87, s86, 3\n"
    "s_lshr_b32 s86, s86, 2\n"
    "s_lshl_b32 s86, s86, 10\n"
    "v_xor_b32 v60, s87, v59\n"
    "v_lshl_add_u32 v48, v60, 5, v58\n"
    "v_add_u32 v48, s86, v48\n"
    "v_add_u32 v52, 57344, v48\n"
    "v_cvt_pk_bf16_f32 v104, v160, v161\n"
    "v_cvt_pk_bf16_f32 v105, v162, v163\n"
    "v_cvt_pk_bf16_f32 v108, v164, v165\n"
    "s_mul_i32 s86, s69, 7\n"
    "s_add_u32 s86, s86, 3\n"
    "s_and_b32 s87, s86, 3\n"
    "s_lshr_b32 s86, s86, 2\n"
    "s_lshl_b32 s86, s86, 10\n"
    "v_xor_b32 v60, s87, v59\n"
    "v_lshl_add_u32 v49, v60, 5, v58\n"
    "v_add_u32 v49, s86, v49\n"
    "v_add_u32 v53, 57344, v49\n"
    "v_cvt_pk_bf16_f32 v109, v166, v167\n"
    "v_cvt_pk_bf16_f32 v106, v168, v169\n"
    "v_cvt_pk_bf16_f32 v107, v170, v171\n"
    "s_lshl_b32 s86, s73, 10\n"
    "s_add_u32 s87, s86, 131072\n"
    "v_lshl_add_u32 v54, v77, 4, s87\n"
    "s_sub_u32 s87, 131072, s86\n"
    "v_lshl_add_u32 v55, v77, 4, s87\n"
    "v_cvt_pk_bf16_f32 v110, v172, v173\n"
    "v_cvt_pk_bf16_f32 v111, v174, v175\n"
    "ds_write_b128 v56, v[104:107] offset:12288\n"
    "ds_write_b128 v57, v[108:111] offset:12288\n"
    "s_waitcnt lgkmcnt(0)\n"
    "s_barrier\n"
    "ds_read_b128 v[0:3], v75 offset:0\n"
    "ds_read_b128 v[4:7], v75 offset:1024\n"
    "ds_read_b128 v[8:11], v75 offset:2048\n"
    "ds_read_b128 v[12:15], v75 offset:3072\n"
    "ds_read_b128 v[16:19], v75 offset:4096\n"
    "ds_read_b128 v[20:23], v75 offset:5120\n"
    "ds_read_b128 v[24:27], v75 offset:6144\n"
    "ds_read_b128 v[28:31], v75 offset:7168\n"
    "ds_write_b128 v74, a[224:227] offset:32768\n"
    "v_accvgpr_read_b32 v144, a64\n"
    "v_accvgpr_read_b32 v145, a65\n"
    "v_accvgpr_read_b32 v146, a66\n"
    "v_accvgpr_read_b32 v147, a67\n"
    "ds_write_b128 v74, a[228:231] offset:33792\n"
    "v_accvgpr_read_b32 v148, a68\n"
    "v_accvgpr_read_b32 v149, a69\n"
    "v_accvgpr_read_b32 v150, a70\n"
    "v_accvgpr_read_b32 v151, a71\n"
    "ds_write_b128 v74, a[232:235] offset:34816\n"
    "buffer_load_dwordx2 a[192:193], v36, s[44:47], 0 offen\n"
    "v_accvgpr_read_b32 v152, a72\n"
    "v_accvgpr_read_b32 v153, a73\n"
    "v_accvgpr_read_b32 v154, a74\n"
    "v_accvgpr_read_b32 v155, a75\n"
    "ds_write_b128 v74, a[236:239] offset:35840\n"
    "v_accvgpr_read_b32 v156, a76\n"
    "v_accvgpr_read_b32 v157, a77\n"
    "v_accvgpr_read_b32 v158, a78\n"
    "v_accvgpr_read_b32 v159, a79\n"
    "ds_write_b128 v74, a[240:243] offset:36864\n"
    "v_accvgpr_read_b32 v160, a80\n"
    "buffer_load_dwordx2 a[194:195], v37, s[44:47], 0 offen\n"
    "v_accvgpr_read_b32 v161, a81\n"
    "v_accvgpr_read_b32 v162, a82\n"
    "v_accvgpr_read_b32 v163, a83\n"
    "ds_write_b128 v74, a[244:247] offset:37888\n"
    "v_accvgpr_read_b32 v164, a84\n"
    "v_accvgpr_read_b32 v165, a85\n"
    "v_accvgpr_read_b32 v166, a86\n"
    "v_accvgpr_read_b32 v167, a87\n"
    "ds_write_b128 v74, a[248:251] offset:38912\n"
    "v_accvgpr_read_b32 v168, a88\n"
    "v_accvgpr_read_b32 v169, a89\n"
    "buffer_load_dwordx2 a[200:201], v40, s[56:59], 0 offen\n"
    "v_accvgpr_read_b32 v170, a90\n"
    "v_accvgpr_read_b32 v171, a91\n"
    "ds_write_b128 v74, a[252:255] offset:39936\n"
    "v_accvgpr_read_b32 v172, a92\n"
    "v_accvgpr_read_b32 v173, a93\n"
    "v_accvgpr_read_b32 v174, a94\n"
    "v_accvgpr_read_b32 v175, a95\n"
    "s_waitcnt lgkmcnt(15)\n"
    "v_pk_add_f32 v[144:145], v[144:145], v[0:1]\n"
    "v_pk_add_f32 v[146:147], v[146:147], v[2:3]\n"
    "s_waitcnt lgkmcnt(14)\n"
    "v_pk_add_f32 v[148:149], v[148:149], v[4:5]\n"
    "v_pk_add_f32 v[150:151], v[150:151], v[6:7]\n"
    "buffer_load_dwordx2 a[196:197], v38, s[44:47], 0 offen\n"
    "s_waitcnt lgkmcnt(13)\n"
    "v_pk_add_f32 v[152:153], v[152:153], v[8:9]\n"
    "v_pk_add_f32 v[154:155], v[154:155], v[10:11]\n"
    "s_waitcnt lgkmcnt(12)\n"
    "v_pk_add_f32 v[156:157], v[156:157], v[12:13]\n"
    "v_pk_add_f32 v[158:159], v[158:159], v[14:15]\n"
    "v_pk_mul_f32 v[144:145], v[144:145], v[62:63]\n"
    "v_pk_mul_f32 v[146:147], v[146:147], v[62:63]\n"
    "v_pk_mul_f32 v[148:149], v[148:149], v[176:177]\n"
    "v_pk_mul_f32 v[150:151], v[150:151], v[176:177]\n"
    "v_pk_mul_f32 v[152:153], v[152:153], v[62:63]\n"
    "v_pk_mul_f32 v[154:155], v[154:155], v[62:63]\n"
    "v_pk_mul_f32 v[156:157], v[156:157], v[176:177]\n"
    "buffer_load_dwordx2 a[198:199], v39, s[44:47], 0 offen\n"
    "v_pk_mul_f32 v[158:159], v[158:159], v[176:177]\n"
    "v_cvt_pk_bf16_f32 v112, v144, v145\n"
    "v_cvt_pk_bf16_f32 v113, v146, v147\n"
    "v_cvt_pk_bf16_f32 v116, v148, v149\n"
    "v_cvt_pk_bf16_f32 v117, v150, v151\n"
    "v_cvt_pk_bf16_f32 v114, v152, v153\n"
    "v_cvt_pk_bf16_f32 v115, v154, v155\n"
    "v_cvt_pk_bf16_f32 v118, v156, v157\n"
    "v_cvt_pk_bf16_f32 v119, v158, v159\n"
    "s_waitcnt lgkmcnt(11)\n"
    "v_pk_add_f32 v[160:161], v[160:161], v[16:17]\n"
    "v_pk_add_f32 v[162:163], v[162:163], v[18:19]\n"
    "buffer_load_dwordx2 a[202:203], v41, s[56:59], 0 offen\n"
    "s_waitcnt lgkmcnt(10)\n"
    "v_pk_add_f32 v[164:165], v[164:165], v[20:21]\n"
    "v_pk_add_f32 v[166:167], v[166:167], v[22:23]\n"
    "s_waitcnt lgkmcnt(9)\n"
    "v_pk_add_f32 v[168:169], v[168:169], v[24:25]\n"
    "v_pk_add_f32 v[170:171], v[170:171], v[26:27]\n"
    "s_waitcnt lgkmcnt(8)\n"
    "v_pk_add_f32 v[172:173], v[172:173], v[28:29]\n"
    "v_pk_add_f32 v[174:175], v[174:175], v[30:31]\n"
    "v_pk_mul_f32 v[160:161], v[160:161], v[62:63]\n"
    "v_pk_mul_f32 v[162:163], v[162:163], v[62:63]\n"
    "v_pk_mul_f32 v[164:165], v[164:165], v[176:177]\n"
    "v_pk_mul_f32 v[166:167], v[166:167], v[176:177]\n"
    "v_pk_mul_f32 v[168:169], v[168:169], v[62:63]\n"
    "s_mul_i32 s86, s69, 7168\n"
    "v_lshl_add_u32 v45, v77, 4, s86\n"
    "s_lshl_b32 s86, s71, 3\n"
    "s_add_u32 s86, s86, s70\n"
    "s_mul_i32 s86, s86, 57344\n"
    "s_add_u32 s64, s36, s86\n"
    "s_addc_u32 s65, s37, 0\n"
    "s_add_u32 s64, s64, 14688256\n"
    "s_addc_u32 s65, s65, 0\n"
    "s_and_b32 s65, s65, 0xffff\n"
    "s_mov_b32 s66, 57344\n"
    "s_mov_b32 s67, 0x20000\n"
    "v_pk_mul_f32 v[170:171], v[170:171], v[62:63]\n"
    "v_pk_mul_f32 v[172:173], v[172:173], v[176:177]\n"
    "v_pk_mul_f32 v[174:175], v[174:175], v[176:177]\n"
    "v_cvt_pk_bf16_f32 v120, v160, v161\n"
    "v_cvt_pk_bf16_f32 v121, v162, v163\n"
    "v_cvt_pk_bf16_f32 v124, v164, v165\n"
    "v_cvt_pk_bf16_f32 v125, v166, v167\n"
    "v_cvt_pk_bf16_f32 v122, v168, v169\n"
    "v_cvt_pk_bf16_f32 v123, v170, v171\n"
    "v_cvt_pk_bf16_f32 v126, v172, v173\n"
    "v_cvt_pk_bf16_f32 v127, v174, v175\n"
    "s_waitcnt lgkmcnt(0)\n"
    "s_barrier\n"
    "ds_read_b128 v[0:3], v75 offset:32768\n"
    "ds_read_b128 v[4:7], v75 offset:33792\n"
    "ds_read_b128 v[8:11], v75 offset:34816\n"
    "ds_read_b128 v[12:15], v75 offset:35840\n"
    "ds_read_b128 v[16:19], v75 offset:36864\n"
    "ds_read_b128 v[20:23], v75 offset:37888\n"
    "ds_read_b128 v[24:27], v75 offset:38912\n"
    "ds_read_b128 v[28:31], v75 offset:39936\n"
    "v_accvgpr_read_b32 v144, a96\n"
    "v_accvgpr_read_b32 v145, a97\n"
    "v_accvgpr_read_b32 v146, a98\n"
    "v_accvgpr_read_b32 v147, a99\n"
    "v_accvgpr_read_b32 v148, a100\n"
    "v_accvgpr_read_b32 v149, a101\n"
    "v_accvgpr_read_b32 v150, a102\n"
    "v_accvgpr_read_b32 v151, a103\n"
    "v_accvgpr_read_b32 v152, a104\n"
    "v_accvgpr_read_b32 v153, a105\n"
    "v_accvgpr_read_b32 v154, a106\n"
    "v_accvgpr_read_b32 v155, a107\n"
    "v_accvgpr_read_b32 v156, a108\n"
    "v_accvgpr_read_b32 v157, a109\n"
    "v_accvgpr_read_b32 v158, a110\n"
    "v_accvgpr_read_b32 v159, a111\n"
    "v_accvgpr_read_b32 v160, a112\n"
    "v_accvgpr_read_b32 v161, a113\n"
    "v_accvgpr_read_b32 v162, a114\n"
    "v_accvgpr_read_b32 v163, a115\n"
    "s_add_u32 m0, s100, 57344\n"
    "s_mov_b32 s75, 57344\n"
    "buffer_load_dwordx4 v64, s[60:63], s75 offen offset:0 lds\n"
    "v_accvgpr_read_b32 v164, a116\n"
    "v_accvgpr_read_b32 v165, a117\n"
    "v_accvgpr_read_b32 v166, a118\n"
    "v_accvgpr_read_b32 v167, a119\n"
    "v_accvgpr_read_b32 v168, a120\n"
    "v_accvgpr_read_b32 v169, a121\n"
    "v_accvgpr_read_b32 v170, a122\n"
    "v_accvgpr_read_b32 v171, a123\n"
    "v_accvgpr_read_b32 v172, a124\n"
    "v_accvgpr_read_b32 v173, a125\n"
    "v_accvgpr_read_b32 v174, a126\n"
    "v_accvgpr_read_b32 v175, a127\n"
    "s_waitcnt lgkmcnt(7)\n"
    "v_pk_add_f32 v[144:145], v[144:145], v[0:1]\n"
    "v_pk_add_f32 v[146:147], v[146:147], v[2:3]\n"
    "s_waitcnt lgkmcnt(6)\n"
    "v_pk_add_f32 v[148:149], v[148:149], v[4:5]\n"
    "v_pk_add_f32 v[150:151], v[150:151], v[6:7]\n"
    "s_waitcnt lgkmcnt(5)\n"
    "v_pk_add_f32 v[152:153], v[152:153], v[8:9]\n"
    "v_pk_add_f32 v[154:155], v[154:155], v[10:11]\n"
    "s_waitcnt lgkmcnt(4)\n"
    "v_pk_add_f32 v[156:157], v[156:157], v[12:13]\n"
    "v_pk_add_f32 v[158:159], v[158:159], v[14:15]\n"
    "buffer_load_dwordx4 v65, s[60:63], s75 offen offset:1024 lds\n"
    "v_pk_mul_f32 v[144:145], v[144:145], v[62:63]\n"
    "v_pk_mul_f32 v[146:147], v[146:147], v[62:63]\n"
    "v_pk_mul_f32 v[148:149], v[148:149], v[176:177]\n"
    "v_pk_mul_f32 v[150:151], v[150:151], v[176:177]\n"
    "v_pk_mul_f32 v[152:153], v[152:153], v[62:63]\n"
    "v_pk_mul_f32 v[154:155], v[154:155], v[62:63]\n"
    "v_pk_mul_f32 v[156:157], v[156:157], v[176:177]\n"
    "v_pk_mul_f32 v[158:159], v[158:159], v[176:177]\n"
    "v_cvt_pk_bf16_f32 v128, v144, v145\n"
    "v_cvt_pk_bf16_f32 v129, v146, v147\n"
    "v_cvt_pk_bf16_f32 v132, v148, v149\n"
    "v_cvt_pk_bf16_f32 v133, v150, v151\n"
    "v_cvt_pk_bf16_f32 v130, v152, v153\n"
    "v_cvt_pk_bf16_f32 v131, v154, v155\n"
    "v_cvt_pk_bf16_f32 v134, v156, v157\n"
    "v_cvt_pk_bf16_f32 v135, v158, v159\n"
    "s_waitcnt lgkmcnt(3)\n"
    "v_pk_add_f32 v[160:161], v[160:161], v[16:17]\n"
    "v_pk_add_f32 v[162:163], v[162:163], v[18:19]\n"
    "s_waitcnt lgkmcnt(2)\n"
    "v_pk_add_f32 v[164:165], v[164:165], v[20:21]\n"
    "v_pk_add_f32 v[166:167], v[166:167], v[22:23]\n"
    "buffer_load_dwordx4 v66, s[60:63], s75 offen offset:2048 lds\n"
    "s_waitcnt lgkmcnt(1)\n"
    "v_pk_add_f32 v[168:169], v[168:169], v[24:25]\n"
    "v_pk_add_f32 v[170:171], v[170:171], v[26:27]\n"
    "s_waitcnt lgkmcnt(0)\n"
    "v_pk_add_f32 v[172:173], v[172:173], v[28:29]\n"
    "v_pk_add_f32 v[174:175], v[174:175], v[30:31]\n"
    "v_pk_mul_f32 v[160:161], v[160:161], v[62:63]\n"
    "v_pk_mul_f32 v[162:163], v[162:163], v[62:63]\n"
    "v_pk_mul_f32 v[164:165], v[164:165], v[176:177]\n"
    "v_pk_mul_f32 v[166:167], v[166:167], v[176:177]\n"
    "v_pk_mul_f32 v[168:169], v[168:169], v[62:63]\n"
    "v_pk_mul_f32 v[170:171], v[170:171], v[62:63]\n"
    "v_pk_mul_f32 v[172:173], v[172:173], v[176:177]\n"
    "v_pk_mul_f32 v[174:175], v[174:175], v[176:177]\n"
    "v_cvt_pk_bf16_f32 v136, v160, v161\n"
    "v_cvt_pk_bf16_f32 v137, v162, v163\n"
    "v_cvt_pk_bf16_f32 v140, v164, v165\n"
    "v_cvt_pk_bf16_f32 v141, v166, v167\n"
    "v_cvt_pk_bf16_f32 v138, v168, v169\n"
    "v_cvt_pk_bf16_f32 v139, v170, v171\n"
    "v_cvt_pk_bf16_f32 v142, v172, v173\n"
    "v_cvt_pk_bf16_f32 v143, v174, v175\n"
    "ds_read_b64_tr_b16 a[0:1], v46 offset:0\n"
    "ds_read_b64_tr_b16 a[2:3], v46 offset:14336\n"
    "ds_read_b128 a[32:35], v54 offset:0\n"
    "ds_read_b128 a[36:39], v55 offset:1024\n"
    "ds_read_b128 a[40:43], v54 offset:2048\n"
    "ds_read_b128 a[44:47], v55 offset:3072\n"
    "ds_read_b64_tr_b16 a[4:5], v47 offset:0\n"
    "ds_read_b64_tr_b16 a[6:7], v47 offset:14336\n"
    "ds_read_b64_tr_b16 a[8:9], v48 offset:0\n"
    "ds_read_b64_tr_b16 a[10:11], v48 offset:14336\n"
    "s_waitcnt lgkmcnt(4)\n"
    "v_mfma_f32_16x16x32_bf16 v[144:147], a[0:3], a[32:35], 0\n"
    "ds_read_b64_tr_b16 a[12:13], v49 offset:0\n"
    "ds_read_b64_tr_b16 a[14:15], v49 offset:14336\n"
    "v_mfma_f32_16x16x32_bf16 v[148:151], a[0:3], a[36:39], 0\n"
    "buffer_load_dwordx4 v67, s[60:63], s75 offen offset:3072 lds\n"
    "v_mfma_f32_16x16x32_bf16 v[152:155], a[0:3], a[40:43], 0\n"
    "ds_read_b128 a[48:51], v54 offset:4096\n"
    "v_mfma_f32_16x16x32_bf16 v[156:159], a[0:3], a[44:47], 0\n"
    "s_waitcnt lgkmcnt(5)\n"
    "v_mfma_f32_16x16x32_bf16 v[160:163], a[4:7], a[32:35], 0\n"
    "s_add_u32 m0, s100, 61440\n"
    "ds_read_b64_tr_b16 a[16:17], v46 offset:1024\n"
    "ds_read_b64_tr_b16 a[18:19], v46 offset:15360\n"
    "v_mfma_f32_16x16x32_bf16 v[164:167], a[4:7], a[36:39], 0\n"
    "buffer_load_dwordx4 v68, s[60:63], s75 offen offset:0 lds\n"
    "v_mfma_f32_16x16x32_bf16 v[168:171], a[4:7], a[40:43], 0\n"
    "ds_read_b128 a[52:55], v55 offset:5120\n"
    "v_mfma_f32_16x16x32_bf16 v[172:175], a[4:7], a[44:47], 0\n"
    "s_waitcnt lgkmcnt(6)\n"
    "v_mfma_f32_16x16x32_bf16 v[176:179], a[8:11], a[32:35], 0\n"
    "ds_read_b64_tr_b16 a[20:21], v47 offset:1024\n"
    "ds_read_b64_tr_b16 a[22:23], v47 offset:15360\n"
    "v_mfma_f32_16x16x32_bf16 v[180:183], a[8:11], a[36:39], 0\n"
    "buffer_load_dwordx4 v69, s[60:63], s75 offen offset:1024 lds\n"
    "v_mfma_f32_16x16x32_bf16 v[184:187], a[8:11], a[40:43], 0\n"
    "ds_read_b128 a[56:59], v54 offset:6144\n"
    "v_mfma_f32_16x16x32_bf16 v[188:191], a[8:11], a[44:47], 0\n"
    "s_waitcnt lgkmcnt(7)\n"
    "v_mfma_f32_16x16x32_bf16 v[192:195], a[12:15], a[32:35], 0\n"
    "ds_read_b64_tr_b16 a[0:1], v48 offset:1024\n"
    "ds_read_b64_tr_b16 a[2:3], v48 offset:15360\n"
    "v_mfma_f32_16x16x32_bf16 v[196:199], a[12:15], a[36:39], 0\n"
    "buffer_load_dwordx4 v70, s[60:63], s75 offen offset:2048 lds\n"
    "v_mfma_f32_16x16x32_bf16 v[200:203], a[12:15], a[40:43], 0\n"
    "s_mov_b32 s76, 86016\n"
    "ds_read_b128 a[60:63], v55 offset:7168\n"
    "v_mfma_f32_16x16x32_bf16 v[204:207], a[12:15], a[44:47], 0\n"
    "s_waitcnt vmcnt(29) lgkmcnt(7)\n"
    "s_barrier\n"
    "v_mfma_f32_16x16x32_bf16 v[208:211], a[16:19], a[32:35], 0\n"
    "s_add_u32 m0, s100, 86016\n"
    "ds_read_b64_tr_b16 a[4:5], v46 offset:28672\n"
    "ds_read_b64_tr_b16 a[6:7], v46 offset:43008\n"
    "v_mfma_f32_16x16x32_bf16 v[212:215], a[16:19], a[36:39], 0\n"
    "buffer_load_dwordx4 v64, s[60:63], s76 offen offset:0 lds\n"
    "v_mfma_f32_16x16x32_bf16 v[216:219], a[16:19], a[40:43], 0\n"
    "v_mfma_f32_16x16x32_bf16 v[220:223], a[16:19], a[44:47], 0\n"
    "s_waitcnt lgkmcnt(6)\n"
    "v_mfma_f32_16x16x32_bf16 v[224:227], a[20:23], a[32:35], 0\n"
    "ds_read_b64_tr_b16 a[8:9], v47 offset:28672\n"
    "ds_read_b64_tr_b16 a[10:11], v47 offset:43008\n"
    "v_mfma_f32_16x16x32_bf16 v[228:231], a[20:23], a[36:39], 0\n"
    "buffer_load_dwordx4 v65, s[60:63], s76 offen offset:1024 lds\n"
    "v_mfma_f32_16x16x32_bf16 v[232:235], a[20:23], a[40:43], 0\n"
    "v_mfma_f32_16x16x32_bf16 v[236:239], a[20:23], a[44:47], 0\n"
    "s_waitcnt lgkmcnt(5)\n"
    "v_mfma_f32_16x16x32_bf16 v[240:243], a[0:3], a[32:35], 0\n"
    "ds_read_b64_tr_b16 a[12:13], v48 offset:28672\n"
    "ds_read_b64_tr_b16 a[14:15], v48 offset:43008\n"
    "v_mfma_f32_16x16x32_bf16 v[244:247], a[0:3], a[36:39], 0\n"
    "buffer_load_dwordx4 v66, s[60:63], s76 offen offset:2048 lds\n"
    "v_mfma_f32_16x16x32_bf16 v[248:251], a[0:3], a[40:43], 0\n"
    "v_mfma_f32_16x16x32_bf16 v[252:255], a[0:3], a[44:47], 0\n"
    "s_waitcnt lgkmcnt(4)\n"
    "v_mfma_f32_16x16x32_bf16 v[144:147], a[4:7], a[48:51], v[144:147]\n"
    "ds_read_b64_tr_b16 a[16:17], v49 offset:28672\n"
    "ds_read_b64_tr_b16 a[18:19], v49 offset:43008\n"
    "v_mfma_f32_16x16x32_bf16 v[148:151], a[4:7], a[52:55], v[148:151]\n"
    "buffer_load_dwordx4 v67, s[60:63], s76 offen offset:3072 lds\n"
    "v_mfma_f32_16x16x32_bf16 v[152:155], a[4:7], a[56:59], v[152:155]\n"
    "ds_read_b128 a[32:35], v54 offset:8192\n"
    "v_mfma_f32_16x16x32_bf16 v[156:159], a[4:7], a[60:63], v[156:159]\n"
    "s_waitcnt lgkmcnt(5)\n"
    "v_mfma_f32_16x16x32_bf16 v[160:163], a[8:11], a[48:51], v[160:163]\n"
    "s_add_u32 m0, s100, 90112\n"
    "ds_read_b64_tr_b16 a[20:21], v46 offset:29696\n"
    "ds_read_b64_tr_b16 a[22:23], v46 offset:44032\n"
    "v_mfma_f32_16x16x32_bf16 v[164:167], a[8:11], a[52:55], v[164:167]\n"
    "buffer_load_dwordx4 v68, s[60:63], s76 offen offset:0 lds\n"
    "v_mfma_f32_16x16x32_bf16 v[168:171], a[8:11], a[56:59], v[168:171]\n"
    "ds_read_b128 a[36:39], v55 offset:9216\n"
    "v_mfma_f32_16x16x32_bf16 v[172:175], a[8:11], a[60:63], v[172:175]\n"
    "s_waitcnt lgkmcnt(6)\n"
    "v_mfma_f32_16x16x32_bf16 v[176:179], a[12:15], a[48:51], v[176:179]\n"
    "ds_read_b64_tr_b16 a[0:1], v47 offset:29696\n"
    "ds_read_b64_tr_b16 a[2:3], v47 offset:44032\n"
    "v_mfma_f32_16x16x32_bf16 v[180:183], a[12:15], a[52:55], v[180:183]\n"
    "buffer_load_dwordx4 v69, s[60:63], s76 offen offset:1024 lds\n"
    "v_mfma_f32_16x16x32_bf16 v[184:187], a[12:15], a[56:59], v[184:187]\n"
    "ds_read_b128 a[40:43], v54 offset:10240\n"
    "v_mfma_f32_16x16x32_bf16 v[188:191], a[12:15], a[60:63], v[188:191]\n"
    "s_waitcnt lgkmcnt(7)\n"
    "v_mfma_f32_16x16x32_bf16 v[192:195], a[16:19], a[48:51], v[192:195]\n"
    "ds_read_b64_tr_b16 a[4:5], v48 offset:29696\n"
    "ds_read_b64_tr_b16 a[6:7], v48 offset:44032\n"
    "v_mfma_f32_16x16x32_bf16 v[196:199], a[16:19], a[52:55], v[196:199]\n"
    "buffer_load_dwordx4 v70, s[60:63], s76 offen offset:2048 lds\n"
    "v_mfma_f32_16x16x32_bf16 v[200:203], a[16:19], a[56:59], v[200:203]\n"
    "s_mov_b32 s75, 229376\n"
    "ds_read_b128 a[44:47], v55 offset:11264\n"
    "v_mfma_f32_16x16x32_bf16 v[204:207], a[16:19], a[60:63], v[204:207]\n"
    "s_waitcnt vmcnt(7) lgkmcnt(7)\n"
    "s_barrier\n"
    "v_mfma_f32_16x16x32_bf16 v[208:211], a[20:23], a[48:51], v[208:211]\n"
    "s_add_u32 m0, s100, 0\n"
    "ds_read_b64_tr_b16 a[8:9], v50 offset:0\n"
    "ds_read_b64_tr_b16 a[10:11], v50 offset:14336\n"
    "v_mfma_f32_16x16x32_bf16 v[212:215], a[20:23], a[52:55], v[212:215]\n"
    "buffer_load_dwordx4 v64, s[60:63], s75 offen offset:0 lds\n"
    "v_mfma_f32_16x16x32_bf16 v[216:219], a[20:23], a[56:59], v[216:219]\n"
    "v_mfma_f32_16x16x32_bf16 v[220:223], a[20:23], a[60:63], v[220:223]\n"
    "s_waitcnt lgkmcnt(6)\n"
    "v_mfma_f32_16x16x32_bf16 v[224:227], a[0:3], a[48:51], v[224:227]\n"
    "ds_read_b64_tr_b16 a[12:13], v51 offset:0\n"
    "ds_read_b64_tr_b16 a[14:15], v51 offset:14336\n"
    "v_mfma_f32_16x16x32_bf16 v[228:231], a[0:3], a[52:55], v[228:231]\n"
    "buffer_load_dwordx4 v65, s[60:63], s75 offen offset:1024 lds\n"
    "v_mfma_f32_16x16x32_bf16 v[232:235], a[0:3], a[56:59], v[232:235]\n"
    "v_mfma_f32_16x16x32_bf16 v[236:239], a[0:3], a[60:63], v[236:239]\n"
    "s_waitcnt lgkmcnt(5)\n"
    "v_mfma_f32_16x16x32_bf16 v[240:243], a[4:7], a[48:51], v[240:243]\n"
    "ds_read_b64_tr_b16 a[16:17], v52 offset:0\n"
    "ds_read_b64_tr_b16 a[18:19], v52 offset:14336\n"
    "v_mfma_f32_16x16x32_bf16 v[244:247], a[4:7], a[52:55], v[244:247]\n"
    "buffer_load_dwordx4 v66, s[60:63], s75 offen offset:2048 lds\n"
    "v_mfma_f32_16x16x32_bf16 v[248:251], a[4:7], a[56:59], v[248:251]\n"
    "v_mfma_f32_16x16x32_bf16 v[252:255], a[4:7], a[60:63], v[252:255]\n"
    "s_waitcnt lgkmcnt(4)\n"
    "v_mfma_f32_16x16x32_bf16 v[144:147], a[8:11], a[32:35], v[144:147]\n"
    "ds_read_b64_tr_b16 a[20:21], v53 offset:0\n"
    "ds_read_b64_tr_b16 a[22:23], v53 offset:14336\n"
    "v_mfma_f32_16x16x32_bf16 v[148:151], a[8:11], a[36:39], v[148:151]\n"
    "buffer_load_dwordx4 v67, s[60:63], s75 offen offset:3072 lds\n"
    "v_mfma_f32_16x16x32_bf16 v[152:155], a[8:11], a[40:43], v[152:155]\n"
    "ds_read_b128 a[48:51], v54 offset:12288\n"
    "v_mfma_f32_16x16x32_bf16 v[156:159], a[8:11], a[44:47], v[156:159]\n"
    "s_waitcnt lgkmcnt(5)\n"
    "v_mfma_f32_16x16x32_bf16 v[160:163], a[12:15], a[32:35], v[160:163]\n"
    "s_add_u32 m0, s100, 4096\n"
    "ds_read_b64_tr_b16 a[0:1], v50 offset:1024\n"
    "ds_read_b64_tr_b16 a[2:3], v50 offset:15360\n"
    "v_mfma_f32_16x16x32_bf16 v[164:167], a[12:15], a[36:39], v[164:167]\n"
    "buffer_load_dwordx4 v68, s[60:63], s75 offen offset:0 lds\n"
    "v_mfma_f32_16x16x32_bf16 v[168:171], a[12:15], a[40:43], v[168:171]\n"
    "ds_read_b128 a[52:55], v55 offset:13312\n"
    "v_mfma_f32_16x16x32_bf16 v[172:175], a[12:15], a[44:47], v[172:175]\n"
    "s_waitcnt lgkmcnt(6)\n"
    "v_mfma_f32_16x16x32_bf16 v[176:179], a[16:19], a[32:35], v[176:179]\n"
    "ds_read_b64_tr_b16 a[4:5], v51 offset:1024\n"
    "ds_read_b64_tr_b16 a[6:7], v51 offset:15360\n"
    "v_mfma_f32_16x16x32_bf16 v[180:183], a[16:19], a[36:39], v[180:183]\n"
    "buffer_load_dwordx4 v69, s[60:63], s75 offen offset:1024 lds\n"
    "v_mfma_f32_16x16x32_bf16 v[184:187], a[16:19], a[40:43], v[184:187]\n"
    "ds_read_b128 a[56:59], v54 offset:14336\n"
    "v_mfma_f32_16x16x32_bf16 v[188:191], a[16:19], a[44:47], v[188:191]\n"
    "s_waitcnt lgkmcnt(7)\n"
    "v_mfma_f32_16x16x32_bf16 v[192:195], a[20:23], a[32:35], v[192:195]\n"
    "ds_read_b64_tr_b16 a[8:9], v52 offset:1024\n"
    "ds_read_b64_tr_b16 a[10:11], v52 offset:15360\n"
    "v_mfma_f32_16x16x32_bf16 v[196:199], a[20:23], a[36:39], v[196:199]\n"
    "buffer_load_dwordx4 v70, s[60:63], s75 offen offset:2048 lds\n"
    "v_mfma_f32_16x16x32_bf16 v[200:203], a[20:23], a[40:43], v[200:203]\n"
    "s_mov_b32 s76, 258048\n"
    "ds_read_b128 a[60:63], v55 offset:15360\n"
    "v_mfma_f32_16x16x32_bf16 v[204:207], a[20:23], a[44:47], v[204:207]\n"
    "s_waitcnt vmcnt(7) lgkmcnt(7)\n"
    "s_barrier\n"
    "v_mfma_f32_16x16x32_bf16 v[208:211], a[0:3], a[32:35], v[208:211]\n"
    "s_add_u32 m0, s100, 28672\n"
    "ds_read_b64_tr_b16 a[12:13], v50 offset:28672\n"
    "ds_read_b64_tr_b16 a[14:15], v50 offset:43008\n"
    "v_mfma_f32_16x16x32_bf16 v[212:215], a[0:3], a[36:39], v[212:215]\n"
    "buffer_load_dwordx4 v64, s[60:63], s76 offen offset:0 lds\n"
    "v_mfma_f32_16x16x32_bf16 v[216:219], a[0:3], a[40:43], v[216:219]\n"
    "v_mfma_f32_16x16x32_bf16 v[220:223], a[0:3], a[44:47], v[220:223]\n"
    "s_waitcnt lgkmcnt(6)\n"
    "v_mfma_f32_16x16x32_bf16 v[224:227], a[4:7], a[32:35], v[224:227]\n"
    "ds_read_b64_tr_b16 a[16:17], v51 offset:28672\n"
    "ds_read_b64_tr_b16 a[18:19], v51 offset:43008\n"
    "v_mfma_f32_16x16x32_bf16 v[228:231], a[4:7], a[36:39], v[228:231]\n"
    "buffer_load_dwordx4 v65, s[60:63], s76 offen offset:1024 lds\n"
    "v_mfma_f32_16x16x32_bf16 v[232:235], a[4:7], a[40:43], v[232:235]\n"
    "v_mfma_f32_16x16x32_bf16 v[236:239], a[4:7], a[44:47], v[236:239]\n"
    "s_waitcnt lgkmcnt(5)\n"
    "v_mfma_f32_16x16x32_bf16 v[240:243], a[8:11], a[32:35], v[240:243]\n"
    "ds_read_b64_tr_b16 a[20:21], v52 offset:28672\n"
    "ds_read_b64_tr_b16 a[22:23], v52 offset:43008\n"
    "v_mfma_f32_16x16x32_bf16 v[244:247], a[8:11], a[36:39], v[244:247]\n"
    "buffer_load_dwordx4 v66, s[60:63], s76 offen offset:2048 lds\n"
    "v_mfma_f32_16x16x32_bf16 v[248:251], a[8:11], a[40:43], v[248:251]\n"
    "v_mfma_f32_16x16x32_bf16 v[252:255], a[8:11], a[44:47], v[252:255]\n"
    "s_waitcnt lgkmcnt(4)\n"
    "v_mfma_f32_16x16x32_bf16 v[144:147], a[12:15], a[48:51], v[144:147]\n"
    "ds_read_b64_tr_b16 a[0:1], v53 offset:28672\n"
    "ds_read_b64_tr_b16 a[2:3], v53 offset:43008\n"
    "v_mfma_f32_16x16x32_bf16 v[148:151], a[12:15], a[52:55], v[148:151]\n"
    "buffer_load_dwordx4 v67, s[60:63], s76 offen offset:3072 lds\n"
    "v_mfma_f32_16x16x32_bf16 v[152:155], a[12:15], a[56:59], v[152:155]\n"
    "ds_read_b128 a[32:35], v54 offset:16384\n"
    "v_mfma_f32_16x16x32_bf16 v[156:159], a[12:15], a[60:63], v[156:159]\n"
    "s_waitcnt lgkmcnt(5)\n"
    "v_mfma_f32_16x16x32_bf16 v[160:163], a[16:19], a[48:51], v[160:163]\n"
    "s_add_u32 m0, s100, 32768\n"
    "ds_read_b64_tr_b16 a[4:5], v50 offset:29696\n"
    "ds_read_b64_tr_b16 a[6:7], v50 offset:44032\n"
    "v_mfma_f32_16x16x32_bf16 v[164:167], a[16:19], a[52:55], v[164:167]\n"
    "buffer_load_dwordx4 v68, s[60:63], s76 offen offset:0 lds\n"
    "v_mfma_f32_16x16x32_bf16 v[168:171], a[16:19], a[56:59], v[168:171]\n"
    "ds_read_b128 a[36:39], v55 offset:17408\n"
    "v_mfma_f32_16x16x32_bf16 v[172:175], a[16:19], a[60:63], v[172:175]\n"
    "s_waitcnt lgkmcnt(6)\n"
    "v_mfma_f32_16x16x32_bf16 v[176:179], a[20:23], a[48:51], v[176:179]\n"
    "ds_read_b64_tr_b16 a[8:9], v51 offset:29696\n"
    "ds_read_b64_tr_b16 a[10:11], v51 offset:44032\n"
    "v_mfma_f32_16x16x32_bf16 v[180:183], a[20:23], a[52:55], v[180:183]\n"
    "buffer_load_dwordx4 v69, s[60:63], s76 offen offset:1024 lds\n"
    "v_mfma_f32_16x16x32_bf16 v[184:187], a[20:23], a[56:59], v[184:187]\n"
    "ds_read_b128 a[40:43], v54 offset:18432\n"
    "v_mfma_f32_16x16x32_bf16 v[188:191], a[20:23], a[60:63], v[188:191]\n"
    "s_waitcnt lgkmcnt(7)\n"
    "v_mfma_f32_16x16x32_bf16 v[192:195], a[0:3], a[48:51], v[192:195]\n"
    "ds_read_b64_tr_b16 a[12:13], v52 offset:29696\n"
    "ds_read_b64_tr_b16 a[14:15], v52 offset:44032\n"
    "v_mfma_f32_16x16x32_bf16 v[196:199], a[0:3], a[52:55], v[196:199]\n"
    "buffer_load_dwordx4 v70, s[60:63], s76 offen offset:2048 lds\n"
    "v_mfma_f32_16x16x32_bf16 v[200:203], a[0:3], a[56:59], v[200:203]\n"
    "s_mov_b32 s75, 286720\n"
    "ds_read_b128 a[44:47], v55 offset:19456\n"
    "v_mfma_f32_16x16x32_bf16 v[204:207], a[0:3], a[60:63], v[204:207]\n"
    "s_waitcnt vmcnt(7) lgkmcnt(7)\n"
    "s_barrier\n"
    "v_mfma_f32_16x16x32_bf16 v[208:211], a[4:7], a[48:51], v[208:211]\n"
    "s_add_u32 m0, s100, 57344\n"
    "ds_read_b64_tr_b16 a[16:17], v46 offset:0\n"
    "ds_read_b64_tr_b16 a[18:19], v46 offset:14336\n"
    "v_mfma_f32_16x16x32_bf16 v[212:215], a[4:7], a[52:55], v[212:215]\n"
    "buffer_load_dwordx4 v64, s[60:63], s75 offen offset:0 lds\n"
    "v_mfma_f32_16x16x32_bf16 v[216:219], a[4:7], a[56:59], v[216:219]\n"
    "v_mfma_f32_16x16x32_bf16 v[220:223], a[4:7], a[60:63], v[220:223]\n"
    "s_waitcnt lgkmcnt(6)\n"
    "v_mfma_f32_16x16x32_bf16 v[224:227], a[8:11], a[48:51], v[224:227]\n"
    "ds_read_b64_tr_b16 a[20:21], v47 offset:0\n"
    "ds_read_b64_tr_b16 a[22:23], v47 offset:14336\n"
    "v_mfma_f32_16x16x32_bf16 v[228:231], a[8:11], a[52:55], v[228:231]\n"
    "buffer_load_dwordx4 v65, s[60:63], s75 offen offset:1024 lds\n"
    "v_mfma_f32_16x16x32_bf16 v[232:235], a[8:11], a[56:59], v[232:235]\n"
    "v_mfma_f32_16x16x32_bf16 v[236:239], a[8:11], a[60:63], v[236:239]\n"
    "s_waitcnt lgkmcnt(5)\n"
    "v_mfma_f32_16x16x32_bf16 v[240:243], a[12:15], a[48:51], v[240:243]\n"
    "ds_read_b64_tr_b16 a[0:1], v48 offset:0\n"
    "ds_read_b64_tr_b16 a[2:3], v48 offset:14336\n"
    "v_mfma_f32_16x16x32_bf16 v[244:247], a[12:15], a[52:55], v[244:247]\n"
    "buffer_load_dwordx4 v66, s[60:63], s75 offen offset:2048 lds\n"
    "v_mfma_f32_16x16x32_bf16 v[248:251], a[12:15], a[56:59], v[248:251]\n"
    "v_mfma_f32_16x16x32_bf16 v[252:255], a[12:15], a[60:63], v[252:255]\n"
    "s_waitcnt lgkmcnt(4)\n"
    "v_mfma_f32_16x16x32_bf16 v[144:147], a[16:19], a[32:35], v[144:147]\n"
    "ds_read_b64_tr_b16 a[4:5], v49 offset:0\n"
    "ds_read_b64_tr_b16 a[6:7], v49 offset:14336\n"
    "v_mfma_f32_16x16x32_bf16 v[148:151], a[16:19], a[36:39], v[148:151]\n"
    "buffer_load_dwordx4 v67, s[60:63], s75 offen offset:3072 lds\n"
    "v_mfma_f32_16x16x32_bf16 v[152:155], a[16:19], a[40:43], v[152:155]\n"
    "ds_read_b128 a[48:51], v54 offset:20480\n"
    "v_mfma_f32_16x16x32_bf16 v[156:159], a[16:19], a[44:47], v[156:159]\n"
    "s_waitcnt lgkmcnt(5)\n"
    "v_mfma_f32_16x16x32_bf16 v[160:163], a[20:23], a[32:35], v[160:163]\n"
    "s_add_u32 m0, s100, 61440\n"
    "ds_read_b64_tr_b16 a[8:9], v46 offset:1024\n"
    "ds_read_b64_tr_b16 a[10:11], v46 offset:15360\n"
    "v_mfma_f32_16x16x32_bf16 v[164:167], a[20:23], a[36:39], v[164:167]\n"
    "buffer_load_dwordx4 v68, s[60:63], s75 offen offset:0 lds\n"
    "v_mfma_f32_16x16x32_bf16 v[168:171], a[20:23], a[40:43], v[168:171]\n"
    "ds_read_b128 a[52:55], v55 offset:21504\n"
    "v_mfma_f32_16x16x32_bf16 v[172:175], a[20:23], a[44:47], v[172:175]\n"
    "s_waitcnt lgkmcnt(6)\n"
    "v_mfma_f32_16x16x32_bf16 v[176:179], a[0:3], a[32:35], v[176:179]\n"
    "ds_read_b64_tr_b16 a[12:13], v47 offset:1024\n"
    "ds_read_b64_tr_b16 a[14:15], v47 offset:15360\n"
    "v_mfma_f32_16x16x32_bf16 v[180:183], a[0:3], a[36:39], v[180:183]\n"
    "buffer_load_dwordx4 v69, s[60:63], s75 offen offset:1024 lds\n"
    "v_mfma_f32_16x16x32_bf16 v[184:187], a[0:3], a[40:43], v[184:187]\n"
    "ds_read_b128 a[56:59], v54 offset:22528\n"
    "v_mfma_f32_16x16x32_bf16 v[188:191], a[0:3], a[44:47], v[188:191]\n"
    "s_waitcnt lgkmcnt(7)\n"
    "v_mfma_f32_16x16x32_bf16 v[192:195], a[4:7], a[32:35], v[192:195]\n"
    "ds_read_b64_tr_b16 a[16:17], v48 offset:1024\n"
    "ds_read_b64_tr_b16 a[18:19], v48 offset:15360\n"
    "v_mfma_f32_16x16x32_bf16 v[196:199], a[4:7], a[36:39], v[196:199]\n"
    "buffer_load_dwordx4 v70, s[60:63], s75 offen offset:2048 lds\n"
    "v_mfma_f32_16x16x32_bf16 v[200:203], a[4:7], a[40:43], v[200:203]\n"
    "s_mov_b32 s76, 315392\n"
    "ds_read_b128 a[60:63], v55 offset:23552\n"
    "v_mfma_f32_16x16x32_bf16 v[204:207], a[4:7], a[44:47], v[204:207]\n"
    "s_waitcnt vmcnt(7) lgkmcnt(7)\n"
    "s_barrier\n"
    "v_mfma_f32_16x16x32_bf16 v[208:211], a[8:11], a[32:35], v[208:211]\n"
    "s_add_u32 m0, s100, 86016\n"
    "ds_read_b64_tr_b16 a[20:21], v46 offset:28672\n"
    "ds_read_b64_tr_b16 a[22:23], v46 offset:43008\n"
    "v_mfma_f32_16x16x32_bf16 v[212:215], a[8:11], a[36:39], v[212:215]\n"
    "buffer_load_dwordx4 v64, s[60:63], s76 offen offset:0 lds\n"
    "v_mfma_f32_16x16x32_bf16 v[216:219], a[8:11], a[40:43], v[216:219]\n"
    "v_mfma_f32_16x16x32_bf16 v[220:223], a[8:11], a[44:47], v[220:223]\n"
    "s_waitcnt lgkmcnt(6)\n"
    "v_mfma_f32_16x16x32_bf16 v[224:227], a[12:15], a[32:35], v[224:227]\n"
    "ds_read_b64_tr_b16 a[0:1], v47 offset:28672\n"
    "ds_read_b64_tr_b16 a[2:3], v47 offset:43008\n"
    "v_mfma_f32_16x16x32_bf16 v[228:231], a[12:15], a[36:39], v[228:231]\n"
    "buffer_load_dwordx4 v65, s[60:63], s76 offen offset:1024 lds\n"
    "v_mfma_f32_16x16x32_bf16 v[232:235], a[12:15], a[40:43], v[232:235]\n"
    "v_mfma_f32_16x16x32_bf16 v[236:239], a[12:15], a[44:47], v[236:239]\n"
    "s_waitcnt lgkmcnt(5)\n"
    "v_mfma_f32_16x16x32_bf16 v[240:243], a[16:19], a[32:35], v[240:243]\n"
    "ds_read_b64_tr_b16 a[4:5], v48 offset:28672\n"
    "ds_read_b64_tr_b16 a[6:7], v48 offset:43008\n"
    "v_mfma_f32_16x16x32_bf16 v[244:247], a[16:19], a[36:39], v[244:247]\n"
    "buffer_load_dwordx4 v66, s[60:63], s76 offen offset:2048 lds\n"
    "v_mfma_f32_16x16x32_bf16 v[248:251], a[16:19], a[40:43], v[248:251]\n"
    "v_mfma_f32_16x16x32_bf16 v[252:255], a[16:19], a[44:47], v[252:255]\n"
    "s_waitcnt lgkmcnt(4)\n"
    "v_mfma_f32_16x16x32_bf16 v[144:147], a[20:23], a[48:51], v[144:147]\n"
    "ds_read_b64_tr_b16 a[8:9], v49 offset:28672\n"
    "ds_read_b64_tr_b16 a[10:11], v49 offset:43008\n"
    "v_mfma_f32_16x16x32_bf16 v[148:151], a[20:23], a[52:55], v[148:151]\n"
    "buffer_load_dwordx4 v67, s[60:63], s76 offen offset:3072 lds\n"
    "v_mfma_f32_16x16x32_bf16 v[152:155], a[20:23], a[56:59], v[152:155]\n"
    "ds_read_b128 a[32:35], v54 offset:24576\n"
    "v_mfma_f32_16x16x32_bf16 v[156:159], a[20:23], a[60:63], v[156:159]\n"
    "s_waitcnt lgkmcnt(5)\n"
    "v_mfma_f32_16x16x32_bf16 v[160:163], a[0:3], a[48:51], v[160:163]\n"
    "s_add_u32 m0, s100, 90112\n"
    "ds_read_b64_tr_b16 a[12:13], v46 offset:29696\n"
    "ds_read_b64_tr_b16 a[14:15], v46 offset:44032\n"
    "v_mfma_f32_16x16x32_bf16 v[164:167], a[0:3], a[52:55], v[164:167]\n"
    "buffer_load_dwordx4 v68, s[60:63], s76 offen offset:0 lds\n"
    "v_mfma_f32_16x16x32_bf16 v[168:171], a[0:3], a[56:59], v[168:171]\n"
    "ds_read_b128 a[36:39], v55 offset:25600\n"
    "v_mfma_f32_16x16x32_bf16 v[172:175], a[0:3], a[60:63], v[172:175]\n"
    "s_waitcnt lgkmcnt(6)\n"
    "v_mfma_f32_16x16x32_bf16 v[176:179], a[4:7], a[48:51], v[176:179]\n"
    "ds_read_b64_tr_b16 a[16:17], v47 offset:29696\n"
    "ds_read_b64_tr_b16 a[18:19], v47 offset:44032\n"
    "v_mfma_f32_16x16x32_bf16 v[180:183], a[4:7], a[52:55], v[180:183]\n"
    "buffer_load_dwordx4 v69, s[60:63], s76 offen offset:1024 lds\n"
    "v_mfma_f32_16x16x32_bf16 v[184:187], a[4:7], a[56:59], v[184:187]\n"
    "ds_read_b128 a[40:43], v54 offset:26624\n"
    "v_mfma_f32_16x16x32_bf16 v[188:191], a[4:7], a[60:63], v[188:191]\n"
    "s_waitcnt lgkmcnt(7)\n"
    "v_mfma_f32_16x16x32_bf16 v[192:195], a[8:11], a[48:51], v[192:195]\n"
    "ds_read_b64_tr_b16 a[20:21], v48 offset:29696\n"
    "ds_read_b64_tr_b16 a[22:23], v48 offset:44032\n"
    "v_mfma_f32_16x16x32_bf16 v[196:199], a[8:11], a[52:55], v[196:199]\n"
    "buffer_load_dwordx4 v70, s[60:63], s76 offen offset:2048 lds\n"
    "v_mfma_f32_16x16x32_bf16 v[200:203], a[8:11], a[56:59], v[200:203]\n"
    "s_mov_b32 s75, 114688\n"
    "ds_read_b128 a[44:47], v55 offset:27648\n"
    "v_mfma_f32_16x16x32_bf16 v[204:207], a[8:11], a[60:63], v[204:207]\n"
    "s_waitcnt vmcnt(7) lgkmcnt(7)\n"
    "s_barrier\n"
    "v_mfma_f32_16x16x32_bf16 v[208:211], a[12:15], a[48:51], v[208:211]\n"
    "s_add_u32 m0, s100, 0\n"
    "ds_read_b64_tr_b16 a[0:1], v50 offset:0\n"
    "ds_read_b64_tr_b16 a[2:3], v50 offset:14336\n"
    "v_mfma_f32_16x16x32_bf16 v[212:215], a[12:15], a[52:55], v[212:215]\n"
    "buffer_load_dwordx4 v64, s[60:63], s75 offen offset:0 lds\n"
    "v_mfma_f32_16x16x32_bf16 v[216:219], a[12:15], a[56:59], v[216:219]\n"
    "ds_write_b128 v56, v[112:115] offset:0\n"
    "v_mfma_f32_16x16x32_bf16 v[220:223], a[12:15], a[60:63], v[220:223]\n"
    "s_waitcnt lgkmcnt(7)\n"
    "v_mfma_f32_16x16x32_bf16 v[224:227], a[16:19], a[48:51], v[224:227]\n"
    "ds_read_b64_tr_b16 a[4:5], v51 offset:0\n"
    "ds_read_b64_tr_b16 a[6:7], v51 offset:14336\n"
    "v_mfma_f32_16x16x32_bf16 v[228:231], a[16:19], a[52:55], v[228:231]\n"
    "buffer_load_dwordx4 v65, s[60:63], s75 offen offset:1024 lds\n"
    "v_mfma_f32_16x16x32_bf16 v[232:235], a[16:19], a[56:59], v[232:235]\n"
    "ds_write_b128 v57, v[116:119] offset:0\n"
    "v_mfma_f32_16x16x32_bf16 v[236:239], a[16:19], a[60:63], v[236:239]\n"
    "s_waitcnt lgkmcnt(7)\n"
    "v_mfma_f32_16x16x32_bf16 v[240:243], a[20:23], a[48:51], v[240:243]\n"
    "ds_read_b64_tr_b16 a[8:9], v52 offset:0\n"
    "ds_read_b64_tr_b16 a[10:11], v52 offset:14336\n"
    "v_mfma_f32_16x16x32_bf16 v[244:247], a[20:23], a[52:55], v[244:247]\n"
    "buffer_load_dwordx4 v66, s[60:63], s75 offen offset:2048 lds\n"
    "v_mfma_f32_16x16x32_bf16 v[248:251], a[20:23], a[56:59], v[248:251]\n"
    "v_mfma_f32_16x16x32_bf16 v[252:255], a[20:23], a[60:63], v[252:255]\n"
    "s_waitcnt lgkmcnt(6)\n"
    "v_mfma_f32_16x16x32_bf16 v[144:147], a[0:3], a[32:35], v[144:147]\n"
    "ds_read_b64_tr_b16 a[12:13], v53 offset:0\n"
    "ds_read_b64_tr_b16 a[14:15], v53 offset:14336\n"
    "v_mfma_f32_16x16x32_bf16 v[148:151], a[0:3], a[36:39], v[148:151]\n"
    "buffer_load_dwordx4 v67, s[60:63], s75 offen offset:3072 lds\n"
    "v_mfma_f32_16x16x32_bf16 v[152:155], a[0:3], a[40:43], v[152:155]\n"
    "ds_read_b128 a[48:51], v54 offset:28672\n"
    "v_mfma_f32_16x16x32_bf16 v[156:159], a[0:3], a[44:47], v[156:159]\n"
    "s_waitcnt lgkmcnt(6)\n"
    "v_mfma_f32_16x16x32_bf16 v[160:163], a[4:7], a[32:35], v[160:163]\n"
    "s_add_u32 m0, s100, 4096\n"
    "ds_read_b64_tr_b16 a[16:17], v50 offset:1024\n"
    "ds_read_b64_tr_b16 a[18:19], v50 offset:15360\n"
    "v_mfma_f32_16x16x32_bf16 v[164:167], a[4:7], a[36:39], v[164:167]\n"
    "buffer_load_dwordx4 v68, s[60:63], s75 offen offset:0 lds\n"
    "v_mfma_f32_16x16x32_bf16 v[168:171], a[4:7], a[40:43], v[168:171]\n"
    "ds_read_b128 a[52:55], v55 offset:29696\n"
    "v_mfma_f32_16x16x32_bf16 v[172:175], a[4:7], a[44:47], v[172:175]\n"
    "s_waitcnt lgkmcnt(6)\n"
    "v_mfma_f32_16x16x32_bf16 v[176:179], a[8:11], a[32:35], v[176:179]\n"
    "ds_read_b64_tr_b16 a[20:21], v51 offset:1024\n"
    "ds_read_b64_tr_b16 a[22:23], v51 offset:15360\n"
    "v_mfma_f32_16x16x32_bf16 v[180:183], a[8:11], a[36:39], v[180:183]\n"
    "buffer_load_dwordx4 v69, s[60:63], s75 offen offset:1024 lds\n"
    "v_mfma_f32_16x16x32_bf16 v[184:187], a[8:11], a[40:43], v[184:187]\n"
    "ds_read_b128 a[56:59], v54 offset:30720\n"
    "v_mfma_f32_16x16x32_bf16 v[188:191], a[8:11], a[44:47], v[188:191]\n"
    "s_waitcnt lgkmcnt(7)\n"
    "v_mfma_f32_16x16x32_bf16 v[192:195], a[12:15], a[32:35], v[192:195]\n"
    "ds_read_b64_tr_b16 a[0:1], v52 offset:1024\n"
    "ds_read_b64_tr_b16 a[2:3], v52 offset:15360\n"
    "v_mfma_f32_16x16x32_bf16 v[196:199], a[12:15], a[36:39], v[196:199]\n"
    "buffer_load_dwordx4 v70, s[60:63], s75 offen offset:2048 lds\n"
    "v_mfma_f32_16x16x32_bf16 v[200:203], a[12:15], a[40:43], v[200:203]\n"
    "s_mov_b32 s76, 143360\n"
    "ds_read_b128 a[60:63], v55 offset:31744\n"
    "v_mfma_f32_16x16x32_bf16 v[204:207], a[12:15], a[44:47], v[204:207]\n"
    "s_waitcnt vmcnt(7) lgkmcnt(7)\n"
    "s_barrier\n"
    "v_mfma_f32_16x16x32_bf16 v[208:211], a[16:19], a[32:35], v[208:211]\n"
    "s_add_u32 m0, s100, 28672\n"
    "ds_read_b64_tr_b16 a[4:5], v50 offset:28672\n"
    "ds_read_b64_tr_b16 a[6:7], v50 offset:43008\n"
    "v_mfma_f32_16x16x32_bf16 v[212:215], a[16:19], a[36:39], v[212:215]\n"
    "buffer_load_dwordx4 v64, s[60:63], s76 offen offset:0 lds\n"
    "v_mfma_f32_16x16x32_bf16 v[216:219], a[16:19], a[40:43], v[216:219]\n"
    "ds_write_b128 v56, v[120:123] offset:4096\n"
    "v_mfma_f32_16x16x32_bf16 v[220:223], a[16:19], a[44:47], v[220:223]\n"
    "s_waitcnt lgkmcnt(7)\n"
    "v_mfma_f32_16x16x32_bf16 v[224:227], a[20:23], a[32:35], v[224:227]\n"
    "ds_read_b64_tr_b16 a[8:9], v51 offset:28672\n"
    "ds_read_b64_tr_b16 a[10:11], v51 offset:43008\n"
    "v_mfma_f32_16x16x32_bf16 v[228:231], a[20:23], a[36:39], v[228:231]\n"
    "buffer_load_dwordx4 v65, s[60:63], s76 offen offset:1024 lds\n"
    "v_mfma_f32_16x16x32_bf16 v[232:235], a[20:23], a[40:43], v[232:235]\n"
    "ds_write_b128 v57, v[124:127] offset:4096\n"
    "v_mfma_f32_16x16x32_bf16 v[236:239], a[20:23], a[44:47], v[236:239]\n"
    "s_waitcnt lgkmcnt(7)\n"
    "v_mfma_f32_16x16x32_bf16 v[240:243], a[0:3], a[32:35], v[240:243]\n"
    "ds_read_b64_tr_b16 a[12:13], v52 offset:28672\n"
    "ds_read_b64_tr_b16 a[14:15], v52 offset:43008\n"
    "v_mfma_f32_16x16x32_bf16 v[244:247], a[0:3], a[36:39], v[244:247]\n"
    "buffer_load_dwordx4 v66, s[60:63], s76 offen offset:2048 lds\n"
    "v_mfma_f32_16x16x32_bf16 v[248:251], a[0:3], a[40:43], v[248:251]\n"
    "v_mfma_f32_16x16x32_bf16 v[252:255], a[0:3], a[44:47], v[252:255]\n"
    "s_waitcnt lgkmcnt(6)\n"
    "v_mfma_f32_16x16x32_bf16 v[144:147], a[4:7], a[48:51], v[144:147]\n"
    "ds_read_b64_tr_b16 a[16:17], v53 offset:28672\n"
    "ds_read_b64_tr_b16 a[18:19], v53 offset:43008\n"
    "v_mfma_f32_16x16x32_bf16 v[148:151], a[4:7], a[52:55], v[148:151]\n"
    "buffer_load_dwordx4 v67, s[60:63], s76 offen offset:3072 lds\n"
    "v_mfma_f32_16x16x32_bf16 v[152:155], a[4:7], a[56:59], v[152:155]\n"
    "ds_read_b128 a[32:35], v54 offset:0\n"
    "v_mfma_f32_16x16x32_bf16 v[156:159], a[4:7], a[60:63], v[156:159]\n"
    "s_waitcnt lgkmcnt(6)\n"
    "v_mfma_f32_16x16x32_bf16 v[160:163], a[8:11], a[48:51], v[160:163]\n"
    "s_add_u32 m0, s100, 32768\n"
    "ds_read_b64_tr_b16 a[20:21], v50 offset:29696\n"
    "ds_read_b64_tr_b16 a[22:23], v50 offset:44032\n"
    "v_mfma_f32_16x16x32_bf16 v[164:167], a[8:11], a[52:55], v[164:167]\n"
    "buffer_load_dwordx4 v68, s[60:63], s76 offen offset:0 lds\n"
    "v_mfma_f32_16x16x32_bf16 v[168:171], a[8:11], a[56:59], v[168:171]\n"
    "ds_read_b128 a[36:39], v55 offset:1024\n"
    "v_mfma_f32_16x16x32_bf16 v[172:175], a[8:11], a[60:63], v[172:175]\n"
    "s_waitcnt lgkmcnt(6)\n"
    "v_mfma_f32_16x16x32_bf16 v[176:179], a[12:15], a[48:51], v[176:179]\n"
    "ds_read_b64_tr_b16 a[0:1], v51 offset:29696\n"
    "ds_read_b64_tr_b16 a[2:3], v51 offset:44032\n"
    "v_mfma_f32_16x16x32_bf16 v[180:183], a[12:15], a[52:55], v[180:183]\n"
    "buffer_load_dwordx4 v69, s[60:63], s76 offen offset:1024 lds\n"
    "v_mfma_f32_16x16x32_bf16 v[184:187], a[12:15], a[56:59], v[184:187]\n"
    "ds_read_b128 a[40:43], v54 offset:2048\n"
    "v_mfma_f32_16x16x32_bf16 v[188:191], a[12:15], a[60:63], v[188:191]\n"
    "s_waitcnt lgkmcnt(7)\n"
    "v_mfma_f32_16x16x32_bf16 v[192:195], a[16:19], a[48:51], v[192:195]\n"
    "ds_read_b64_tr_b16 a[4:5], v52 offset:29696\n"
    "ds_read_b64_tr_b16 a[6:7], v52 offset:44032\n"
    "v_mfma_f32_16x16x32_bf16 v[196:199], a[16:19], a[52:55], v[196:199]\n"
    "buffer_load_dwordx4 v70, s[60:63], s76 offen offset:2048 lds\n"
    "v_mfma_f32_16x16x32_bf16 v[200:203], a[16:19], a[56:59], v[200:203]\n"
    "s_mov_b32 s75, 172032\n"
    "ds_read_b128 a[44:47], v55 offset:3072\n"
    "v_mfma_f32_16x16x32_bf16 v[204:207], a[16:19], a[60:63], v[204:207]\n"
    "s_waitcnt vmcnt(7) lgkmcnt(7)\n"
    "s_barrier\n"
    "v_mfma_f32_16x16x32_bf16 v[208:211], a[20:23], a[48:51], v[208:211]\n"
    "s_add_u32 m0, s100, 57344\n"
    "ds_read_b64_tr_b16 a[8:9], v46 offset:0\n"
    "ds_read_b64_tr_b16 a[10:11], v46 offset:14336\n"
    "v_mfma_f32_16x16x32_bf16 v[212:215], a[20:23], a[52:55], v[212:215]\n"
    "buffer_load_dwordx4 v64, s[60:63], s75 offen offset:0 lds\n"
    "v_mfma_f32_16x16x32_bf16 v[216:219], a[20:23], a[56:59], v[216:219]\n"
    "ds_write_b128 v56, v[128:131] offset:8192\n"
    "v_mfma_f32_16x16x32_bf16 v[220:223], a[20:23], a[60:63], v[220:223]\n"
    "s_waitcnt lgkmcnt(7)\n"
    "v_mfma_f32_16x16x32_bf16 v[224:227], a[0:3], a[48:51], v[224:227]\n"
    "ds_read_b64_tr_b16 a[12:13], v47 offset:0\n"
    "ds_read_b64_tr_b16 a[14:15], v47 offset:14336\n"
    "v_mfma_f32_16x16x32_bf16 v[228:231], a[0:3], a[52:55], v[228:231]\n"
    "buffer_load_dwordx4 v65, s[60:63], s75 offen offset:1024 lds\n"
    "v_mfma_f32_16x16x32_bf16 v[232:235], a[0:3], a[56:59], v[232:235]\n"
    "ds_write_b128 v57, v[132:135] offset:8192\n"
    "v_mfma_f32_16x16x32_bf16 v[236:239], a[0:3], a[60:63], v[236:239]\n"
    "s_waitcnt lgkmcnt(7)\n"
    "v_mfma_f32_16x16x32_bf16 v[240:243], a[4:7], a[48:51], v[240:243]\n"
    "ds_read_b64_tr_b16 a[16:17], v48 offset:0\n"
    "ds_read_b64_tr_b16 a[18:19], v48 offset:14336\n"
    "v_mfma_f32_16x16x32_bf16 v[244:247], a[4:7], a[52:55], v[244:247]\n"
    "buffer_load_dwordx4 v66, s[60:63], s75 offen offset:2048 lds\n"
    "v_mfma_f32_16x16x32_bf16 v[248:251], a[4:7], a[56:59], v[248:251]\n"
    "v_mfma_f32_16x16x32_bf16 v[252:255], a[4:7], a[60:63], v[252:255]\n"
    "s_waitcnt lgkmcnt(6)\n"
    "v_mfma_f32_16x16x32_bf16 v[144:147], a[8:11], a[32:35], v[144:147]\n"
    "ds_read_b64_tr_b16 a[20:21], v49 offset:0\n"
    "ds_read_b64_tr_b16 a[22:23], v49 offset:14336\n"
    "v_mfma_f32_16x16x32_bf16 v[148:151], a[8:11], a[36:39], v[148:151]\n"
    "buffer_load_dwordx4 v67, s[60:63], s75 offen offset:3072 lds\n"
    "v_mfma_f32_16x16x32_bf16 v[152:155], a[8:11], a[40:43], v[152:155]\n"
    "ds_read_b128 a[48:51], v54 offset:4096\n"
    "v_mfma_f32_16x16x32_bf16 v[156:159], a[8:11], a[44:47], v[156:159]\n"
    "s_waitcnt lgkmcnt(6)\n"
    "v_mfma_f32_16x16x32_bf16 v[160:163], a[12:15], a[32:35], v[160:163]\n"
    "s_add_u32 m0, s100, 61440\n"
    "ds_read_b64_tr_b16 a[0:1], v46 offset:1024\n"
    "ds_read_b64_tr_b16 a[2:3], v46 offset:15360\n"
    "v_mfma_f32_16x16x32_bf16 v[164:167], a[12:15], a[36:39], v[164:167]\n"
    "buffer_load_dwordx4 v68, s[60:63], s75 offen offset:0 lds\n"
    "v_mfma_f32_16x16x32_bf16 v[168:171], a[12:15], a[40:43], v[168:171]\n"
    "ds_read_b128 a[52:55], v55 offset:5120\n"
    "v_mfma_f32_16x16x32_bf16 v[172:175], a[12:15], a[44:47], v[172:175]\n"
    "s_waitcnt lgkmcnt(6)\n"
    "v_mfma_f32_16x16x32_bf16 v[176:179], a[16:19], a[32:35], v[176:179]\n"
    "ds_read_b64_tr_b16 a[4:5], v47 offset:1024\n"
    "ds_read_b64_tr_b16 a[6:7], v47 offset:15360\n"
    "v_mfma_f32_16x16x32_bf16 v[180:183], a[16:19], a[36:39], v[180:183]\n"
    "buffer_load_dwordx4 v69, s[60:63], s75 offen offset:1024 lds\n"
    "v_mfma_f32_16x16x32_bf16 v[184:187], a[16:19], a[40:43], v[184:187]\n"
    "ds_read_b128 a[56:59], v54 offset:6144\n"
    "v_mfma_f32_16x16x32_bf16 v[188:191], a[16:19], a[44:47], v[188:191]\n"
    "s_waitcnt lgkmcnt(7)\n"
    "v_mfma_f32_16x16x32_bf16 v[192:195], a[20:23], a[32:35], v[192:195]\n"
    "ds_read_b64_tr_b16 a[8:9], v48 offset:1024\n"
    "ds_read_b64_tr_b16 a[10:11], v48 offset:15360\n"
    "v_mfma_f32_16x16x32_bf16 v[196:199], a[20:23], a[36:39], v[196:199]\n"
    "buffer_load_dwordx4 v70, s[60:63], s75 offen offset:2048 lds\n"
    "v_mfma_f32_16x16x32_bf16 v[200:203], a[20:23], a[40:43], v[200:203]\n"
    "s_mov_b32 s76, 200704\n"
    "ds_read_b128 a[60:63], v55 offset:7168\n"
    "v_mfma_f32_16x16x32_bf16 v[204:207], a[20:23], a[44:47], v[204:207]\n"
    "s_waitcnt vmcnt(7) lgkmcnt(7)\n"
    "s_barrier\n"
    "v_mfma_f32_16x16x32_bf16 v[208:211], a[0:3], a[32:35], v[208:211]\n"
    "s_add_u32 m0, s100, 86016\n"
    "ds_read_b64_tr_b16 a[12:13], v46 offset:28672\n"
    "ds_read_b64_tr_b16 a[14:15], v46 offset:43008\n"
    "v_mfma_f32_16x16x32_bf16 v[212:215], a[0:3], a[36:39], v[212:215]\n"
    "buffer_load_dwordx4 v64, s[60:63], s76 offen offset:0 lds\n"
    "v_mfma_f32_16x16x32_bf16 v[216:219], a[0:3], a[40:43], v[216:219]\n"
    "ds_write_b128 v56, v[136:139] offset:12288\n"
    "v_mfma_f32_16x16x32_bf16 v[220:223], a[0:3], a[44:47], v[220:223]\n"
    "s_waitcnt lgkmcnt(7)\n"
    "v_mfma_f32_16x16x32_bf16 v[224:227], a[4:7], a[32:35], v[224:227]\n"
    "ds_read_b64_tr_b16 a[16:17], v47 offset:28672\n"
    "ds_read_b64_tr_b16 a[18:19], v47 offset:43008\n"
    "v_mfma_f32_16x16x32_bf16 v[228:231], a[4:7], a[36:39], v[228:231]\n"
    "buffer_load_dwordx4 v65, s[60:63], s76 offen offset:1024 lds\n"
    "v_mfma_f32_16x16x32_bf16 v[232:235], a[4:7], a[40:43], v[232:235]\n"
    "ds_write_b128 v57, v[140:143] offset:12288\n"
    "v_mfma_f32_16x16x32_bf16 v[236:239], a[4:7], a[44:47], v[236:239]\n"
    "s_waitcnt lgkmcnt(7)\n"
    "v_mfma_f32_16x16x32_bf16 v[240:243], a[8:11], a[32:35], v[240:243]\n"
    "ds_read_b64_tr_b16 a[20:21], v48 offset:28672\n"
    "ds_read_b64_tr_b16 a[22:23], v48 offset:43008\n"
    "v_mfma_f32_16x16x32_bf16 v[244:247], a[8:11], a[36:39], v[244:247]\n"
    "buffer_load_dwordx4 v66, s[60:63], s76 offen offset:2048 lds\n"
    "v_mfma_f32_16x16x32_bf16 v[248:251], a[8:11], a[40:43], v[248:251]\n"
    "v_mfma_f32_16x16x32_bf16 v[252:255], a[8:11], a[44:47], v[252:255]\n"
    "s_waitcnt lgkmcnt(6)\n"
    "v_mfma_f32_16x16x32_bf16 v[144:147], a[12:15], a[48:51], v[144:147]\n"
    "ds_read_b64_tr_b16 a[0:1], v49 offset:28672\n"
    "ds_read_b64_tr_b16 a[2:3], v49 offset:43008\n"
    "v_mfma_f32_16x16x32_bf16 v[148:151], a[12:15], a[52:55], v[148:151]\n"
    "buffer_load_dwordx4 v67, s[60:63], s76 offen offset:3072 lds\n"
    "v_mfma_f32_16x16x32_bf16 v[152:155], a[12:15], a[56:59], v[152:155]\n"
    "ds_read_b128 a[32:35], v54 offset:8192\n"
    "v_mfma_f32_16x16x32_bf16 v[156:159], a[12:15], a[60:63], v[156:159]\n"
    "s_waitcnt lgkmcnt(6)\n"
    "v_mfma_f32_16x16x32_bf16 v[160:163], a[16:19], a[48:51], v[160:163]\n"
    "s_add_u32 m0, s100, 90112\n"
    "ds_read_b64_tr_b16 a[4:5], v46 offset:29696\n"
    "ds_read_b64_tr_b16 a[6:7], v46 offset:44032\n"
    "v_mfma_f32_16x16x32_bf16 v[164:167], a[16:19], a[52:55], v[164:167]\n"
    "buffer_load_dwordx4 v68, s[60:63], s76 offen offset:0 lds\n"
    "v_mfma_f32_16x16x32_bf16 v[168:171], a[16:19], a[56:59], v[168:171]\n"
    "ds_read_b128 a[36:39], v55 offset:9216\n"
    "v_mfma_f32_16x16x32_bf16 v[172:175], a[16:19], a[60:63], v[172:175]\n"
    "s_waitcnt lgkmcnt(6)\n"
    "v_mfma_f32_16x16x32_bf16 v[176:179], a[20:23], a[48:51], v[176:179]\n"
    "ds_read_b64_tr_b16 a[8:9], v47 offset:29696\n"
    "ds_read_b64_tr_b16 a[10:11], v47 offset:44032\n"
    "v_mfma_f32_16x16x32_bf16 v[180:183], a[20:23], a[52:55], v[180:183]\n"
    "buffer_load_dwordx4 v69, s[60:63], s76 offen offset:1024 lds\n"
    "v_mfma_f32_16x16x32_bf16 v[184:187], a[20:23], a[56:59], v[184:187]\n"
    "ds_read_b128 a[40:43], v54 offset:10240\n"
    "v_mfma_f32_16x16x32_bf16 v[188:191], a[20:23], a[60:63], v[188:191]\n"
    "s_waitcnt lgkmcnt(7)\n"
    "v_mfma_f32_16x16x32_bf16 v[192:195], a[0:3], a[48:51], v[192:195]\n"
    "ds_read_b64_tr_b16 a[12:13], v48 offset:29696\n"
    "ds_read_b64_tr_b16 a[14:15], v48 offset:44032\n"
    "v_mfma_f32_16x16x32_bf16 v[196:199], a[0:3], a[52:55], v[196:199]\n"
    "buffer_load_dwordx4 v70, s[60:63], s76 offen offset:2048 lds\n"
    "v_mfma_f32_16x16x32_bf16 v[200:203], a[0:3], a[56:59], v[200:203]\n"
    "s_mov_b32 s75, 344064\n"
    "ds_read_b128 a[44:47], v55 offset:11264\n"
    "v_mfma_f32_16x16x32_bf16 v[204:207], a[0:3], a[60:63], v[204:207]\n"
    "s_waitcnt vmcnt(7) lgkmcnt(7)\n"
    "s_barrier\n"
    "v_mfma_f32_16x16x32_bf16 v[208:211], a[4:7], a[48:51], v[208:211]\n"
    "s_add_u32 m0, s100, 0\n"
    "ds_read_b64_tr_b16 a[16:17], v50 offset:0\n"
    "ds_read_b64_tr_b16 a[18:19], v50 offset:14336\n"
    "v_mfma_f32_16x16x32_bf16 v[212:215], a[4:7], a[52:55], v[212:215]\n"
    "buffer_load_dwordx4 v64, s[60:63], s75 offen offset:0 lds\n"
    "v_mfma_f32_16x16x32_bf16 v[216:219], a[4:7], a[56:59], v[216:219]\n"
    "v_mfma_f32_16x16x32_bf16 v[220:223], a[4:7], a[60:63], v[220:223]\n"
    "s_waitcnt lgkmcnt(6)\n"
    "v_mfma_f32_16x16x32_bf16 v[224:227], a[8:11], a[48:51], v[224:227]\n"
    "ds_read_b64_tr_b16 a[20:21], v51 offset:0\n"
    "ds_read_b64_tr_b16 a[22:23], v51 offset:14336\n"
    "v_mfma_f32_16x16x32_bf16 v[228:231], a[8:11], a[52:55], v[228:231]\n"
    "buffer_load_dwordx4 v65, s[60:63], s75 offen offset:1024 lds\n"
    "v_mfma_f32_16x16x32_bf16 v[232:235], a[8:11], a[56:59], v[232:235]\n"
    "v_mfma_f32_16x16x32_bf16 v[236:239], a[8:11], a[60:63], v[236:239]\n"
    "s_waitcnt lgkmcnt(5)\n"
    "v_mfma_f32_16x16x32_bf16 v[240:243], a[12:15], a[48:51], v[240:243]\n"
    "ds_read_b64_tr_b16 a[0:1], v52 offset:0\n"
    "ds_read_b64_tr_b16 a[2:3], v52 offset:14336\n"
    "v_mfma_f32_16x16x32_bf16 v[244:247], a[12:15], a[52:55], v[244:247]\n"
    "buffer_load_dwordx4 v66, s[60:63], s75 offen offset:2048 lds\n"
    "v_mfma_f32_16x16x32_bf16 v[248:251], a[12:15], a[56:59], v[248:251]\n"
    "v_mfma_f32_16x16x32_bf16 v[252:255], a[12:15], a[60:63], v[252:255]\n"
    "s_waitcnt lgkmcnt(4)\n"
    "v_mfma_f32_16x16x32_bf16 v[144:147], a[16:19], a[32:35], v[144:147]\n"
    "ds_read_b64_tr_b16 a[4:5], v53 offset:0\n"
    "ds_read_b64_tr_b16 a[6:7], v53 offset:14336\n"
    "v_mfma_f32_16x16x32_bf16 v[148:151], a[16:19], a[36:39], v[148:151]\n"
    "buffer_load_dwordx4 v67, s[60:63], s75 offen offset:3072 lds\n"
    "v_mfma_f32_16x16x32_bf16 v[152:155], a[16:19], a[40:43], v[152:155]\n"
    "ds_read_b128 a[48:51], v54 offset:12288\n"
    "v_mfma_f32_16x16x32_bf16 v[156:159], a[16:19], a[44:47], v[156:159]\n"
    "s_waitcnt lgkmcnt(5)\n"
    "v_mfma_f32_16x16x32_bf16 v[160:163], a[20:23], a[32:35], v[160:163]\n"
    "s_add_u32 m0, s100, 4096\n"
    "ds_read_b64_tr_b16 a[8:9], v50 offset:1024\n"
    "ds_read_b64_tr_b16 a[10:11], v50 offset:15360\n"
    "v_mfma_f32_16x16x32_bf16 v[164:167], a[20:23], a[36:39], v[164:167]\n"
    "buffer_load_dwordx4 v68, s[60:63], s75 offen offset:0 lds\n"
    "v_mfma_f32_16x16x32_bf16 v[168:171], a[20:23], a[40:43], v[168:171]\n"
    "ds_read_b128 a[52:55], v55 offset:13312\n"
    "v_mfma_f32_16x16x32_bf16 v[172:175], a[20:23], a[44:47], v[172:175]\n"
    "s_waitcnt lgkmcnt(6)\n"
    "v_mfma_f32_16x16x32_bf16 v[176:179], a[0:3], a[32:35], v[176:179]\n"
    "ds_read_b64_tr_b16 a[12:13], v51 offset:1024\n"
    "ds_read_b64_tr_b16 a[14:15], v51 offset:15360\n"
    "v_mfma_f32_16x16x32_bf16 v[180:183], a[0:3], a[36:39], v[180:183]\n"
    "buffer_load_dwordx4 v69, s[60:63], s75 offen offset:1024 lds\n"
    "v_mfma_f32_16x16x32_bf16 v[184:187], a[0:3], a[40:43], v[184:187]\n"
    "ds_read_b128 a[56:59], v54 offset:14336\n"
    "v_mfma_f32_16x16x32_bf16 v[188:191], a[0:3], a[44:47], v[188:191]\n"
    "s_waitcnt lgkmcnt(7)\n"
    "v_mfma_f32_16x16x32_bf16 v[192:195], a[4:7], a[32:35], v[192:195]\n"
    "ds_read_b64_tr_b16 a[16:17], v52 offset:1024\n"
    "ds_read_b64_tr_b16 a[18:19], v52 offset:15360\n"
    "v_mfma_f32_16x16x32_bf16 v[196:199], a[4:7], a[36:39], v[196:199]\n"
    "buffer_load_dwordx4 v70, s[60:63], s75 offen offset:2048 lds\n"
    "v_mfma_f32_16x16x32_bf16 v[200:203], a[4:7], a[40:43], v[200:203]\n"
    "s_mov_b32 s76, 372736\n"
    "ds_read_b128 a[60:63], v55 offset:15360\n"
    "v_mfma_f32_16x16x32_bf16 v[204:207], a[4:7], a[44:47], v[204:207]\n"
    "s_waitcnt vmcnt(7) lgkmcnt(7)\n"
    "s_barrier\n"
    "v_mfma_f32_16x16x32_bf16 v[208:211], a[8:11], a[32:35], v[208:211]\n"
    "s_add_u32 m0, s100, 28672\n"
    "ds_read_b64_tr_b16 a[20:21], v50 offset:28672\n"
    "ds_read_b64_tr_b16 a[22:23], v50 offset:43008\n"
    "v_mfma_f32_16x16x32_bf16 v[212:215], a[8:11], a[36:39], v[212:215]\n"
    "buffer_load_dwordx4 v64, s[60:63], s76 offen offset:0 lds\n"
    "v_mfma_f32_16x16x32_bf16 v[216:219], a[8:11], a[40:43], v[216:219]\n"
    "v_mfma_f32_16x16x32_bf16 v[220:223], a[8:11], a[44:47], v[220:223]\n"
    "s_waitcnt lgkmcnt(6)\n"
    "v_mfma_f32_16x16x32_bf16 v[224:227], a[12:15], a[32:35], v[224:227]\n"
    "ds_read_b64_tr_b16 a[0:1], v51 offset:28672\n"
    "ds_read_b64_tr_b16 a[2:3], v51 offset:43008\n"
    "v_mfma_f32_16x16x32_bf16 v[228:231], a[12:15], a[36:39], v[228:231]\n"
    "buffer_load_dwordx4 v65, s[60:63], s76 offen offset:1024 lds\n"
    "v_mfma_f32_16x16x32_bf16 v[232:235], a[12:15], a[40:43], v[232:235]\n"
    "v_mfma_f32_16x16x32_bf16 v[236:239], a[12:15], a[44:47], v[236:239]\n"
    "s_waitcnt lgkmcnt(5)\n"
    "v_mfma_f32_16x16x32_bf16 v[240:243], a[16:19], a[32:35], v[240:243]\n"
    "ds_read_b64_tr_b16 a[4:5], v52 offset:28672\n"
    "ds_read_b64_tr_b16 a[6:7], v52 offset:43008\n"
    "v_mfma_f32_16x16x32_bf16 v[244:247], a[16:19], a[36:39], v[244:247]\n"
    "buffer_load_dwordx4 v66, s[60:63], s76 offen offset:2048 lds\n"
    "v_mfma_f32_16x16x32_bf16 v[248:251], a[16:19], a[40:43], v[248:251]\n"
    "v_mfma_f32_16x16x32_bf16 v[252:255], a[16:19], a[44:47], v[252:255]\n"
    "s_waitcnt lgkmcnt(4)\n"
    "v_mfma_f32_16x16x32_bf16 v[144:147], a[20:23], a[48:51], v[144:147]\n"
    "ds_read_b64_tr_b16 a[8:9], v53 offset:28672\n"
    "ds_read_b64_tr_b16 a[10:11], v53 offset:43008\n"
    "v_mfma_f32_16x16x32_bf16 v[148:151], a[20:23], a[52:55], v[148:151]\n"
    "buffer_load_dwordx4 v67, s[60:63], s76 offen offset:3072 lds\n"
    "v_mfma_f32_16x16x32_bf16 v[152:155], a[20:23], a[56:59], v[152:155]\n"
    "ds_read_b128 a[32:35], v54 offset:16384\n"
    "v_mfma_f32_16x16x32_bf16 v[156:159], a[20:23], a[60:63], v[156:159]\n"
    "s_waitcnt lgkmcnt(5)\n"
    "v_mfma_f32_16x16x32_bf16 v[160:163], a[0:3], a[48:51], v[160:163]\n"
    "s_add_u32 m0, s100, 32768\n"
    "ds_read_b64_tr_b16 a[12:13], v50 offset:29696\n"
    "ds_read_b64_tr_b16 a[14:15], v50 offset:44032\n"
    "v_mfma_f32_16x16x32_bf16 v[164:167], a[0:3], a[52:55], v[164:167]\n"
    "buffer_load_dwordx4 v68, s[60:63], s76 offen offset:0 lds\n"
    "v_mfma_f32_16x16x32_bf16 v[168:171], a[0:3], a[56:59], v[168:171]\n"
    "ds_read_b128 a[36:39], v55 offset:17408\n"
    "v_mfma_f32_16x16x32_bf16 v[172:175], a[0:3], a[60:63], v[172:175]\n"
    "s_waitcnt lgkmcnt(6)\n"
    "v_mfma_f32_16x16x32_bf16 v[176:179], a[4:7], a[48:51], v[176:179]\n"
    "ds_read_b64_tr_b16 a[16:17], v51 offset:29696\n"
    "ds_read_b64_tr_b16 a[18:19], v51 offset:44032\n"
    "v_mfma_f32_16x16x32_bf16 v[180:183], a[4:7], a[52:55], v[180:183]\n"
    "buffer_load_dwordx4 v69, s[60:63], s76 offen offset:1024 lds\n"
    "v_mfma_f32_16x16x32_bf16 v[184:187], a[4:7], a[56:59], v[184:187]\n"
    "ds_read_b128 a[40:43], v54 offset:18432\n"
    "v_mfma_f32_16x16x32_bf16 v[188:191], a[4:7], a[60:63], v[188:191]\n"
    "s_waitcnt lgkmcnt(7)\n"
    "v_mfma_f32_16x16x32_bf16 v[192:195], a[8:11], a[48:51], v[192:195]\n"
    "ds_read_b64_tr_b16 a[20:21], v52 offset:29696\n"
    "ds_read_b64_tr_b16 a[22:23], v52 offset:44032\n"
    "v_mfma_f32_16x16x32_bf16 v[196:199], a[8:11], a[52:55], v[196:199]\n"
    "buffer_load_dwordx4 v70, s[60:63], s76 offen offset:2048 lds\n"
    "v_mfma_f32_16x16x32_bf16 v[200:203], a[8:11], a[56:59], v[200:203]\n"
    "s_mov_b32 s75, 401408\n"
    "ds_read_b128 a[44:47], v55 offset:19456\n"
    "v_mfma_f32_16x16x32_bf16 v[204:207], a[8:11], a[60:63], v[204:207]\n"
    "s_waitcnt vmcnt(7) lgkmcnt(7)\n"
    "s_barrier\n"
    "v_mfma_f32_16x16x32_bf16 v[208:211], a[12:15], a[48:51], v[208:211]\n"
    "s_add_u32 m0, s100, 57344\n"
    "ds_read_b64_tr_b16 a[0:1], v46 offset:0\n"
    "ds_read_b64_tr_b16 a[2:3], v46 offset:14336\n"
    "v_mfma_f32_16x16x32_bf16 v[212:215], a[12:15], a[52:55], v[212:215]\n"
    "buffer_load_dwordx4 v64, s[60:63], s75 offen offset:0 lds\n"
    "v_mfma_f32_16x16x32_bf16 v[216:219], a[12:15], a[56:59], v[216:219]\n"
    "v_mfma_f32_16x16x32_bf16 v[220:223], a[12:15], a[60:63], v[220:223]\n"
    "s_waitcnt lgkmcnt(6)\n"
    "v_mfma_f32_16x16x32_bf16 v[224:227], a[16:19], a[48:51], v[224:227]\n"
    "ds_read_b64_tr_b16 a[4:5], v47 offset:0\n"
    "ds_read_b64_tr_b16 a[6:7], v47 offset:14336\n"
    "v_mfma_f32_16x16x32_bf16 v[228:231], a[16:19], a[52:55], v[228:231]\n"
    "buffer_load_dwordx4 v65, s[60:63], s75 offen offset:1024 lds\n"
    "v_mfma_f32_16x16x32_bf16 v[232:235], a[16:19], a[56:59], v[232:235]\n"
    "v_mfma_f32_16x16x32_bf16 v[236:239], a[16:19], a[60:63], v[236:239]\n"
    "s_waitcnt lgkmcnt(5)\n"
    "v_mfma_f32_16x16x32_bf16 v[240:243], a[20:23], a[48:51], v[240:243]\n"
    "ds_read_b64_tr_b16 a[8:9], v48 offset:0\n"
    "ds_read_b64_tr_b16 a[10:11], v48 offset:14336\n"
    "v_mfma_f32_16x16x32_bf16 v[244:247], a[20:23], a[52:55], v[244:247]\n"
    "buffer_load_dwordx4 v66, s[60:63], s75 offen offset:2048 lds\n"
    "v_mfma_f32_16x16x32_bf16 v[248:251], a[20:23], a[56:59], v[248:251]\n"
    "v_mfma_f32_16x16x32_bf16 v[252:255], a[20:23], a[60:63], v[252:255]\n"
    "s_waitcnt lgkmcnt(4)\n"
    "v_mfma_f32_16x16x32_bf16 v[144:147], a[0:3], a[32:35], v[144:147]\n"
    "ds_read_b64_tr_b16 a[12:13], v49 offset:0\n"
    "ds_read_b64_tr_b16 a[14:15], v49 offset:14336\n"
    "v_mfma_f32_16x16x32_bf16 v[148:151], a[0:3], a[36:39], v[148:151]\n"
    "buffer_load_dwordx4 v67, s[60:63], s75 offen offset:3072 lds\n"
    "v_mfma_f32_16x16x32_bf16 v[152:155], a[0:3], a[40:43], v[152:155]\n"
    "ds_read_b128 a[48:51], v54 offset:20480\n"
    "v_mfma_f32_16x16x32_bf16 v[156:159], a[0:3], a[44:47], v[156:159]\n"
    "s_waitcnt lgkmcnt(5)\n"
    "v_mfma_f32_16x16x32_bf16 v[160:163], a[4:7], a[32:35], v[160:163]\n"
    "s_add_u32 m0, s100, 61440\n"
    "ds_read_b64_tr_b16 a[16:17], v46 offset:1024\n"
    "ds_read_b64_tr_b16 a[18:19], v46 offset:15360\n"
    "v_mfma_f32_16x16x32_bf16 v[164:167], a[4:7], a[36:39], v[164:167]\n"
    "buffer_load_dwordx4 v68, s[60:63], s75 offen offset:0 lds\n"
    "v_mfma_f32_16x16x32_bf16 v[168:171], a[4:7], a[40:43], v[168:171]\n"
    "ds_read_b128 a[52:55], v55 offset:21504\n"
    "v_mfma_f32_16x16x32_bf16 v[172:175], a[4:7], a[44:47], v[172:175]\n"
    "s_waitcnt lgkmcnt(6)\n"
    "v_mfma_f32_16x16x32_bf16 v[176:179], a[8:11], a[32:35], v[176:179]\n"
    "ds_read_b64_tr_b16 a[20:21], v47 offset:1024\n"
    "ds_read_b64_tr_b16 a[22:23], v47 offset:15360\n"
    "v_mfma_f32_16x16x32_bf16 v[180:183], a[8:11], a[36:39], v[180:183]\n"
    "buffer_load_dwordx4 v69, s[60:63], s75 offen offset:1024 lds\n"
    "v_mfma_f32_16x16x32_bf16 v[184:187], a[8:11], a[40:43], v[184:187]\n"
    "ds_read_b128 a[56:59], v54 offset:22528\n"
    "v_mfma_f32_16x16x32_bf16 v[188:191], a[8:11], a[44:47], v[188:191]\n"
    "s_waitcnt lgkmcnt(7)\n"
    "v_mfma_f32_16x16x32_bf16 v[192:195], a[12:15], a[32:35], v[192:195]\n"
    "ds_read_b64_tr_b16 a[0:1], v48 offset:1024\n"
    "ds_read_b64_tr_b16 a[2:3], v48 offset:15360\n"
    "v_mfma_f32_16x16x32_bf16 v[196:199], a[12:15], a[36:39], v[196:199]\n"
    "buffer_load_dwordx4 v70, s[60:63], s75 offen offset:2048 lds\n"
    "v_mfma_f32_16x16x32_bf16 v[200:203], a[12:15], a[40:43], v[200:203]\n"
    "s_mov_b32 s76, 430080\n"
    "ds_read_b128 a[60:63], v55 offset:23552\n"
    "v_mfma_f32_16x16x32_bf16 v[204:207], a[12:15], a[44:47], v[204:207]\n"
    "s_waitcnt vmcnt(7) lgkmcnt(7)\n"
    "s_barrier\n"
    "v_mfma_f32_16x16x32_bf16 v[208:211], a[16:19], a[32:35], v[208:211]\n"
    "s_add_u32 m0, s100, 86016\n"
    "ds_read_b64_tr_b16 a[4:5], v46 offset:28672\n"
    "ds_read_b64_tr_b16 a[6:7], v46 offset:43008\n"
    "v_mfma_f32_16x16x32_bf16 v[212:215], a[16:19], a[36:39], v[212:215]\n"
    "buffer_load_dwordx4 v64, s[60:63], s76 offen offset:0 lds\n"
    "v_mfma_f32_16x16x32_bf16 v[216:219], a[16:19], a[40:43], v[216:219]\n"
    "v_mfma_f32_16x16x32_bf16 v[220:223], a[16:19], a[44:47], v[220:223]\n"
    "s_waitcnt lgkmcnt(6)\n"
    "v_mfma_f32_16x16x32_bf16 v[224:227], a[20:23], a[32:35], v[224:227]\n"
    "ds_read_b64_tr_b16 a[8:9], v47 offset:28672\n"
    "ds_read_b64_tr_b16 a[10:11], v47 offset:43008\n"
    "v_mfma_f32_16x16x32_bf16 v[228:231], a[20:23], a[36:39], v[228:231]\n"
    "buffer_load_dwordx4 v65, s[60:63], s76 offen offset:1024 lds\n"
    "v_mfma_f32_16x16x32_bf16 v[232:235], a[20:23], a[40:43], v[232:235]\n"
    "v_mfma_f32_16x16x32_bf16 v[236:239], a[20:23], a[44:47], v[236:239]\n"
    "s_waitcnt lgkmcnt(5)\n"
    "v_mfma_f32_16x16x32_bf16 v[240:243], a[0:3], a[32:35], v[240:243]\n"
    "ds_read_b64_tr_b16 a[12:13], v48 offset:28672\n"
    "ds_read_b64_tr_b16 a[14:15], v48 offset:43008\n"
    "v_mfma_f32_16x16x32_bf16 v[244:247], a[0:3], a[36:39], v[244:247]\n"
    "buffer_load_dwordx4 v66, s[60:63], s76 offen offset:2048 lds\n"
    "v_mfma_f32_16x16x32_bf16 v[248:251], a[0:3], a[40:43], v[248:251]\n"
    "v_mfma_f32_16x16x32_bf16 v[252:255], a[0:3], a[44:47], v[252:255]\n"
    "s_waitcnt lgkmcnt(4)\n"
    "v_mfma_f32_16x16x32_bf16 v[144:147], a[4:7], a[48:51], v[144:147]\n"
    "ds_read_b64_tr_b16 a[16:17], v49 offset:28672\n"
    "ds_read_b64_tr_b16 a[18:19], v49 offset:43008\n"
    "v_mfma_f32_16x16x32_bf16 v[148:151], a[4:7], a[52:55], v[148:151]\n"
    "buffer_load_dwordx4 v67, s[60:63], s76 offen offset:3072 lds\n"
    "v_mfma_f32_16x16x32_bf16 v[152:155], a[4:7], a[56:59], v[152:155]\n"
    "ds_read_b128 a[32:35], v54 offset:24576\n"
    "v_mfma_f32_16x16x32_bf16 v[156:159], a[4:7], a[60:63], v[156:159]\n"
    "s_waitcnt lgkmcnt(5)\n"
    "v_mfma_f32_16x16x32_bf16 v[160:163], a[8:11], a[48:51], v[160:163]\n"
    "s_add_u32 m0, s100, 90112\n"
    "ds_read_b64_tr_b16 a[20:21], v46 offset:29696\n"
    "ds_read_b64_tr_b16 a[22:23], v46 offset:44032\n"
    "v_mfma_f32_16x16x32_bf16 v[164:167], a[8:11], a[52:55], v[164:167]\n"
    "buffer_load_dwordx4 v68, s[60:63], s76 offen offset:0 lds\n"
    "v_mfma_f32_16x16x32_bf16 v[168:171], a[8:11], a[56:59], v[168:171]\n"
    "ds_read_b128 a[36:39], v55 offset:25600\n"
    "v_mfma_f32_16x16x32_bf16 v[172:175], a[8:11], a[60:63], v[172:175]\n"
    "s_waitcnt lgkmcnt(6)\n"
    "v_mfma_f32_16x16x32_bf16 v[176:179], a[12:15], a[48:51], v[176:179]\n"
    "ds_read_b64_tr_b16 a[0:1], v47 offset:29696\n"
    "ds_read_b64_tr_b16 a[2:3], v47 offset:44032\n"
    "v_mfma_f32_16x16x32_bf16 v[180:183], a[12:15], a[52:55], v[180:183]\n"
    "buffer_load_dwordx4 v69, s[60:63], s76 offen offset:1024 lds\n"
    "v_mfma_f32_16x16x32_bf16 v[184:187], a[12:15], a[56:59], v[184:187]\n"
    "ds_read_b128 a[40:43], v54 offset:26624\n"
    "v_mfma_f32_16x16x32_bf16 v[188:191], a[12:15], a[60:63], v[188:191]\n"
    "s_waitcnt lgkmcnt(7)\n"
    "v_mfma_f32_16x16x32_bf16 v[192:195], a[16:19], a[48:51], v[192:195]\n"
    "ds_read_b64_tr_b16 a[4:5], v48 offset:29696\n"
    "ds_read_b64_tr_b16 a[6:7], v48 offset:44032\n"
    "v_mfma_f32_16x16x32_bf16 v[196:199], a[16:19], a[52:55], v[196:199]\n"
    "buffer_load_dwordx4 v70, s[60:63], s76 offen offset:2048 lds\n"
    "v_mfma_f32_16x16x32_bf16 v[200:203], a[16:19], a[56:59], v[200:203]\n"
    "ds_read_b128 a[44:47], v55 offset:27648\n"
    "v_mfma_f32_16x16x32_bf16 v[204:207], a[16:19], a[60:63], v[204:207]\n"
    "s_waitcnt vmcnt(7) lgkmcnt(7)\n"
    "s_barrier\n"
    "v_mfma_f32_16x16x32_bf16 v[208:211], a[20:23], a[48:51], v[208:211]\n"
    "ds_read_b64_tr_b16 a[8:9], v50 offset:0\n"
    "ds_read_b64_tr_b16 a[10:11], v50 offset:14336\n"
    "v_mfma_f32_16x16x32_bf16 v[212:215], a[20:23], a[52:55], v[212:215]\n"
    "v_mfma_f32_16x16x32_bf16 v[216:219], a[20:23], a[56:59], v[216:219]\n"
    "v_mfma_f32_16x16x32_bf16 v[220:223], a[20:23], a[60:63], v[220:223]\n"
    "s_waitcnt lgkmcnt(6)\n"
    "v_mfma_f32_16x16x32_bf16 v[224:227], a[0:3], a[48:51], v[224:227]\n"
    "ds_read_b64_tr_b16 a[12:13], v51 offset:0\n"
    "ds_read_b64_tr_b16 a[14:15], v51 offset:14336\n"
    "v_mfma_f32_16x16x32_bf16 v[228:231], a[0:3], a[52:55], v[228:231]\n"
    "v_mfma_f32_16x16x32_bf16 v[232:235], a[0:3], a[56:59], v[232:235]\n"
    "v_mfma_f32_16x16x32_bf16 v[236:239], a[0:3], a[60:63], v[236:239]\n"
    "s_waitcnt lgkmcnt(5)\n"
    "v_mfma_f32_16x16x32_bf16 v[240:243], a[4:7], a[48:51], v[240:243]\n"
    "ds_read_b64_tr_b16 a[16:17], v52 offset:0\n"
    "ds_read_b64_tr_b16 a[18:19], v52 offset:14336\n"
    "v_mfma_f32_16x16x32_bf16 v[244:247], a[4:7], a[52:55], v[244:247]\n"
    "v_mfma_f32_16x16x32_bf16 v[248:251], a[4:7], a[56:59], v[248:251]\n"
    "v_mfma_f32_16x16x32_bf16 v[252:255], a[4:7], a[60:63], v[252:255]\n"
    "s_waitcnt lgkmcnt(4)\n"
    "v_mfma_f32_16x16x32_bf16 v[144:147], a[8:11], a[32:35], v[144:147]\n"
    "ds_read_b64_tr_b16 a[20:21], v53 offset:0\n"
    "ds_read_b64_tr_b16 a[22:23], v53 offset:14336\n"
    "v_mfma_f32_16x16x32_bf16 v[148:151], a[8:11], a[36:39], v[148:151]\n"
    "v_mfma_f32_16x16x32_bf16 v[152:155], a[8:11], a[40:43], v[152:155]\n"
    "ds_read_b128 a[48:51], v54 offset:28672\n"
    "v_mfma_f32_16x16x32_bf16 v[156:159], a[8:11], a[44:47], v[156:159]\n"
    "s_waitcnt lgkmcnt(5)\n"
    "v_mfma_f32_16x16x32_bf16 v[160:163], a[12:15], a[32:35], v[160:163]\n"
    "ds_read_b64_tr_b16 a[0:1], v50 offset:1024\n"
    "ds_read_b64_tr_b16 a[2:3], v50 offset:15360\n"
    "v_mfma_f32_16x16x32_bf16 v[164:167], a[12:15], a[36:39], v[164:167]\n"
    "v_mfma_f32_16x16x32_bf16 v[168:171], a[12:15], a[40:43], v[168:171]\n"
    "ds_read_b128 a[52:55], v55 offset:29696\n"
    "v_mfma_f32_16x16x32_bf16 v[172:175], a[12:15], a[44:47], v[172:175]\n"
    "s_waitcnt lgkmcnt(6)\n"
    "v_mfma_f32_16x16x32_bf16 v[176:179], a[16:19], a[32:35], v[176:179]\n"
    "ds_read_b64_tr_b16 a[4:5], v51 offset:1024\n"
    "ds_read_b64_tr_b16 a[6:7], v51 offset:15360\n"
    "v_mfma_f32_16x16x32_bf16 v[180:183], a[16:19], a[36:39], v[180:183]\n"
    "v_mfma_f32_16x16x32_bf16 v[184:187], a[16:19], a[40:43], v[184:187]\n"
    "ds_read_b128 a[56:59], v54 offset:30720\n"
    "v_mfma_f32_16x16x32_bf16 v[188:191], a[16:19], a[44:47], v[188:191]\n"
    "s_waitcnt lgkmcnt(7)\n"
    "v_mfma_f32_16x16x32_bf16 v[192:195], a[20:23], a[32:35], v[192:195]\n"
    "ds_read_b64_tr_b16 a[8:9], v52 offset:1024\n"
    "ds_read_b64_tr_b16 a[10:11], v52 offset:15360\n"
    "v_mfma_f32_16x16x32_bf16 v[196:199], a[20:23], a[36:39], v[196:199]\n"
    "v_mfma_f32_16x16x32_bf16 v[200:203], a[20:23], a[40:43], v[200:203]\n"
    "ds_read_b128 a[60:63], v55 offset:31744\n"
    "v_mfma_f32_16x16x32_bf16 v[204:207], a[20:23], a[44:47], v[204:207]\n"
    "s_waitcnt vmcnt(0) lgkmcnt(7)\n"
    "s_barrier\n"
    "v_mfma_f32_16x16x32_bf16 v[208:211], a[0:3], a[32:35], v[208:211]\n"
    "ds_read_b64_tr_b16 a[12:13], v50 offset:28672\n"
    "ds_read_b64_tr_b16 a[14:15], v50 offset:43008\n"
    "v_mfma_f32_16x16x32_bf16 v[212:215], a[0:3], a[36:39], v[212:215]\n"
    "v_mfma_f32_16x16x32_bf16 v[216:219], a[0:3], a[40:43], v[216:219]\n"
    "v_mfma_f32_16x16x32_bf16 v[220:223], a[0:3], a[44:47], v[220:223]\n"
    "s_waitcnt lgkmcnt(6)\n"
    "v_mfma_f32_16x16x32_bf16 v[224:227], a[4:7], a[32:35], v[224:227]\n"
    "ds_read_b64_tr_b16 a[16:17], v51 offset:28672\n"
    "ds_read_b64_tr_b16 a[18:19], v51 offset:43008\n"
    "v_mfma_f32_16x16x32_bf16 v[228:231], a[4:7], a[36:39], v[228:231]\n"
    "v_mfma_f32_16x16x32_bf16 v[232:235], a[4:7], a[40:43], v[232:235]\n"
    "v_mfma_f32_16x16x32_bf16 v[236:239], a[4:7], a[44:47], v[236:239]\n"
    "s_waitcnt lgkmcnt(5)\n"
    "v_mfma_f32_16x16x32_bf16 v[240:243], a[8:11], a[32:35], v[240:243]\n"
    "ds_read_b64_tr_b16 a[20:21], v52 offset:28672\n"
    "ds_read_b64_tr_b16 a[22:23], v52 offset:43008\n"
    "v_mfma_f32_16x16x32_bf16 v[244:247], a[8:11], a[36:39], v[244:247]\n"
    "v_mfma_f32_16x16x32_bf16 v[248:251], a[8:11], a[40:43], v[248:251]\n"
    "v_mfma_f32_16x16x32_bf16 v[252:255], a[8:11], a[44:47], v[252:255]\n"
    "s_waitcnt lgkmcnt(4)\n"
    "v_mfma_f32_16x16x32_bf16 v[144:147], a[12:15], a[48:51], v[144:147]\n"
    "ds_read_b64_tr_b16 a[0:1], v53 offset:28672\n"
    "ds_read_b64_tr_b16 a[2:3], v53 offset:43008\n"
    "v_mfma_f32_16x16x32_bf16 v[148:151], a[12:15], a[52:55], v[148:151]\n"
    "v_mfma_f32_16x16x32_bf16 v[152:155], a[12:15], a[56:59], v[152:155]\n"
    "v_mfma_f32_16x16x32_bf16 v[156:159], a[12:15], a[60:63], v[156:159]\n"
    "s_waitcnt lgkmcnt(4)\n"
    "v_mfma_f32_16x16x32_bf16 v[160:163], a[16:19], a[48:51], v[160:163]\n"
    "ds_read_b64_tr_b16 a[4:5], v50 offset:29696\n"
    "ds_read_b64_tr_b16 a[6:7], v50 offset:44032\n"
    "v_mfma_f32_16x16x32_bf16 v[164:167], a[16:19], a[52:55], v[164:167]\n"
    "v_mfma_f32_16x16x32_bf16 v[168:171], a[16:19], a[56:59], v[168:171]\n"
    "v_mfma_f32_16x16x32_bf16 v[172:175], a[16:19], a[60:63], v[172:175]\n"
    "s_waitcnt lgkmcnt(4)\n"
    "v_mfma_f32_16x16x32_bf16 v[176:179], a[20:23], a[48:51], v[176:179]\n"
    "ds_read_b64_tr_b16 a[8:9], v51 offset:29696\n"
    "ds_read_b64_tr_b16 a[10:11], v51 offset:44032\n"
    "v_mfma_f32_16x16x32_bf16 v[180:183], a[20:23], a[52:55], v[180:183]\n"
    "v_cvt_pk_bf16_f32 v144, v144, v145\n"
    "v_cvt_pk_bf16_f32 v145, v146, v147\n"
    "v_mfma_f32_16x16x32_bf16 v[184:187], a[20:23], a[56:59], v[184:187]\n"
    "v_cvt_pk_bf16_f32 v146, v148, v149\n"
    "v_cvt_pk_bf16_f32 v147, v150, v151\n"
    "v_mfma_f32_16x16x32_bf16 v[188:191], a[20:23], a[60:63], v[188:191]\n"
    "s_waitcnt lgkmcnt(4)\n"
    "s_mov_b32 s90, 0\n"
    "buffer_store_dwordx4 v[144:147], v45, s[64:67], s90 offen offset:0 sc1\n"
    "v_cvt_pk_bf16_f32 v152, v152, v153\n"
    "v_cvt_pk_bf16_f32 v153, v154, v155\n"
    "v_mfma_f32_16x16x32_bf16 v[192:195], a[0:3], a[48:51], v[192:195]\n"
    "ds_read_b64_tr_b16 a[12:13], v52 offset:29696\n"
    "ds_read_b64_tr_b16 a[14:15], v52 offset:44032\n"
    "v_cvt_pk_bf16_f32 v154, v156, v157\n"
    "v_cvt_pk_bf16_f32 v155, v158, v159\n"
    "v_mfma_f32_16x16x32_bf16 v[196:199], a[0:3], a[52:55], v[196:199]\n"
    "s_mov_b32 s101, 28672\n"
    "buffer_store_dwordx4 v[152:155], v45, s[64:67], s101 offen offset:0 sc1\n"
    "v_cvt_pk_bf16_f32 v160, v160, v161\n"
    "v_cvt_pk_bf16_f32 v161, v162, v163\n"
    "v_mfma_f32_16x16x32_bf16 v[200:203], a[0:3], a[56:59], v[200:203]\n"
    "v_cvt_pk_bf16_f32 v162, v164, v165\n"
    "v_cvt_pk_bf16_f32 v163, v166, v167\n"
    "v_mfma_f32_16x16x32_bf16 v[204:207], a[0:3], a[60:63], v[204:207]\n"
    "s_waitcnt lgkmcnt(4)\n"
    "s_mov_b32 s90, 0\n"
    "buffer_store_dwordx4 v[160:163], v45, s[64:67], s90 offen offset:1024 sc1\n"
    "v_cvt_pk_bf16_f32 v168, v168, v169\n"
    "v_cvt_pk_bf16_f32 v169, v170, v171\n"
    "v_mfma_f32_16x16x32_bf16 v[208:211], a[4:7], a[48:51], v[208:211]\n"
    "v_cvt_pk_bf16_f32 v170, v172, v173\n"
    "v_cvt_pk_bf16_f32 v171, v174, v175\n"
    "v_mfma_f32_16x16x32_bf16 v[212:215], a[4:7], a[52:55], v[212:215]\n"
    "s_mov_b32 s101, 28672\n"
    "buffer_store_dwordx4 v[168:171], v45, s[64:67], s101 offen offset:1024 sc1\n"
    "v_cvt_pk_bf16_f32 v176, v176, v177\n"
    "v_cvt_pk_bf16_f32 v177, v178, v179\n"
    "v_mfma_f32_16x16x32_bf16 v[216:219], a[4:7], a[56:59], v[216:219]\n"
    "v_cvt_pk_bf16_f32 v178, v180, v181\n"
    "v_cvt_pk_bf16_f32 v179, v182, v183\n"
    "v_mfma_f32_16x16x32_bf16 v[220:223], a[4:7], a[60:63], v[220:223]\n"
    "s_waitcnt lgkmcnt(2)\n"
    "s_mov_b32 s90, 0\n"
    "buffer_store_dwordx4 v[176:179], v45, s[64:67], s90 offen offset:2048 sc1\n"
    "v_cvt_pk_bf16_f32 v184, v184, v185\n"
    "v_cvt_pk_bf16_f32 v185, v186, v187\n"
    "v_mfma_f32_16x16x32_bf16 v[224:227], a[8:11], a[48:51], v[224:227]\n"
    "v_cvt_pk_bf16_f32 v186, v188, v189\n"
    "v_cvt_pk_bf16_f32 v187, v190, v191\n"
    "v_mfma_f32_16x16x32_bf16 v[228:231], a[8:11], a[52:55], v[228:231]\n"
    "s_mov_b32 s101, 28672\n"
    "buffer_store_dwordx4 v[184:187], v45, s[64:67], s101 offen offset:2048 sc1\n"
    "v_cvt_pk_bf16_f32 v192, v192, v193\n"
    "v_cvt_pk_bf16_f32 v193, v194, v195\n"
    "v_mfma_f32_16x16x32_bf16 v[232:235], a[8:11], a[56:59], v[232:235]\n"
    "v_cvt_pk_bf16_f32 v194, v196, v197\n"
    "v_cvt_pk_bf16_f32 v195, v198, v199\n"
    "v_mfma_f32_16x16x32_bf16 v[236:239], a[8:11], a[60:63], v[236:239]\n"
    "s_waitcnt lgkmcnt(0)\n"
    "s_mov_b32 s90, 0\n"
    "buffer_store_dwordx4 v[192:195], v45, s[64:67], s90 offen offset:3072 sc1\n"
    "v_cvt_pk_bf16_f32 v200, v200, v201\n"
    "v_cvt_pk_bf16_f32 v201, v202, v203\n"
    "v_mfma_f32_16x16x32_bf16 v[240:243], a[12:15], a[48:51], v[240:243]\n"
    "v_cvt_pk_bf16_f32 v202, v204, v205\n"
    "v_cvt_pk_bf16_f32 v203, v206, v207\n"
    "v_mfma_f32_16x16x32_bf16 v[244:247], a[12:15], a[52:55], v[244:247]\n"
    "s_mov_b32 s101, 28672\n"
    "buffer_store_dwordx4 v[200:203], v45, s[64:67], s101 offen offset:3072 sc1\n"
    "v_cvt_pk_bf16_f32 v208, v208, v209\n"
    "v_cvt_pk_bf16_f32 v209, v210, v211\n"
    "v_mfma_f32_16x16x32_bf16 v[248:251], a[12:15], a[56:59], v[248:251]\n"
    "v_cvt_pk_bf16_f32 v210, v212, v213\n"
    "v_cvt_pk_bf16_f32 v211, v214, v215\n"
    "v_mfma_f32_16x16x32_bf16 v[252:255], a[12:15], a[60:63], v[252:255]\n"
    "s_mov_b32 s90, 4096\n"
    "buffer_store_dwordx4 v[208:211], v45, s[64:67], s90 offen offset:0 sc1\n"
    "s_nop 7\n"
    "v_cvt_pk_bf16_f32 v216, v216, v217\n"
    "v_cvt_pk_bf16_f32 v217, v218, v219\n"
    "v_cvt_pk_bf16_f32 v218, v220, v221\n"
    "v_cvt_pk_bf16_f32 v219, v222, v223\n"
    "s_mov_b32 s101, 32768\n"
    "s_nop 0\n"
    "buffer_store_dwordx4 v[216:219], v45, s[64:67], s101 offen offset:0 sc1\n"
    "v_cvt_pk_bf16_f32 v224, v224, v225\n"
    "v_cvt_pk_bf16_f32 v225, v226, v227\n"
    "v_cvt_pk_bf16_f32 v226, v228, v229\n"
    "v_cvt_pk_bf16_f32 v227, v230, v231\n"
    "s_mov_b32 s90, 4096\n"
    "s_nop 0\n"
    "buffer_store_dwordx4 v[224:227], v45, s[64:67], s90 offen offset:1024 sc1\n"
    "v_cvt_pk_bf16_f32 v232, v232, v233\n"
    "v_cvt_pk_bf16_f32 v233, v234, v235\n"
    "v_cvt_pk_bf16_f32 v234, v236, v237\n"
    "v_cvt_pk_bf16_f32 v235, v238, v239\n"
    "s_mov_b32 s101, 32768\n"
    "s_nop 0\n"
    "buffer_store_dwordx4 v[232:235], v45, s[64:67], s101 offen offset:1024 sc1\n"
    "v_cvt_pk_bf16_f32 v240, v240, v241\n"
    "v_cvt_pk_bf16_f32 v241, v242, v243\n"
    "v_cvt_pk_bf16_f32 v242, v244, v245\n"
    "v_cvt_pk_bf16_f32 v243, v246, v247\n"
    "s_mov_b32 s90, 4096\n"
    "s_nop 0\n"
    "buffer_store_dwordx4 v[240:243], v45, s[64:67], s90 offen offset:2048 sc1\n"
    "v_cvt_pk_bf16_f32 v248, v248, v249\n"
    "v_cvt_pk_bf16_f32 v249, v250, v251\n"
    "v_cvt_pk_bf16_f32 v250, v252, v253\n"
    "v_cvt_pk_bf16_f32 v251, v254, v255\n"
    "s_mov_b32 s101, 32768\n"
    "s_nop 0\n"
    "buffer_store_dwordx4 v[248:251], v45, s[64:67], s101 offen offset:2048 sc1\n"
    "s_waitcnt vmcnt(0)\n"
    "s_barrier\n"
    "s_cmp_lg_u32 s69, 0\n"
    "s_cbranch_scc1 .Lnfs\n"
    "v_mbcnt_lo_u32_b32 v43, -1, 0\n"
    "v_mbcnt_hi_u32_b32 v43, -1, v43\n"
    "v_and_b32 v43, 7, v43\n"
    "v_lshlrev_b32 v43, 2, v43\n"
    "s_lshl_b32 s86, s71, 8\n"
    "v_add_u32 v43, s86, v43\n"
    "v_add_u32 v44, 0x1, v79\n"
    "s_lshl_b64 exec, 1, s70\n"
    "global_store_dword v43, v44, s[36:37] offset:0 sc1\n"
    "s_mov_b64 exec, -1\n"
    ".Lnfs:\n"
    "s_mov_b32 s86, 0x00003f80\n"
    "s_mov_b32 s87, 0x3f800000\n"
    "s_add_u32 s40, s40, 14680064\n"
    "s_addc_u32 s41, s41, 0\n"
    "global_load_dword v28, v76, s[36:37] sc1\n"
    "v_add_u32 v42, 1, v79\n"
    "v_accvgpr_read_b32 v16, a200\n"
    "v_accvgpr_read_b32 v17, a201\n"
    "v_lshlrev_b32 v96, 16, v16\n"
    "v_and_b32 v97, 0xffff0000, v16\n"
    "v_lshlrev_b32 v98, 16, v17\n"
    "v_and_b32 v99, 0xffff0000, v17\n"
    "v_lshlrev_b32 v100, 16, v16\n"
    "v_and_b32 v101, 0xffff0000, v16\n"
    "v_lshlrev_b32 v102, 16, v17\n"
    "v_and_b32 v103, 0xffff0000, v17\n"
    "v_accvgpr_read_b32 v20, a128\n"
    "v_accvgpr_read_b32 v21, a129\n"
    "v_accvgpr_read_b32 v22, a130\n"
    "v_accvgpr_read_b32 v23, a131\n"
    "v_dot2c_f32_bf16 v96, s86, v20\n"
    "v_dot2c_f32_bf16 v97, s87, v20\n"
    "v_dot2c_f32_bf16 v98, s86, v21\n"
    "v_dot2c_f32_bf16 v99, s87, v21\n"
    "v_dot2c_f32_bf16 v100, s86, v22\n"
    "v_dot2c_f32_bf16 v101, s87, v22\n"
    "v_dot2c_f32_bf16 v102, s86, v23\n"
    "v_dot2c_f32_bf16 v103, s87, v23\n"
    "v_accvgpr_read_b32 v24, a132\n"
    "v_accvgpr_read_b32 v25, a133\n"
    "v_accvgpr_read_b32 v26, a134\n"
    "v_accvgpr_read_b32 v27, a135\n"
    "v_dot2c_f32_bf16 v96, s86, v24\n"
    "v_dot2c_f32_bf16 v97, s87, v24\n"
    "v_dot2c_f32_bf16 v98, s86, v25\n"
    "v_dot2c_f32_bf16 v99, s87, v25\n"
    "v_dot2c_f32_bf16 v100, s86, v26\n"
    "v_dot2c_f32_bf16 v101, s87, v26\n"
    "v_dot2c_f32_bf16 v102, s86, v27\n"
    "v_dot2c_f32_bf16 v103, s87, v27\n"
    "v_accvgpr_read_b32 v20, a136\n"
    "v_accvgpr_read_b32 v21, a137\n"
    "v_accvgpr_read_b32 v22, a138\n"
    "v_accvgpr_read_b32 v23, a139\n"
    "v_dot2c_f32_bf16 v96, s86, v20\n"
    "v_dot2c_f32_bf16 v97, s87, v20\n"
    "v_dot2c_f32_bf16 v98, s86, v21\n"
    "v_dot2c_f32_bf16 v99, s87, v21\n"
    "v_dot2c_f32_bf16 v100, s86, v22\n"
    "v_dot2c_f32_bf16 v101, s87, v22\n"
    "v_dot2c_f32_bf16 v102, s86, v23\n"
    "v_dot2c_f32_bf16 v103, s87, v23\n"
    "v_accvgpr_read_b32 v24, a140\n"
    "v_accvgpr_read_b32 v25, a141\n"
    "v_accvgpr_read_b32 v26, a142\n"
    "v_accvgpr_read_b32 v27, a143\n"
    "v_dot2c_f32_bf16 v96, s86, v24\n"
    "v_dot2c_f32_bf16 v97, s87, v24\n"
    "v_dot2c_f32_bf16 v98, s86, v25\n"
    "v_dot2c_f32_bf16 v99, s87, v25\n"
    "v_dot2c_f32_bf16 v100, s86, v26\n"
    "v_dot2c_f32_bf16 v101, s87, v26\n"
    "v_dot2c_f32_bf16 v102, s86, v27\n"
    "v_dot2c_f32_bf16 v103, s87, v27\n"
    "v_accvgpr_read_b32 v20, a144\n"
    "v_accvgpr_read_b32 v21, a145\n"
    "v_accvgpr_read_b32 v22, a146\n"
    "v_accvgpr_read_b32 v23, a147\n"
    "v_dot2c_f32_bf16 v96, s86, v20\n"
    "v_dot2c_f32_bf16 v97, s87, v20\n"
    "v_dot2c_f32_bf16 v98, s86, v21\n"
    "v_dot2c_f32_bf16 v99, s87, v21\n"
    "v_dot2c_f32_bf16 v100, s86, v22\n"
    "v_dot2c_f32_bf16 v101, s87, v22\n"
    "v_dot2c_f32_bf16 v102, s86, v23\n"
    "v_dot2c_f32_bf16 v103, s87, v23\n"
    "v_accvgpr_read_b32 v24, a148\n"
    "v_accvgpr_read_b32 v25, a149\n"
    "v_accvgpr_read_b32 v26, a150\n"
    "v_accvgpr_read_b32 v27, a151\n"
    "v_dot2c_f32_bf16 v96, s86, v24\n"
    "v_dot2c_f32_bf16 v97, s87, v24\n"
    "v_dot2c_f32_bf16 v98, s86, v25\n"
    "v_dot2c_f32_bf16 v99, s87, v25\n"
    "v_dot2c_f32_bf16 v100, s86, v26\n"
    "v_dot2c_f32_bf16 v101, s87, v26\n"
    "v_dot2c_f32_bf16 v102, s86, v27\n"
    "v_dot2c_f32_bf16 v103, s87, v27\n"
    "v_accvgpr_read_b32 v20, a152\n"
    "v_accvgpr_read_b32 v21, a153\n"
    "v_accvgpr_read_b32 v22, a154\n"
    "v_accvgpr_read_b32 v23, a155\n"
    "v_dot2c_f32_bf16 v96, s86, v20\n"
    "v_dot2c_f32_bf16 v97, s87, v20\n"
    "v_dot2c_f32_bf16 v98, s86, v21\n"
    "v_dot2c_f32_bf16 v99, s87, v21\n"
    "v_dot2c_f32_bf16 v100, s86, v22\n"
    "v_dot2c_f32_bf16 v101, s87, v22\n"
    "v_dot2c_f32_bf16 v102, s86, v23\n"
    "v_dot2c_f32_bf16 v103, s87, v23\n"
    "v_accvgpr_read_b32 v24, a156\n"
    "v_accvgpr_read_b32 v25, a157\n"
    "v_accvgpr_read_b32 v26, a158\n"
    "v_accvgpr_read_b32 v27, a159\n"
    "v_dot2c_f32_bf16 v96, s86, v24\n"
    "v_dot2c_f32_bf16 v97, s87, v24\n"
    "v_dot2c_f32_bf16 v98, s86, v25\n"
    "v_dot2c_f32_bf16 v99, s87, v25\n"
    "v_dot2c_f32_bf16 v100, s86, v26\n"
    "v_dot2c_f32_bf16 v101, s87, v26\n"
    "v_dot2c_f32_bf16 v102, s86, v27\n"
    "v_dot2c_f32_bf16 v103, s87, v27\n"
    "v_accvgpr_read_b32 v18, a192\n"
    "v_lshlrev_b32 v112, 16, v18\n"
    "v_and_b32 v113, 0xffff0000, v18\n"
    "v_accvgpr_read_b32 v18, a193\n"
    "v_lshlrev_b32 v114, 16, v18\n"
    "v_and_b32 v115, 0xffff0000, v18\n"
    "v_accvgpr_read_b32 v18, a194\n"
    "v_lshlrev_b32 v116, 16, v18\n"
    "v_and_b32 v117, 0xffff0000, v18\n"
    "v_accvgpr_read_b32 v18, a195\n"
    "v_lshlrev_b32 v118, 16, v18\n"
    "v_and_b32 v119, 0xffff0000, v18\n"
    "global_load_dword v29, v76, s[36:37] sc1\n"
    "v_accvgpr_read_b32 v16, a202\n"
    "v_accvgpr_read_b32 v17, a203\n"
    "v_lshlrev_b32 v104, 16, v16\n"
    "v_and_b32 v105, 0xffff0000, v16\n"
    "v_lshlrev_b32 v106, 16, v17\n"
    "v_and_b32 v107, 0xffff0000, v17\n"
    "v_lshlrev_b32 v108, 16, v16\n"
    "v_and_b32 v109, 0xffff0000, v16\n"
    "v_lshlrev_b32 v110, 16, v17\n"
    "v_and_b32 v111, 0xffff0000, v17\n"
    "v_accvgpr_read_b32 v20, a160\n"
    "v_accvgpr_read_b32 v21, a161\n"
    "v_accvgpr_read_b32 v22, a162\n"
    "v_accvgpr_read_b32 v23, a163\n"
    "v_dot2c_f32_bf16 v104, s86, v20\n"
    "v_dot2c_f32_bf16 v105, s87, v20\n"
    "v_dot2c_f32_bf16 v106, s86, v21\n"
    "v_dot2c_f32_bf16 v107, s87, v21\n"
    "v_dot2c_f32_bf16 v108, s86, v22\n"
    "v_dot2c_f32_bf16 v109, s87, v22\n"
    "v_dot2c_f32_bf16 v110, s86, v23\n"
    "v_dot2c_f32_bf16 v111, s87, v23\n"
    "v_accvgpr_read_b32 v24, a164\n"
    "v_accvgpr_read_b32 v25, a165\n"
    "v_accvgpr_read_b32 v26, a166\n"
    "v_accvgpr_read_b32 v27, a167\n"
    "v_dot2c_f32_bf16 v104, s86, v24\n"
    "v_dot2c_f32_bf16 v105, s87, v24\n"
    "v_dot2c_f32_bf16 v106, s86, v25\n"
    "v_dot2c_f32_bf16 v107, s87, v25\n"
    "v_dot2c_f32_bf16 v108, s86, v26\n"
    "v_dot2c_f32_bf16 v109, s87, v26\n"
    "v_dot2c_f32_bf16 v110, s86, v27\n"
    "v_dot2c_f32_bf16 v111, s87, v27\n"
    "v_accvgpr_read_b32 v20, a168\n"
    "v_accvgpr_read_b32 v21, a169\n"
    "v_accvgpr_read_b32 v22, a170\n"
    "v_accvgpr_read_b32 v23, a171\n"
    "v_dot2c_f32_bf16 v104, s86, v20\n"
    "v_dot2c_f32_bf16 v105, s87, v20\n"
    "v_dot2c_f32_bf16 v106, s86, v21\n"
    "v_dot2c_f32_bf16 v107, s87, v21\n"
    "v_dot2c_f32_bf16 v108, s86, v22\n"
    "v_dot2c_f32_bf16 v109, s87, v22\n"
    "v_dot2c_f32_bf16 v110, s86, v23\n"
    "v_dot2c_f32_bf16 v111, s87, v23\n"
    "v_accvgpr_read_b32 v24, a172\n"
    "v_accvgpr_read_b32 v25, a173\n"
    "v_accvgpr_read_b32 v26, a174\n"
    "v_accvgpr_read_b32 v27, a175\n"
    "v_dot2c_f32_bf16 v104, s86, v24\n"
    "v_dot2c_f32_bf16 v105, s87, v24\n"
    "v_dot2c_f32_bf16 v106, s86, v25\n"
    "v_dot2c_f32_bf16 v107, s87, v25\n"
    "v_dot2c_f32_bf16 v108, s86, v26\n"
    "v_dot2c_f32_bf16 v109, s87, v26\n"
    "v_dot2c_f32_bf16 v110, s86, v27\n"
    "v_dot2c_f32_bf16 v111, s87, v27\n"
    "v_accvgpr_read_b32 v20, a176\n"
    "v_accvgpr_read_b32 v21, a177\n"
    "v_accvgpr_read_b32 v22, a178\n"
    "v_accvgpr_read_b32 v23, a179\n"
    "v_dot2c_f32_bf16 v104, s86, v20\n"
    "v_dot2c_f32_bf16 v105, s87, v20\n"
    "v_dot2c_f32_bf16 v106, s86, v21\n"
    "v_dot2c_f32_bf16 v107, s87, v21\n"
    "v_dot2c_f32_bf16 v108, s86, v22\n"
    "v_dot2c_f32_bf16 v109, s87, v22\n"
    "v_dot2c_f32_bf16 v110, s86, v23\n"
    "v_dot2c_f32_bf16 v111, s87, v23\n"
    "v_accvgpr_read_b32 v24, a180\n"
    "v_accvgpr_read_b32 v25, a181\n"
    "v_accvgpr_read_b32 v26, a182\n"
    "v_accvgpr_read_b32 v27, a183\n"
    "v_dot2c_f32_bf16 v104, s86, v24\n"
    "v_dot2c_f32_bf16 v105, s87, v24\n"
    "v_dot2c_f32_bf16 v106, s86, v25\n"
    "v_dot2c_f32_bf16 v107, s87, v25\n"
    "v_dot2c_f32_bf16 v108, s86, v26\n"
    "v_dot2c_f32_bf16 v109, s87, v26\n"
    "v_dot2c_f32_bf16 v110, s86, v27\n"
    "v_dot2c_f32_bf16 v111, s87, v27\n"
    "v_accvgpr_read_b32 v20, a184\n"
    "v_accvgpr_read_b32 v21, a185\n"
    "v_accvgpr_read_b32 v22, a186\n"
    "v_accvgpr_read_b32 v23, a187\n"
    "v_dot2c_f32_bf16 v104, s86, v20\n"
    "v_dot2c_f32_bf16 v105, s87, v20\n"
    "v_dot2c_f32_bf16 v106, s86, v21\n"
    "v_dot2c_f32_bf16 v107, s87, v21\n"
    "v_dot2c_f32_bf16 v108, s86, v22\n"
    "v_dot2c_f32_bf16 v109, s87, v22\n"
    "v_dot2c_f32_bf16 v110, s86, v23\n"
    "v_dot2c_f32_bf16 v111, s87, v23\n"
    "v_accvgpr_read_b32 v24, a188\n"
    "v_accvgpr_read_b32 v25, a189\n"
    "v_accvgpr_read_b32 v26, a190\n"
    "v_accvgpr_read_b32 v27, a191\n"
    "v_dot2c_f32_bf16 v104, s86, v24\n"
    "v_dot2c_f32_bf16 v105, s87, v24\n"
    "v_dot2c_f32_bf16 v106, s86, v25\n"
    "v_dot2c_f32_bf16 v107, s87, v25\n"
    "v_dot2c_f32_bf16 v108, s86, v26\n"
    "v_dot2c_f32_bf16 v109, s87, v26\n"
    "v_dot2c_f32_bf16 v110, s86, v27\n"
    "v_dot2c_f32_bf16 v111, s87, v27\n"
    "v_accvgpr_read_b32 v18, a196\n"
    "v_lshlrev_b32 v120, 16, v18\n"
    "v_and_b32 v121, 0xffff0000, v18\n"
    "v_accvgpr_read_b32 v18, a197\n"
    "v_lshlrev_b32 v122, 16, v18\n"
    "v_and_b32 v123, 0xffff0000, v18\n"
    "v_accvgpr_read_b32 v18, a198\n"
    "v_lshlrev_b32 v124, 16, v18\n"
    "v_and_b32 v125, 0xffff0000, v18\n"
    "v_accvgpr_read_b32 v18, a199\n"
    "v_lshlrev_b32 v126, 16, v18\n"
    "v_and_b32 v127, 0xffff0000, v18\n"
    "s_nop 2\n"
    "v_mul_f32 v80, 0xbfb8aa3b, v96\n"
    "v_mul_f32 v81, 0xbfb8aa3b, v97\n"
    "v_mul_f32 v82, 0xbfb8aa3b, v98\n"
    "v_mul_f32 v83, 0xbfb8aa3b, v99\n"
    "v_mul_f32 v84, 0xbfb8aa3b, v100\n"
    "v_mul_f32 v85, 0xbfb8aa3b, v101\n"
    "v_mul_f32 v86, 0xbfb8aa3b, v102\n"
    "v_mul_f32 v87, 0xbfb8aa3b, v103\n"
    "v_exp_f32 v80, v80\n"
    "v_exp_f32 v81, v81\n"
    "v_exp_f32 v82, v82\n"
    "v_exp_f32 v83, v83\n"
    "v_exp_f32 v84, v84\n"
    "v_exp_f32 v85, v85\n"
    "v_exp_f32 v86, v86\n"
    "v_exp_f32 v87, v87\n"
    "v_add_f32 v80, 1.0, v80\n"
    "v_add_f32 v81, 1.0, v81\n"
    "v_add_f32 v82, 1.0, v82\n"
    "v_add_f32 v83, 1.0, v83\n"
    "v_add_f32 v84, 1.0, v84\n"
    "v_add_f32 v85, 1.0, v85\n"
    "v_add_f32 v86, 1.0, v86\n"
    "v_add_f32 v87, 1.0, v87\n"
    "v_rcp_f32 v80, v80\n"
    "v_rcp_f32 v81, v81\n"
    "v_rcp_f32 v82, v82\n"
    "v_rcp_f32 v83, v83\n"
    "v_rcp_f32 v84, v84\n"
    "v_rcp_f32 v85, v85\n"
    "v_rcp_f32 v86, v86\n"
    "v_rcp_f32 v87, v87\n"
    "v_mul_f32 v96, v96, v80\n"
    "v_mul_f32 v97, v97, v81\n"
    "v_mul_f32 v98, v98, v82\n"
    "v_mul_f32 v99, v99, v83\n"
    "v_mul_f32 v100, v100, v84\n"
    "v_mul_f32 v101, v101, v85\n"
    "v_mul_f32 v102, v102, v86\n"
    "v_mul_f32 v103, v103, v87\n"
    "v_mul_f32 v80, 0xbfb8aa3b, v104\n"
    "v_mul_f32 v81, 0xbfb8aa3b, v105\n"
    "v_mul_f32 v82, 0xbfb8aa3b, v106\n"
    "v_mul_f32 v83, 0xbfb8aa3b, v107\n"
    "v_mul_f32 v84, 0xbfb8aa3b, v108\n"
    "v_mul_f32 v85, 0xbfb8aa3b, v109\n"
    "v_mul_f32 v86, 0xbfb8aa3b, v110\n"
    "v_mul_f32 v87, 0xbfb8aa3b, v111\n"
    "v_exp_f32 v80, v80\n"
    "v_exp_f32 v81, v81\n"
    "v_exp_f32 v82, v82\n"
    "v_exp_f32 v83, v83\n"
    "v_exp_f32 v84, v84\n"
    "v_exp_f32 v85, v85\n"
    "v_exp_f32 v86, v86\n"
    "v_exp_f32 v87, v87\n"
    "v_add_f32 v80, 1.0, v80\n"
    "v_add_f32 v81, 1.0, v81\n"
    "v_add_f32 v82, 1.0, v82\n"
    "v_add_f32 v83, 1.0, v83\n"
    "v_add_f32 v84, 1.0, v84\n"
    "v_add_f32 v85, 1.0, v85\n"
    "v_add_f32 v86, 1.0, v86\n"
    "v_add_f32 v87, 1.0, v87\n"
    "v_rcp_f32 v80, v80\n"
    "v_rcp_f32 v81, v81\n"
    "v_rcp_f32 v82, v82\n"
    "v_rcp_f32 v83, v83\n"
    "v_rcp_f32 v84, v84\n"
    "v_rcp_f32 v85, v85\n"
    "v_rcp_f32 v86, v86\n"
    "v_rcp_f32 v87, v87\n"
    "v_mul_f32 v104, v104, v80\n"
    "v_mul_f32 v105, v105, v81\n"
    "v_mul_f32 v106, v106, v82\n"
    "v_mul_f32 v107, v107, v83\n"
    "v_mul_f32 v108, v108, v84\n"
    "v_mul_f32 v109, v109, v85\n"
    "v_mul_f32 v110, v110, v86\n"
    "v_mul_f32 v111, v111, v87\n"
    "global_load_dword v30, v76, s[36:37] sc1\n"
    "v_mov_b32 v0, 0\n"
    "v_mov_b32 v1, 0\n"
    "v_mov_b32 v2, 0\n"
    "v_mov_b32 v3, 0\n"
    "v_mov_b32 v4, 0\n"
    "v_mov_b32 v5, 0\n"
    "v_mov_b32 v6, 0\n"
    "v_mov_b32 v7, 0\n"
    "v_mov_b32 v8, 0\n"
    "v_mov_b32 v9, 0\n"
    "v_mov_b32 v10, 0\n"
    "v_mov_b32 v11, 0\n"
    "v_mov_b32 v12, 0\n"
    "v_mov_b32 v13, 0\n"
    "v_mov_b32 v14, 0\n"
    "v_mov_b32 v15, 0\n"
    "s_mov_b32 s85, 4194304\n"
    ".Lpp:\n"
    "s_waitcnt vmcnt(2)\n"
    "v_cmp_eq_u32 vcc, v28, v42\n"
    "s_cmp_eq_u64 vcc, -1\n"
    "s_cbranch_scc1 .Lgo\n"
    "global_load_dword v28, v76, s[36:37] sc1\n"
    "s_waitcnt vmcnt(2)\n"
    "v_cmp_eq_u32 vcc, v29, v42\n"
    "s_cmp_eq_u64 vcc, -1\n"
    "s_cbranch_scc1 .Lgo\n"
    "global_load_dword v29, v76, s[36:37] sc1\n"
    "s_waitcnt vmcnt(2)\n"
    "v_cmp_eq_u32 vcc, v30, v42\n"
    "s_cmp_eq_u64 vcc, -1\n"
    "s_cbranch_scc1 .Lgo\n"
    "global_load_dword v30, v76, s[36:37] sc1\n"
    "s_sub_u32 s85, s85, 1\n"
    "s_cbranch_scc0 .Lpp\n"
    ".Lgo:\n"
    "buffer_load_dwordx4 v[144:147], v34, s[40:43], 0 offen sc1\n"
    "buffer_load_dwordx4 v[176:179], v35, s[40:43], 0 offen sc1\n"
    "buffer_load_dwordx4 v[148:151], v34, s[40:43], s52 offen sc1\n"
    "buffer_load_dwordx4 v[180:183], v35, s[40:43], s52 offen sc1\n"
    "buffer_load_dwordx4 v[152:155], v34, s[40:43], s53 offen sc1\n"
    "buffer_load_dwordx4 v[184:187], v35, s[40:43], s53 offen sc1\n"
    "buffer_load_dwordx4 v[156:159], v34, s[40:43], s54 offen sc1\n"
    "buffer_load_dwordx4 v[188:191], v35, s[40:43], s54 offen sc1\n"
    "buffer_load_dwordx4 v[160:163], v34, s[40:43], s55 offen sc1\n"
    "buffer_load_dwordx4 v[192:195], v35, s[40:43], s55 offen sc1\n"
    "buffer_load_dwordx4 v[164:167], v34, s[40:43], s77 offen sc1\n"
    "buffer_load_dwordx4 v[196:199], v35, s[40:43], s77 offen sc1\n"
    "buffer_load_dwordx4 v[168:171], v34, s[40:43], s84 offen sc1\n"
    "buffer_load_dwordx4 v[200:203], v35, s[40:43], s84 offen sc1\n"
    "buffer_load_dwordx4 v[172:175], v34, s[40:43], s74 offen sc1\n"
    "buffer_load_dwordx4 v[204:207], v35, s[40:43], s74 offen sc1\n"
    "s_waitcnt vmcnt(15)\n"
    "v_dot2c_f32_bf16 v0, s86, v144\n"
    "v_dot2c_f32_bf16 v1, s87, v144\n"
    "v_dot2c_f32_bf16 v2, s86, v145\n"
    "v_dot2c_f32_bf16 v3, s87, v145\n"
    "v_dot2c_f32_bf16 v4, s86, v146\n"
    "v_dot2c_f32_bf16 v5, s87, v146\n"
    "v_dot2c_f32_bf16 v6, s86, v147\n"
    "v_dot2c_f32_bf16 v7, s87, v147\n"
    "s_waitcnt vmcnt(14)\n"
    "v_dot2c_f32_bf16 v8, s86, v176\n"
    "v_dot2c_f32_bf16 v9, s87, v176\n"
    "v_dot2c_f32_bf16 v10, s86, v177\n"
    "v_dot2c_f32_bf16 v11, s87, v177\n"
    "v_dot2c_f32_bf16 v12, s86, v178\n"
    "v_dot2c_f32_bf16 v13, s87, v178\n"
    "v_dot2c_f32_bf16 v14, s86, v179\n"
    "v_dot2c_f32_bf16 v15, s87, v179\n"
    "s_waitcnt vmcnt(13)\n"
    "v_dot2c_f32_bf16 v0, s86, v148\n"
    "v_dot2c_f32_bf16 v1, s87, v148\n"
    "v_dot2c_f32_bf16 v2, s86, v149\n"
    "v_dot2c_f32_bf16 v3, s87, v149\n"
    "v_dot2c_f32_bf16 v4, s86, v150\n"
    "v_dot2c_f32_bf16 v5, s87, v150\n"
    "v_dot2c_f32_bf16 v6, s86, v151\n"
    "v_dot2c_f32_bf16 v7, s87, v151\n"
    "s_waitcnt vmcnt(12)\n"
    "v_dot2c_f32_bf16 v8, s86, v180\n"
    "v_dot2c_f32_bf16 v9, s87, v180\n"
    "v_dot2c_f32_bf16 v10, s86, v181\n"
    "v_dot2c_f32_bf16 v11, s87, v181\n"
    "v_dot2c_f32_bf16 v12, s86, v182\n"
    "v_dot2c_f32_bf16 v13, s87, v182\n"
    "v_dot2c_f32_bf16 v14, s86, v183\n"
    "v_dot2c_f32_bf16 v15, s87, v183\n"
    "s_waitcnt vmcnt(11)\n"
    "v_dot2c_f32_bf16 v0, s86, v152\n"
    "v_dot2c_f32_bf16 v1, s87, v152\n"
    "v_dot2c_f32_bf16 v2, s86, v153\n"
    "v_dot2c_f32_bf16 v3, s87, v153\n"
    "v_dot2c_f32_bf16 v4, s86, v154\n"
    "v_dot2c_f32_bf16 v5, s87, v154\n"
    "v_dot2c_f32_bf16 v6, s86, v155\n"
    "v_dot2c_f32_bf16 v7, s87, v155\n"
    "s_waitcnt vmcnt(10)\n"
    "v_dot2c_f32_bf16 v8, s86, v184\n"
    "v_dot2c_f32_bf16 v9, s87, v184\n"
    "v_dot2c_f32_bf16 v10, s86, v185\n"
    "v_dot2c_f32_bf16 v11, s87, v185\n"
    "v_dot2c_f32_bf16 v12, s86, v186\n"
    "v_dot2c_f32_bf16 v13, s87, v186\n"
    "v_dot2c_f32_bf16 v14, s86, v187\n"
    "v_dot2c_f32_bf16 v15, s87, v187\n"
    "s_waitcnt vmcnt(9)\n"
    "v_dot2c_f32_bf16 v0, s86, v156\n"
    "v_dot2c_f32_bf16 v1, s87, v156\n"
    "v_dot2c_f32_bf16 v2, s86, v157\n"
    "v_dot2c_f32_bf16 v3, s87, v157\n"
    "v_dot2c_f32_bf16 v4, s86, v158\n"
    "v_dot2c_f32_bf16 v5, s87, v158\n"
    "v_dot2c_f32_bf16 v6, s86, v159\n"
    "v_dot2c_f32_bf16 v7, s87, v159\n"
    "s_waitcnt vmcnt(8)\n"
    "v_dot2c_f32_bf16 v8, s86, v188\n"
    "v_dot2c_f32_bf16 v9, s87, v188\n"
    "v_dot2c_f32_bf16 v10, s86, v189\n"
    "v_dot2c_f32_bf16 v11, s87, v189\n"
    "v_dot2c_f32_bf16 v12, s86, v190\n"
    "v_dot2c_f32_bf16 v13, s87, v190\n"
    "v_dot2c_f32_bf16 v14, s86, v191\n"
    "v_dot2c_f32_bf16 v15, s87, v191\n"
    "s_waitcnt vmcnt(7)\n"
    "v_dot2c_f32_bf16 v0, s86, v160\n"
    "v_dot2c_f32_bf16 v1, s87, v160\n"
    "v_dot2c_f32_bf16 v2, s86, v161\n"
    "v_dot2c_f32_bf16 v3, s87, v161\n"
    "v_dot2c_f32_bf16 v4, s86, v162\n"
    "v_dot2c_f32_bf16 v5, s87, v162\n"
    "v_dot2c_f32_bf16 v6, s86, v163\n"
    "v_dot2c_f32_bf16 v7, s87, v163\n"
    "s_waitcnt vmcnt(6)\n"
    "v_dot2c_f32_bf16 v8, s86, v192\n"
    "v_dot2c_f32_bf16 v9, s87, v192\n"
    "v_dot2c_f32_bf16 v10, s86, v193\n"
    "v_dot2c_f32_bf16 v11, s87, v193\n"
    "v_dot2c_f32_bf16 v12, s86, v194\n"
    "v_dot2c_f32_bf16 v13, s87, v194\n"
    "v_dot2c_f32_bf16 v14, s86, v195\n"
    "v_dot2c_f32_bf16 v15, s87, v195\n"
    "s_waitcnt vmcnt(5)\n"
    "v_dot2c_f32_bf16 v0, s86, v164\n"
    "v_dot2c_f32_bf16 v1, s87, v164\n"
    "v_dot2c_f32_bf16 v2, s86, v165\n"
    "v_dot2c_f32_bf16 v3, s87, v165\n"
    "v_dot2c_f32_bf16 v4, s86, v166\n"
    "v_dot2c_f32_bf16 v5, s87, v166\n"
    "v_dot2c_f32_bf16 v6, s86, v167\n"
    "v_dot2c_f32_bf16 v7, s87, v167\n"
    "s_waitcnt vmcnt(4)\n"
    "v_dot2c_f32_bf16 v8, s86, v196\n"
    "v_dot2c_f32_bf16 v9, s87, v196\n"
    "v_dot2c_f32_bf16 v10, s86, v197\n"
    "v_dot2c_f32_bf16 v11, s87, v197\n"
    "v_dot2c_f32_bf16 v12, s86, v198\n"
    "v_dot2c_f32_bf16 v13, s87, v198\n"
    "v_dot2c_f32_bf16 v14, s86, v199\n"
    "v_dot2c_f32_bf16 v15, s87, v199\n"
    "s_waitcnt vmcnt(3)\n"
    "v_dot2c_f32_bf16 v0, s86, v168\n"
    "v_dot2c_f32_bf16 v1, s87, v168\n"
    "v_dot2c_f32_bf16 v2, s86, v169\n"
    "v_dot2c_f32_bf16 v3, s87, v169\n"
    "v_dot2c_f32_bf16 v4, s86, v170\n"
    "v_dot2c_f32_bf16 v5, s87, v170\n"
    "v_dot2c_f32_bf16 v6, s86, v171\n"
    "v_dot2c_f32_bf16 v7, s87, v171\n"
    "s_waitcnt vmcnt(2)\n"
    "v_dot2c_f32_bf16 v8, s86, v200\n"
    "v_dot2c_f32_bf16 v9, s87, v200\n"
    "v_dot2c_f32_bf16 v10, s86, v201\n"
    "v_dot2c_f32_bf16 v11, s87, v201\n"
    "v_dot2c_f32_bf16 v12, s86, v202\n"
    "v_dot2c_f32_bf16 v13, s87, v202\n"
    "v_dot2c_f32_bf16 v14, s86, v203\n"
    "v_dot2c_f32_bf16 v15, s87, v203\n"
    "s_waitcnt vmcnt(1)\n"
    "v_dot2c_f32_bf16 v0, s86, v172\n"
    "v_dot2c_f32_bf16 v1, s87, v172\n"
    "v_dot2c_f32_bf16 v2, s86, v173\n"
    "v_dot2c_f32_bf16 v3, s87, v173\n"
    "v_dot2c_f32_bf16 v4, s86, v174\n"
    "v_dot2c_f32_bf16 v5, s87, v174\n"
    "v_dot2c_f32_bf16 v6, s86, v175\n"
    "v_dot2c_f32_bf16 v7, s87, v175\n"
    "s_waitcnt vmcnt(0)\n"
    "v_dot2c_f32_bf16 v8, s86, v204\n"
    "v_dot2c_f32_bf16 v9, s87, v204\n"
    "v_dot2c_f32_bf16 v10, s86, v205\n"
    "v_dot2c_f32_bf16 v11, s87, v205\n"
    "v_dot2c_f32_bf16 v12, s86, v206\n"
    "v_dot2c_f32_bf16 v13, s87, v206\n"
    "v_dot2c_f32_bf16 v14, s86, v207\n"
    "v_dot2c_f32_bf16 v15, s87, v207\n"
    "s_nop 2\n"
    "v_fma_f32 v0, v0, v96, v112\n"
    "v_fma_f32 v1, v1, v97, v113\n"
    "v_fma_f32 v2, v2, v98, v114\n"
    "v_fma_f32 v3, v3, v99, v115\n"
    "v_fma_f32 v4, v4, v100, v116\n"
    "v_fma_f32 v5, v5, v101, v117\n"
    "v_fma_f32 v6, v6, v102, v118\n"
    "v_fma_f32 v7, v7, v103, v119\n"
    "v_cvt_pk_bf16_f32 v16, v0, v1\n"
    "v_cvt_pk_bf16_f32 v17, v2, v3\n"
    "v_cvt_pk_bf16_f32 v18, v4, v5\n"
    "v_cvt_pk_bf16_f32 v19, v6, v7\n"
    "buffer_store_dwordx2 v[16:17], v36, s[48:51], 0 offen\n"
    "buffer_store_dwordx2 v[18:19], v37, s[48:51], 0 offen\n"
    "v_fma_f32 v8, v8, v104, v120\n"
    "v_fma_f32 v9, v9, v105, v121\n"
    "v_fma_f32 v10, v10, v106, v122\n"
    "v_fma_f32 v11, v11, v107, v123\n"
    "v_fma_f32 v12, v12, v108, v124\n"
    "v_fma_f32 v13, v13, v109, v125\n"
    "v_fma_f32 v14, v14, v110, v126\n"
    "v_fma_f32 v15, v15, v111, v127\n"
    "v_cvt_pk_bf16_f32 v20, v8, v9\n"
    "v_cvt_pk_bf16_f32 v21, v10, v11\n"
    "v_cvt_pk_bf16_f32 v22, v12, v13\n"
    "v_cvt_pk_bf16_f32 v23, v14, v15\n"
    "buffer_store_dwordx2 v[20:21], v38, s[48:51], 0 offen\n"
    "buffer_store_dwordx2 v[22:23], v39, s[48:51], 0 offen\n"
    "s_waitcnt vmcnt(0)\n"
    ".Lmainend:\n"
    "s_endpgm\n"
    ".Lslow:\n"
    "s_waitcnt vmcnt(0) lgkmcnt(0)\n"
    "s_barrier\n"
    "s_lshl_b32 s86, s70, 20\n"
    "s_add_u32 s52, s18, s86\n"
    "s_addc_u32 s53, s19, 0\n"
    "s_sub_u32 s52, s52, 3168\n"
    "s_subb_u32 s53, s53, 0\n"
    "s_and_b32 s53, s53, 0xffff\n"
    "s_mov_b32 s54, 8391776\n"
    "s_mov_b32 s55, 0x20000\n"
    "s_sub_u32 s54, s54, s86\n"
    "s_add_u32 s56, s20, s86\n"
    "s_addc_u32 s57, s21, 0\n"
    "s_sub_u32 s56, s56, 3168\n"
    "s_subb_u32 s57, s57, 0\n"
    "s_and_b32 s57, s57, 0xffff\n"
    "s_mov_b32 s58, 8391776\n"
    "s_mov_b32 s59, 0x20000\n"
    "s_sub_u32 s58, s58, s86\n"
    "v_accvgpr_write_b32 a0, 0\n"
    "v_accvgpr_write_b32 a1, 0\n"
    "v_accvgpr_write_b32 a2, 0\n"
    "v_accvgpr_write_b32 a3, 0\n"
    "v_accvgpr_write_b32 a4, 0\n"
    "v_accvgpr_write_b32 a5, 0\n"
    "v_accvgpr_write_b32 a6, 0\n"
    "v_accvgpr_write_b32 a7, 0\n"
    "v_accvgpr_write_b32 a8, 0\n"
    "v_accvgpr_write_b32 a9, 0\n"
    "v_accvgpr_write_b32 a10, 0\n"
    "v_accvgpr_write_b32 a11, 0\n"
    "v_accvgpr_write_b32 a12, 0\n"
    "v_accvgpr_write_b32 a13, 0\n"
    "v_accvgpr_write_b32 a14, 0\n"
    "v_accvgpr_write_b32 a15, 0\n"
    "v_accvgpr_write_b32 a16, 0\n"
    "v_accvgpr_write_b32 a17, 0\n"
    "v_accvgpr_write_b32 a18, 0\n"
    "v_accvgpr_write_b32 a19, 0\n"
    "v_accvgpr_write_b32 a20, 0\n"
    "v_accvgpr_write_b32 a21, 0\n"
    "v_accvgpr_write_b32 a22, 0\n"
    "v_accvgpr_write_b32 a23, 0\n"
    "v_accvgpr_write_b32 a24, 0\n"
    "v_accvgpr_write_b32 a25, 0\n"
    "v_accvgpr_write_b32 a26, 0\n"
    "v_accvgpr_write_b32 a27, 0\n"
    "v_accvgpr_write_b32 a28, 0\n"
    "v_accvgpr_write_b32 a29, 0\n"
    "v_accvgpr_write_b32 a30, 0\n"
    "v_accvgpr_write_b32 a31, 0\n"
    "v_accvgpr_write_b32 a32, 0\n"
    "v_accvgpr_write_b32 a33, 0\n"
    "v_accvgpr_write_b32 a34, 0\n"
    "v_accvgpr_write_b32 a35, 0\n"
    "v_accvgpr_write_b32 a36, 0\n"
    "v_accvgpr_write_b32 a37, 0\n"
    "v_accvgpr_write_b32 a38, 0\n"
    "v_accvgpr_write_b32 a39, 0\n"
    "v_accvgpr_write_b32 a40, 0\n"
    "v_accvgpr_write_b32 a41, 0\n"
    "v_accvgpr_write_b32 a42, 0\n"
    "v_accvgpr_write_b32 a43, 0\n"
    "v_accvgpr_write_b32 a44, 0\n"
    "v_accvgpr_write_b32 a45, 0\n"
    "v_accvgpr_write_b32 a46, 0\n"
    "v_accvgpr_write_b32 a47, 0\n"
    "v_accvgpr_write_b32 a48, 0\n"
    "v_accvgpr_write_b32 a49, 0\n"
    "v_accvgpr_write_b32 a50, 0\n"
    "v_accvgpr_write_b32 a51, 0\n"
    "v_accvgpr_write_b32 a52, 0\n"
    "v_accvgpr_write_b32 a53, 0\n"
    "v_accvgpr_write_b32 a54, 0\n"
    "v_accvgpr_write_b32 a55, 0\n"
    "v_accvgpr_write_b32 a56, 0\n"
    "v_accvgpr_write_b32 a57, 0\n"
    "v_accvgpr_write_b32 a58, 0\n"
    "v_accvgpr_write_b32 a59, 0\n"
    "v_accvgpr_write_b32 a60, 0\n"
    "v_accvgpr_write_b32 a61, 0\n"
    "v_accvgpr_write_b32 a62, 0\n"
    "v_accvgpr_write_b32 a63, 0\n"
    "v_accvgpr_write_b32 a64, 0\n"
    "v_accvgpr_write_b32 a65, 0\n"
    "v_accvgpr_write_b32 a66, 0\n"
    "v_accvgpr_write_b32 a67, 0\n"
    "v_accvgpr_write_b32 a68, 0\n"
    "v_accvgpr_write_b32 a69, 0\n"
    "v_accvgpr_write_b32 a70, 0\n"
    "v_accvgpr_write_b32 a71, 0\n"
    "v_accvgpr_write_b32 a72, 0\n"
    "v_accvgpr_write_b32 a73, 0\n"
    "v_accvgpr_write_b32 a74, 0\n"
    "v_accvgpr_write_b32 a75, 0\n"
    "v_accvgpr_write_b32 a76, 0\n"
    "v_accvgpr_write_b32 a77, 0\n"
    "v_accvgpr_write_b32 a78, 0\n"
    "v_accvgpr_write_b32 a79, 0\n"
    "v_accvgpr_write_b32 a80, 0\n"
    "v_accvgpr_write_b32 a81, 0\n"
    "v_accvgpr_write_b32 a82, 0\n"
    "v_accvgpr_write_b32 a83, 0\n"
    "v_accvgpr_write_b32 a84, 0\n"
    "v_accvgpr_write_b32 a85, 0\n"
    "v_accvgpr_write_b32 a86, 0\n"
    "v_accvgpr_write_b32 a87, 0\n"
    "v_accvgpr_write_b32 a88, 0\n"
    "v_accvgpr_write_b32 a89, 0\n"
    "v_accvgpr_write_b32 a90, 0\n"
    "v_accvgpr_write_b32 a91, 0\n"
    "v_accvgpr_write_b32 a92, 0\n"
    "v_accvgpr_write_b32 a93, 0\n"
    "v_accvgpr_write_b32 a94, 0\n"
    "v_accvgpr_write_b32 a95, 0\n"
    "v_accvgpr_write_b32 a96, 0\n"
    "v_accvgpr_write_b32 a97, 0\n"
    "v_accvgpr_write_b32 a98, 0\n"
    "v_accvgpr_write_b32 a99, 0\n"
    "v_accvgpr_write_b32 a100, 0\n"
    "v_accvgpr_write_b32 a101, 0\n"
    "v_accvgpr_write_b32 a102, 0\n"
    "v_accvgpr_write_b32 a103, 0\n"
    "v_accvgpr_write_b32 a104, 0\n"
    "v_accvgpr_write_b32 a105, 0\n"
    "v_accvgpr_write_b32 a106, 0\n"
    "v_accvgpr_write_b32 a107, 0\n"
    "v_accvgpr_write_b32 a108, 0\n"
    "v_accvgpr_write_b32 a109, 0\n"
    "v_accvgpr_write_b32 a110, 0\n"
    "v_accvgpr_write_b32 a111, 0\n"
    "v_accvgpr_write_b32 a112, 0\n"
    "v_accvgpr_write_b32 a113, 0\n"
    "v_accvgpr_write_b32 a114, 0\n"
    "v_accvgpr_write_b32 a115, 0\n"
    "v_accvgpr_write_b32 a116, 0\n"
    "v_accvgpr_write_b32 a117, 0\n"
    "v_accvgpr_write_b32 a118, 0\n"
    "v_accvgpr_write_b32 a119, 0\n"
    "v_accvgpr_write_b32 a120, 0\n"
    "v_accvgpr_write_b32 a121, 0\n"
    "v_accvgpr_write_b32 a122, 0\n"
    "v_accvgpr_write_b32 a123, 0\n"
    "v_accvgpr_write_b32 a124, 0\n"
    "v_accvgpr_write_b32 a125, 0\n"
    "v_accvgpr_write_b32 a126, 0\n"
    "v_accvgpr_write_b32 a127, 0\n"
    "v_accvgpr_write_b32 a128, 0\n"
    "v_accvgpr_write_b32 a129, 0\n"
    "v_accvgpr_write_b32 a130, 0\n"
    "v_accvgpr_write_b32 a131, 0\n"
    "v_accvgpr_write_b32 a132, 0\n"
    "v_accvgpr_write_b32 a133, 0\n"
    "v_accvgpr_write_b32 a134, 0\n"
    "v_accvgpr_write_b32 a135, 0\n"
    "v_accvgpr_write_b32 a136, 0\n"
    "v_accvgpr_write_b32 a137, 0\n"
    "v_accvgpr_write_b32 a138, 0\n"
    "v_accvgpr_write_b32 a139, 0\n"
    "v_accvgpr_write_b32 a140, 0\n"
    "v_accvgpr_write_b32 a141, 0\n"
    "v_accvgpr_write_b32 a142, 0\n"
    "v_accvgpr_write_b32 a143, 0\n"
    "v_accvgpr_write_b32 a144, 0\n"
    "v_accvgpr_write_b32 a145, 0\n"
    "v_accvgpr_write_b32 a146, 0\n"
    "v_accvgpr_write_b32 a147, 0\n"
    "v_accvgpr_write_b32 a148, 0\n"
    "v_accvgpr_write_b32 a149, 0\n"
    "v_accvgpr_write_b32 a150, 0\n"
    "v_accvgpr_write_b32 a151, 0\n"
    "v_accvgpr_write_b32 a152, 0\n"
    "v_accvgpr_write_b32 a153, 0\n"
    "v_accvgpr_write_b32 a154, 0\n"
    "v_accvgpr_write_b32 a155, 0\n"
    "v_accvgpr_write_b32 a156, 0\n"
    "v_accvgpr_write_b32 a157, 0\n"
    "v_accvgpr_write_b32 a158, 0\n"
    "v_accvgpr_write_b32 a159, 0\n"
    "v_accvgpr_write_b32 a160, 0\n"
    "v_accvgpr_write_b32 a161, 0\n"
    "v_accvgpr_write_b32 a162, 0\n"
    "v_accvgpr_write_b32 a163, 0\n"
    "v_accvgpr_write_b32 a164, 0\n"
    "v_accvgpr_write_b32 a165, 0\n"
    "v_accvgpr_write_b32 a166, 0\n"
    "v_accvgpr_write_b32 a167, 0\n"
    "v_accvgpr_write_b32 a168, 0\n"
    "v_accvgpr_write_b32 a169, 0\n"
    "v_accvgpr_write_b32 a170, 0\n"
    "v_accvgpr_write_b32 a171, 0\n"
    "v_accvgpr_write_b32 a172, 0\n"
    "v_accvgpr_write_b32 a173, 0\n"
    "v_accvgpr_write_b32 a174, 0\n"
    "v_accvgpr_write_b32 a175, 0\n"
    "v_accvgpr_write_b32 a176, 0\n"
    "v_accvgpr_write_b32 a177, 0\n"
    "v_accvgpr_write_b32 a178, 0\n"
    "v_accvgpr_write_b32 a179, 0\n"
    "v_accvgpr_write_b32 a180, 0\n"
    "v_accvgpr_write_b32 a181, 0\n"
    "v_accvgpr_write_b32 a182, 0\n"
    "v_accvgpr_write_b32 a183, 0\n"
    "v_accvgpr_write_b32 a184, 0\n"
    "v_accvgpr_write_b32 a185, 0\n"
    "v_accvgpr_write_b32 a186, 0\n"
    "v_accvgpr_write_b32 a187, 0\n"
    "v_accvgpr_write_b32 a188, 0\n"
    "v_accvgpr_write_b32 a189, 0\n"
    "v_accvgpr_write_b32 a190, 0\n"
    "v_accvgpr_write_b32 a191, 0\n"
    "v_accvgpr_write_b32 a192, 0\n"
    "v_accvgpr_write_b32 a193, 0\n"
    "v_accvgpr_write_b32 a194, 0\n"
    "v_accvgpr_write_b32 a195, 0\n"
    "v_accvgpr_write_b32 a196, 0\n"
    "v_accvgpr_write_b32 a197, 0\n"
    "v_accvgpr_write_b32 a198, 0\n"
    "v_accvgpr_write_b32 a199, 0\n"
    "v_accvgpr_write_b32 a200, 0\n"
    "v_accvgpr_write_b32 a201, 0\n"
    "v_accvgpr_write_b32 a202, 0\n"
    "v_accvgpr_write_b32 a203, 0\n"
    "v_accvgpr_write_b32 a204, 0\n"
    "v_accvgpr_write_b32 a205, 0\n"
    "v_accvgpr_write_b32 a206, 0\n"
    "v_accvgpr_write_b32 a207, 0\n"
    "v_accvgpr_write_b32 a208, 0\n"
    "v_accvgpr_write_b32 a209, 0\n"
    "v_accvgpr_write_b32 a210, 0\n"
    "v_accvgpr_write_b32 a211, 0\n"
    "v_accvgpr_write_b32 a212, 0\n"
    "v_accvgpr_write_b32 a213, 0\n"
    "v_accvgpr_write_b32 a214, 0\n"
    "v_accvgpr_write_b32 a215, 0\n"
    "v_accvgpr_write_b32 a216, 0\n"
    "v_accvgpr_write_b32 a217, 0\n"
    "v_accvgpr_write_b32 a218, 0\n"
    "v_accvgpr_write_b32 a219, 0\n"
    "v_accvgpr_write_b32 a220, 0\n"
    "v_accvgpr_write_b32 a221, 0\n"
    "v_accvgpr_write_b32 a222, 0\n"
    "v_accvgpr_write_b32 a223, 0\n"
    "v_accvgpr_write_b32 a224, 0\n"
    "v_accvgpr_write_b32 a225, 0\n"
    "v_accvgpr_write_b32 a226, 0\n"
    "v_accvgpr_write_b32 a227, 0\n"
    "v_accvgpr_write_b32 a228, 0\n"
    "v_accvgpr_write_b32 a229, 0\n"
    "v_accvgpr_write_b32 a230, 0\n"
    "v_accvgpr_write_b32 a231, 0\n"
    "v_accvgpr_write_b32 a232, 0\n"
    "v_accvgpr_write_b32 a233, 0\n"
    "v_accvgpr_write_b32 a234, 0\n"
    "v_accvgpr_write_b32 a235, 0\n"
    "v_accvgpr_write_b32 a236, 0\n"
    "v_accvgpr_write_b32 a237, 0\n"
    "v_accvgpr_write_b32 a238, 0\n"
    "v_accvgpr_write_b32 a239, 0\n"
    "v_accvgpr_write_b32 a240, 0\n"
    "v_accvgpr_write_b32 a241, 0\n"
    "v_accvgpr_write_b32 a242, 0\n"
    "v_accvgpr_write_b32 a243, 0\n"
    "v_accvgpr_write_b32 a244, 0\n"
    "v_accvgpr_write_b32 a245, 0\n"
    "v_accvgpr_write_b32 a246, 0\n"
    "v_accvgpr_write_b32 a247, 0\n"
    "v_accvgpr_write_b32 a248, 0\n"
    "v_accvgpr_write_b32 a249, 0\n"
    "v_accvgpr_write_b32 a250, 0\n"
    "v_accvgpr_write_b32 a251, 0\n"
    "v_accvgpr_write_b32 a252, 0\n"
    "v_accvgpr_write_b32 a253, 0\n"
    "v_accvgpr_write_b32 a254, 0\n"
    "v_accvgpr_write_b32 a255, 0\n"
    "v_mov_b32 v64, 0xff800000\n"
    "v_mov_b32 v80, 0\n"
    "v_mov_b32 v68, 0xff800000\n"
    "v_mov_b32 v81, 0\n"
    "v_mov_b32 v72, 0xff800000\n"
    "v_mov_b32 v82, 0\n"
    "v_mov_b32 v76, 0xff800000\n"
    "v_mov_b32 v83, 0\n"
    "s_mov_b32 s85, 32\n"
    ".Lsloop:\n"
    "s_barrier\n"
    "s_add_u32 m0, s74, 0\n"
    "s_nop 0\n"
    "buffer_load_dwordx4 v126, s[52:55], s77 offen offset:0 lds\n"
    "buffer_load_dwordx4 v126, s[52:55], s78 offen offset:1056 lds\n"
    "buffer_load_dwordx4 v126, s[52:55], s79 offen offset:2112 lds\n"
    "buffer_load_dwordx4 v126, s[52:55], s80 offen offset:3168 lds\n"
    "s_add_u32 m0, s74, 4224\n"
    "s_nop 0\n"
    "buffer_load_dwordx4 v126, s[52:55], s81 offen offset:0 lds\n"
    "buffer_load_dwordx4 v126, s[52:55], s82 offen offset:1056 lds\n"
    "buffer_load_dwordx4 v126, s[52:55], s83 offen offset:2112 lds\n"
    "buffer_load_dwordx4 v126, s[52:55], s84 offen offset:3168 lds\n"
    "s_add_u32 s52, s52, 32768\n"
    "s_addc_u32 s53, s53, 0\n"
    "s_sub_u32 s54, s54, 32768\n"
    "s_add_u32 m0, s74, 67584\n"
    "s_nop 0\n"
    "buffer_load_dwordx4 v126, s[56:59], s77 offen offset:0 lds\n"
    "buffer_load_dwordx4 v126, s[56:59], s78 offen offset:1056 lds\n"
    "buffer_load_dwordx4 v126, s[56:59], s79 offen offset:2112 lds\n"
    "buffer_load_dwordx4 v126, s[56:59], s80 offen offset:3168 lds\n"
    "s_add_u32 m0, s74, 71808\n"
    "s_nop 0\n"
    "buffer_load_dwordx4 v126, s[56:59], s81 offen offset:0 lds\n"
    "buffer_load_dwordx4 v126, s[56:59], s82 offen offset:1056 lds\n"
    "buffer_load_dwordx4 v126, s[56:59], s83 offen offset:2112 lds\n"
    "buffer_load_dwordx4 v126, s[56:59], s84 offen offset:3168 lds\n"
    "s_add_u32 s56, s56, 32768\n"
    "s_addc_u32 s57, s57, 0\n"
    "s_sub_u32 s58, s58, 32768\n"
    "s_waitcnt vmcnt(0)\n"
    "s_barrier\n"
    "ds_read_b128 v[84:87], v124 offset:0\n"
    "s_waitcnt lgkmcnt(0)\n"
    "v_mfma_f32_16x16x32_bf16 v[0:3], v[84:87], v[128:131], 0\n"
    "v_mfma_f32_16x16x32_bf16 v[8:11], v[84:87], v[132:135], 0\n"
    "v_mfma_f32_16x16x32_bf16 v[16:19], v[84:87], v[192:195], 0\n"
    "v_mfma_f32_16x16x32_bf16 v[24:27], v[84:87], v[196:199], 0\n"
    "ds_read_b128 v[88:91], v124 offset:64\n"
    "s_waitcnt lgkmcnt(0)\n"
    "v_mfma_f32_16x16x32_bf16 v[0:3], v[88:91], v[136:139], v[0:3]\n"
    "v_mfma_f32_16x16x32_bf16 v[8:11], v[88:91], v[140:143], v[8:11]\n"
    "v_mfma_f32_16x16x32_bf16 v[16:19], v[88:91], v[200:203], v[16:19]\n"
    "v_mfma_f32_16x16x32_bf16 v[24:27], v[88:91], v[204:207], v[24:27]\n"
    "ds_read_b128 v[92:95], v124 offset:128\n"
    "s_waitcnt lgkmcnt(0)\n"
    "v_mfma_f32_16x16x32_bf16 v[0:3], v[92:95], v[144:147], v[0:3]\n"
    "v_mfma_f32_16x16x32_bf16 v[8:11], v[92:95], v[148:151], v[8:11]\n"
    "v_mfma_f32_16x16x32_bf16 v[16:19], v[92:95], v[208:211], v[16:19]\n"
    "v_mfma_f32_16x16x32_bf16 v[24:27], v[92:95], v[212:215], v[24:27]\n"
    "ds_read_b128 v[96:99], v124 offset:192\n"
    "s_waitcnt lgkmcnt(0)\n"
    "v_mfma_f32_16x16x32_bf16 v[0:3], v[96:99], v[152:155], v[0:3]\n"
    "v_mfma_f32_16x16x32_bf16 v[8:11], v[96:99], v[156:159], v[8:11]\n"
    "v_mfma_f32_16x16x32_bf16 v[16:19], v[96:99], v[216:219], v[16:19]\n"
    "v_mfma_f32_16x16x32_bf16 v[24:27], v[96:99], v[220:223], v[24:27]\n"
    "ds_read_b128 v[100:103], v119 offset:256\n"
    "s_waitcnt lgkmcnt(0)\n"
    "v_mfma_f32_16x16x32_bf16 v[0:3], v[100:103], v[160:163], v[0:3]\n"
    "v_mfma_f32_16x16x32_bf16 v[8:11], v[100:103], v[164:167], v[8:11]\n"
    "v_mfma_f32_16x16x32_bf16 v[16:19], v[100:103], v[224:227], v[16:19]\n"
    "v_mfma_f32_16x16x32_bf16 v[24:27], v[100:103], v[228:231], v[24:27]\n"
    "ds_read_b128 v[104:107], v119 offset:320\n"
    "s_waitcnt lgkmcnt(0)\n"
    "v_mfma_f32_16x16x32_bf16 v[0:3], v[104:107], v[168:171], v[0:3]\n"
    "v_mfma_f32_16x16x32_bf16 v[8:11], v[104:107], v[172:175], v[8:11]\n"
    "v_mfma_f32_16x16x32_bf16 v[16:19], v[104:107], v[232:235], v[16:19]\n"
    "v_mfma_f32_16x16x32_bf16 v[24:27], v[104:107], v[236:239], v[24:27]\n"
    "ds_read_b128 v[108:111], v119 offset:384\n"
    "s_waitcnt lgkmcnt(0)\n"
    "v_mfma_f32_16x16x32_bf16 v[0:3], v[108:111], v[176:179], v[0:3]\n"
    "v_mfma_f32_16x16x32_bf16 v[8:11], v[108:111], v[180:183], v[8:11]\n"
    "v_mfma_f32_16x16x32_bf16 v[16:19], v[108:111], v[240:243], v[16:19]\n"
    "v_mfma_f32_16x16x32_bf16 v[24:27], v[108:111], v[244:247], v[24:27]\n"
    "ds_read_b128 v[112:115], v119 offset:448\n"
    "s_waitcnt lgkmcnt(0)\n"
    "v_mfma_f32_16x16x32_bf16 v[0:3], v[112:115], v[184:187], v[0:3]\n"
    "v_mfma_f32_16x16x32_bf16 v[8:11], v[112:115], v[188:191], v[8:11]\n"
    "v_mfma_f32_16x16x32_bf16 v[16:19], v[112:115], v[248:251], v[16:19]\n"
    "v_mfma_f32_16x16x32_bf16 v[24:27], v[112:115], v[252:255], v[24:27]\n"
    "ds_read_b128 v[84:87], v124 offset:16896\n"
    "s_waitcnt lgkmcnt(0)\n"
    "v_mfma_f32_16x16x32_bf16 v[4:7], v[84:87], v[128:131], 0\n"
    "v_mfma_f32_16x16x32_bf16 v[12:15], v[84:87], v[132:135], 0\n"
    "v_mfma_f32_16x16x32_bf16 v[20:23], v[84:87], v[192:195], 0\n"
    "v_mfma_f32_16x16x32_bf16 v[28:31], v[84:87], v[196:199], 0\n"
    "ds_read_b128 v[88:91], v124 offset:16960\n"
    "s_waitcnt lgkmcnt(0)\n"
    "v_mfma_f32_16x16x32_bf16 v[4:7], v[88:91], v[136:139], v[4:7]\n"
    "v_mfma_f32_16x16x32_bf16 v[12:15], v[88:91], v[140:143], v[12:15]\n"
    "v_mfma_f32_16x16x32_bf16 v[20:23], v[88:91], v[200:203], v[20:23]\n"
    "v_mfma_f32_16x16x32_bf16 v[28:31], v[88:91], v[204:207], v[28:31]\n"
    "ds_read_b128 v[92:95], v124 offset:17024\n"
    "s_waitcnt lgkmcnt(0)\n"
    "v_mfma_f32_16x16x32_bf16 v[4:7], v[92:95], v[144:147], v[4:7]\n"
    "v_mfma_f32_16x16x32_bf16 v[12:15], v[92:95], v[148:151], v[12:15]\n"
    "v_mfma_f32_16x16x32_bf16 v[20:23], v[92:95], v[208:211], v[20:23]\n"
    "v_mfma_f32_16x16x32_bf16 v[28:31], v[92:95], v[212:215], v[28:31]\n"
    "ds_read_b128 v[96:99], v124 offset:17088\n"
    "s_waitcnt lgkmcnt(0)\n"
    "v_mfma_f32_16x16x32_bf16 v[4:7], v[96:99], v[152:155], v[4:7]\n"
    "v_mfma_f32_16x16x32_bf16 v[12:15], v[96:99], v[156:159], v[12:15]\n"
    "v_mfma_f32_16x16x32_bf16 v[20:23], v[96:99], v[216:219], v[20:23]\n"
    "v_mfma_f32_16x16x32_bf16 v[28:31], v[96:99], v[220:223], v[28:31]\n"
    "ds_read_b128 v[100:103], v119 offset:17152\n"
    "s_waitcnt lgkmcnt(0)\n"
    "v_mfma_f32_16x16x32_bf16 v[4:7], v[100:103], v[160:163], v[4:7]\n"
    "v_mfma_f32_16x16x32_bf16 v[12:15], v[100:103], v[164:167], v[12:15]\n"
    "v_mfma_f32_16x16x32_bf16 v[20:23], v[100:103], v[224:227], v[20:23]\n"
    "v_mfma_f32_16x16x32_bf16 v[28:31], v[100:103], v[228:231], v[28:31]\n"
    "ds_read_b128 v[104:107], v119 offset:17216\n"
    "s_waitcnt lgkmcnt(0)\n"
    "v_mfma_f32_16x16x32_bf16 v[4:7], v[104:107], v[168:171], v[4:7]\n"
    "v_mfma_f32_16x16x32_bf16 v[12:15], v[104:107], v[172:175], v[12:15]\n"
    "v_mfma_f32_16x16x32_bf16 v[20:23], v[104:107], v[232:235], v[20:23]\n"
    "v_mfma_f32_16x16x32_bf16 v[28:31], v[104:107], v[236:239], v[28:31]\n"
    "ds_read_b128 v[108:111], v119 offset:17280\n"
    "s_waitcnt lgkmcnt(0)\n"
    "v_mfma_f32_16x16x32_bf16 v[4:7], v[108:111], v[176:179], v[4:7]\n"
    "v_mfma_f32_16x16x32_bf16 v[12:15], v[108:111], v[180:183], v[12:15]\n"
    "v_mfma_f32_16x16x32_bf16 v[20:23], v[108:111], v[240:243], v[20:23]\n"
    "v_mfma_f32_16x16x32_bf16 v[28:31], v[108:111], v[244:247], v[28:31]\n"
    "ds_read_b128 v[112:115], v119 offset:17344\n"
    "s_waitcnt lgkmcnt(0)\n"
    "v_mfma_f32_16x16x32_bf16 v[4:7], v[112:115], v[184:187], v[4:7]\n"
    "v_mfma_f32_16x16x32_bf16 v[12:15], v[112:115], v[188:191], v[12:15]\n"
    "v_mfma_f32_16x16x32_bf16 v[20:23], v[112:115], v[248:251], v[20:23]\n"
    "v_mfma_f32_16x16x32_bf16 v[28:31], v[112:115], v[252:255], v[28:31]\n"
    "s_nop 15\n"
    "v_max3_f32 v65, v0, v1, v2\n"
    "v_max3_f32 v65, v65, v3, v4\n"
    "v_max3_f32 v65, v65, v5, v6\n"
    "v_max_f32 v65, v65, v7\n"
    "v_mov_b32 v66, v65\n"
    "s_nop 1\n"
    "v_permlane32_swap_b32 v65, v66\n"
    "s_nop 1\n"
    "v_max_f32 v65, v65, v66\n"
    "v_mov_b32 v66, v65\n"
    "s_nop 1\n"
    "v_permlane16_swap_b32 v65, v66\n"
    "s_nop 1\n"
    "v_max_f32 v65, v65, v66\n"
    "v_max_f32 v65, v65, v64\n"
    "v_sub_f32 v66, v64, v65\n"
    "v_exp_f32 v66, v66\n"
    "v_mov_b32 v64, v65\n"
    "v_mul_f32 v80, v80, v66\n"
    "v_sub_f32 v0, v0, v64\n"
    "v_sub_f32 v1, v1, v64\n"
    "v_sub_f32 v2, v2, v64\n"
    "v_sub_f32 v3, v3, v64\n"
    "v_sub_f32 v4, v4, v64\n"
    "v_sub_f32 v5, v5, v64\n"
    "v_sub_f32 v6, v6, v64\n"
    "v_sub_f32 v7, v7, v64\n"
    "v_accvgpr_read_b32 v32, a0\n"
    "v_accvgpr_read_b32 v33, a1\n"
    "v_accvgpr_read_b32 v34, a2\n"
    "v_accvgpr_read_b32 v35, a3\n"
    "v_mul_f32 v32, v32, v66\n"
    "v_mul_f32 v33, v33, v66\n"
    "v_mul_f32 v34, v34, v66\n"
    "v_mul_f32 v35, v35, v66\n"
    "v_accvgpr_write_b32 a0, v32\n"
    "v_accvgpr_write_b32 a1, v33\n"
    "v_accvgpr_write_b32 a2, v34\n"
    "v_accvgpr_write_b32 a3, v35\n"
    "v_accvgpr_read_b32 v32, a8\n"
    "v_accvgpr_read_b32 v33, a9\n"
    "v_accvgpr_read_b32 v34, a10\n"
    "v_accvgpr_read_b32 v35, a11\n"
    "v_mul_f32 v32, v32, v66\n"
    "v_mul_f32 v33, v33, v66\n"
    "v_mul_f32 v34, v34, v66\n"
    "v_mul_f32 v35, v35, v66\n"
    "v_accvgpr_write_b32 a8, v32\n"
    "v_accvgpr_write_b32 a9, v33\n"
    "v_accvgpr_write_b32 a10, v34\n"
    "v_accvgpr_write_b32 a11, v35\n"
    "v_accvgpr_read_b32 v32, a16\n"
    "v_accvgpr_read_b32 v33, a17\n"
    "v_accvgpr_read_b32 v34, a18\n"
    "v_accvgpr_read_b32 v35, a19\n"
    "v_mul_f32 v32, v32, v66\n"
    "v_mul_f32 v33, v33, v66\n"
    "v_mul_f32 v34, v34, v66\n"
    "v_mul_f32 v35, v35, v66\n"
    "v_accvgpr_write_b32 a16, v32\n"
    "v_accvgpr_write_b32 a17, v33\n"
    "v_accvgpr_write_b32 a18, v34\n"
    "v_accvgpr_write_b32 a19, v35\n"
    "v_accvgpr_read_b32 v32, a24\n"
    "v_accvgpr_read_b32 v33, a25\n"
    "v_accvgpr_read_b32 v34, a26\n"
    "v_accvgpr_read_b32 v35, a27\n"
    "v_mul_f32 v32, v32, v66\n"
    "v_mul_f32 v33, v33, v66\n"
    "v_mul_f32 v34, v34, v66\n"
    "v_mul_f32 v35, v35, v66\n"
    "v_accvgpr_write_b32 a24, v32\n"
    "v_accvgpr_write_b32 a25, v33\n"
    "v_accvgpr_write_b32 a26, v34\n"
    "v_accvgpr_write_b32 a27, v35\n"
    "v_accvgpr_read_b32 v32, a32\n"
    "v_accvgpr_read_b32 v33, a33\n"
    "v_accvgpr_read_b32 v34, a34\n"
    "v_accvgpr_read_b32 v35, a35\n"
    "v_mul_f32 v32, v32, v66\n"
    "v_mul_f32 v33, v33, v66\n"
    "v_mul_f32 v34, v34, v66\n"
    "v_mul_f32 v35, v35, v66\n"
    "v_accvgpr_write_b32 a32, v32\n"
    "v_accvgpr_write_b32 a33, v33\n"
    "v_accvgpr_write_b32 a34, v34\n"
    "v_accvgpr_write_b32 a35, v35\n"
    "v_accvgpr_read_b32 v32, a40\n"
    "v_accvgpr_read_b32 v33, a41\n"
    "v_accvgpr_read_b32 v34, a42\n"
    "v_accvgpr_read_b32 v35, a43\n"
    "v_mul_f32 v32, v32, v66\n"
    "v_mul_f32 v33, v33, v66\n"
    "v_mul_f32 v34, v34, v66\n"
    "v_mul_f32 v35, v35, v66\n"
    "v_accvgpr_write_b32 a40, v32\n"
    "v_accvgpr_write_b32 a41, v33\n"
    "v_accvgpr_write_b32 a42, v34\n"
    "v_accvgpr_write_b32 a43, v35\n"
    "v_accvgpr_read_b32 v32, a48\n"
    "v_accvgpr_read_b32 v33, a49\n"
    "v_accvgpr_read_b32 v34, a50\n"
    "v_accvgpr_read_b32 v35, a51\n"
    "v_mul_f32 v32, v32, v66\n"
    "v_mul_f32 v33, v33, v66\n"
    "v_mul_f32 v34, v34, v66\n"
    "v_mul_f32 v35, v35, v66\n"
    "v_accvgpr_write_b32 a48, v32\n"
    "v_accvgpr_write_b32 a49, v33\n"
    "v_accvgpr_write_b32 a50, v34\n"
    "v_accvgpr_write_b32 a51, v35\n"
    "v_accvgpr_read_b32 v32, a56\n"
    "v_accvgpr_read_b32 v33, a57\n"
    "v_accvgpr_read_b32 v34, a58\n"
    "v_accvgpr_read_b32 v35, a59\n"
    "v_mul_f32 v32, v32, v66\n"
    "v_mul_f32 v33, v33, v66\n"
    "v_mul_f32 v34, v34, v66\n"
    "v_mul_f32 v35, v35, v66\n"
    "v_accvgpr_write_b32 a56, v32\n"
    "v_accvgpr_write_b32 a57, v33\n"
    "v_accvgpr_write_b32 a58, v34\n"
    "v_accvgpr_write_b32 a59, v35\n"
    "v_accvgpr_read_b32 v32, a64\n"
    "v_accvgpr_read_b32 v33, a65\n"
    "v_accvgpr_read_b32 v34, a66\n"
    "v_accvgpr_read_b32 v35, a67\n"
    "v_mul_f32 v32, v32, v66\n"
    "v_mul_f32 v33, v33, v66\n"
    "v_mul_f32 v34, v34, v66\n"
    "v_mul_f32 v35, v35, v66\n"
    "v_accvgpr_write_b32 a64, v32\n"
    "v_accvgpr_write_b32 a65, v33\n"
    "v_accvgpr_write_b32 a66, v34\n"
    "v_accvgpr_write_b32 a67, v35\n"
    "v_accvgpr_read_b32 v32, a72\n"
    "v_accvgpr_read_b32 v33, a73\n"
    "v_accvgpr_read_b32 v34, a74\n"
    "v_accvgpr_read_b32 v35, a75\n"
    "v_mul_f32 v32, v32, v66\n"
    "v_mul_f32 v33, v33, v66\n"
    "v_mul_f32 v34, v34, v66\n"
    "v_mul_f32 v35, v35, v66\n"
    "v_accvgpr_write_b32 a72, v32\n"
    "v_accvgpr_write_b32 a73, v33\n"
    "v_accvgpr_write_b32 a74, v34\n"
    "v_accvgpr_write_b32 a75, v35\n"
    "v_accvgpr_read_b32 v32, a80\n"
    "v_accvgpr_read_b32 v33, a81\n"
    "v_accvgpr_read_b32 v34, a82\n"
    "v_accvgpr_read_b32 v35, a83\n"
    "v_mul_f32 v32, v32, v66\n"
    "v_mul_f32 v33, v33, v66\n"
    "v_mul_f32 v34, v34, v66\n"
    "v_mul_f32 v35, v35, v66\n"
    "v_accvgpr_write_b32 a80, v32\n"
    "v_accvgpr_write_b32 a81, v33\n"
    "v_accvgpr_write_b32 a82, v34\n"
    "v_accvgpr_write_b32 a83, v35\n"
    "v_accvgpr_read_b32 v32, a88\n"
    "v_accvgpr_read_b32 v33, a89\n"
    "v_accvgpr_read_b32 v34, a90\n"
    "v_accvgpr_read_b32 v35, a91\n"
    "v_mul_f32 v32, v32, v66\n"
    "v_mul_f32 v33, v33, v66\n"
    "v_mul_f32 v34, v34, v66\n"
    "v_mul_f32 v35, v35, v66\n"
    "v_accvgpr_write_b32 a88, v32\n"
    "v_accvgpr_write_b32 a89, v33\n"
    "v_accvgpr_write_b32 a90, v34\n"
    "v_accvgpr_write_b32 a91, v35\n"
    "v_accvgpr_read_b32 v32, a96\n"
    "v_accvgpr_read_b32 v33, a97\n"
    "v_accvgpr_read_b32 v34, a98\n"
    "v_accvgpr_read_b32 v35, a99\n"
    "v_mul_f32 v32, v32, v66\n"
    "v_mul_f32 v33, v33, v66\n"
    "v_mul_f32 v34, v34, v66\n"
    "v_mul_f32 v35, v35, v66\n"
    "v_accvgpr_write_b32 a96, v32\n"
    "v_accvgpr_write_b32 a97, v33\n"
    "v_accvgpr_write_b32 a98, v34\n"
    "v_accvgpr_write_b32 a99, v35\n"
    "v_accvgpr_read_b32 v32, a104\n"
    "v_accvgpr_read_b32 v33, a105\n"
    "v_accvgpr_read_b32 v34, a106\n"
    "v_accvgpr_read_b32 v35, a107\n"
    "v_mul_f32 v32, v32, v66\n"
    "v_mul_f32 v33, v33, v66\n"
    "v_mul_f32 v34, v34, v66\n"
    "v_mul_f32 v35, v35, v66\n"
    "v_accvgpr_write_b32 a104, v32\n"
    "v_accvgpr_write_b32 a105, v33\n"
    "v_accvgpr_write_b32 a106, v34\n"
    "v_accvgpr_write_b32 a107, v35\n"
    "v_accvgpr_read_b32 v32, a112\n"
    "v_accvgpr_read_b32 v33, a113\n"
    "v_accvgpr_read_b32 v34, a114\n"
    "v_accvgpr_read_b32 v35, a115\n"
    "v_mul_f32 v32, v32, v66\n"
    "v_mul_f32 v33, v33, v66\n"
    "v_mul_f32 v34, v34, v66\n"
    "v_mul_f32 v35, v35, v66\n"
    "v_accvgpr_write_b32 a112, v32\n"
    "v_accvgpr_write_b32 a113, v33\n"
    "v_accvgpr_write_b32 a114, v34\n"
    "v_accvgpr_write_b32 a115, v35\n"
    "v_accvgpr_read_b32 v32, a120\n"
    "v_accvgpr_read_b32 v33, a121\n"
    "v_accvgpr_read_b32 v34, a122\n"
    "v_accvgpr_read_b32 v35, a123\n"
    "v_mul_f32 v32, v32, v66\n"
    "v_mul_f32 v33, v33, v66\n"
    "v_mul_f32 v34, v34, v66\n"
    "v_mul_f32 v35, v35, v66\n"
    "v_accvgpr_write_b32 a120, v32\n"
    "v_accvgpr_write_b32 a121, v33\n"
    "v_accvgpr_write_b32 a122, v34\n"
    "v_accvgpr_write_b32 a123, v35\n"
    "v_max3_f32 v69, v8, v9, v10\n"
    "v_max3_f32 v69, v69, v11, v12\n"
    "v_max3_f32 v69, v69, v13, v14\n"
    "v_max_f32 v69, v69, v15\n"
    "v_mov_b32 v70, v69\n"
    "s_nop 1\n"
    "v_permlane32_swap_b32 v69, v70\n"
    "s_nop 1\n"
    "v_max_f32 v69, v69, v70\n"
    "v_mov_b32 v70, v69\n"
    "s_nop 1\n"
    "v_permlane16_swap_b32 v69, v70\n"
    "s_nop 1\n"
    "v_max_f32 v69, v69, v70\n"
    "v_max_f32 v69, v69, v68\n"
    "v_sub_f32 v70, v68, v69\n"
    "v_exp_f32 v70, v70\n"
    "v_mov_b32 v68, v69\n"
    "v_mul_f32 v81, v81, v70\n"
    "v_sub_f32 v8, v8, v68\n"
    "v_sub_f32 v9, v9, v68\n"
    "v_sub_f32 v10, v10, v68\n"
    "v_sub_f32 v11, v11, v68\n"
    "v_sub_f32 v12, v12, v68\n"
    "v_sub_f32 v13, v13, v68\n"
    "v_sub_f32 v14, v14, v68\n"
    "v_sub_f32 v15, v15, v68\n"
    "v_accvgpr_read_b32 v32, a4\n"
    "v_accvgpr_read_b32 v33, a5\n"
    "v_accvgpr_read_b32 v34, a6\n"
    "v_accvgpr_read_b32 v35, a7\n"
    "v_mul_f32 v32, v32, v70\n"
    "v_mul_f32 v33, v33, v70\n"
    "v_mul_f32 v34, v34, v70\n"
    "v_mul_f32 v35, v35, v70\n"
    "v_accvgpr_write_b32 a4, v32\n"
    "v_accvgpr_write_b32 a5, v33\n"
    "v_accvgpr_write_b32 a6, v34\n"
    "v_accvgpr_write_b32 a7, v35\n"
    "v_accvgpr_read_b32 v32, a12\n"
    "v_accvgpr_read_b32 v33, a13\n"
    "v_accvgpr_read_b32 v34, a14\n"
    "v_accvgpr_read_b32 v35, a15\n"
    "v_mul_f32 v32, v32, v70\n"
    "v_mul_f32 v33, v33, v70\n"
    "v_mul_f32 v34, v34, v70\n"
    "v_mul_f32 v35, v35, v70\n"
    "v_accvgpr_write_b32 a12, v32\n"
    "v_accvgpr_write_b32 a13, v33\n"
    "v_accvgpr_write_b32 a14, v34\n"
    "v_accvgpr_write_b32 a15, v35\n"
    "v_accvgpr_read_b32 v32, a20\n"
    "v_accvgpr_read_b32 v33, a21\n"
    "v_accvgpr_read_b32 v34, a22\n"
    "v_accvgpr_read_b32 v35, a23\n"
    "v_mul_f32 v32, v32, v70\n"
    "v_mul_f32 v33, v33, v70\n"
    "v_mul_f32 v34, v34, v70\n"
    "v_mul_f32 v35, v35, v70\n"
    "v_accvgpr_write_b32 a20, v32\n"
    "v_accvgpr_write_b32 a21, v33\n"
    "v_accvgpr_write_b32 a22, v34\n"
    "v_accvgpr_write_b32 a23, v35\n"
    "v_accvgpr_read_b32 v32, a28\n"
    "v_accvgpr_read_b32 v33, a29\n"
    "v_accvgpr_read_b32 v34, a30\n"
    "v_accvgpr_read_b32 v35, a31\n"
    "v_mul_f32 v32, v32, v70\n"
    "v_mul_f32 v33, v33, v70\n"
    "v_mul_f32 v34, v34, v70\n"
    "v_mul_f32 v35, v35, v70\n"
    "v_accvgpr_write_b32 a28, v32\n"
    "v_accvgpr_write_b32 a29, v33\n"
    "v_accvgpr_write_b32 a30, v34\n"
    "v_accvgpr_write_b32 a31, v35\n"
    "v_accvgpr_read_b32 v32, a36\n"
    "v_accvgpr_read_b32 v33, a37\n"
    "v_accvgpr_read_b32 v34, a38\n"
    "v_accvgpr_read_b32 v35, a39\n"
    "v_mul_f32 v32, v32, v70\n"
    "v_mul_f32 v33, v33, v70\n"
    "v_mul_f32 v34, v34, v70\n"
    "v_mul_f32 v35, v35, v70\n"
    "v_accvgpr_write_b32 a36, v32\n"
    "v_accvgpr_write_b32 a37, v33\n"
    "v_accvgpr_write_b32 a38, v34\n"
    "v_accvgpr_write_b32 a39, v35\n"
    "v_accvgpr_read_b32 v32, a44\n"
    "v_accvgpr_read_b32 v33, a45\n"
    "v_accvgpr_read_b32 v34, a46\n"
    "v_accvgpr_read_b32 v35, a47\n"
    "v_mul_f32 v32, v32, v70\n"
    "v_mul_f32 v33, v33, v70\n"
    "v_mul_f32 v34, v34, v70\n"
    "v_mul_f32 v35, v35, v70\n"
    "v_accvgpr_write_b32 a44, v32\n"
    "v_accvgpr_write_b32 a45, v33\n"
    "v_accvgpr_write_b32 a46, v34\n"
    "v_accvgpr_write_b32 a47, v35\n"
    "v_accvgpr_read_b32 v32, a52\n"
    "v_accvgpr_read_b32 v33, a53\n"
    "v_accvgpr_read_b32 v34, a54\n"
    "v_accvgpr_read_b32 v35, a55\n"
    "v_mul_f32 v32, v32, v70\n"
    "v_mul_f32 v33, v33, v70\n"
    "v_mul_f32 v34, v34, v70\n"
    "v_mul_f32 v35, v35, v70\n"
    "v_accvgpr_write_b32 a52, v32\n"
    "v_accvgpr_write_b32 a53, v33\n"
    "v_accvgpr_write_b32 a54, v34\n"
    "v_accvgpr_write_b32 a55, v35\n"
    "v_accvgpr_read_b32 v32, a60\n"
    "v_accvgpr_read_b32 v33, a61\n"
    "v_accvgpr_read_b32 v34, a62\n"
    "v_accvgpr_read_b32 v35, a63\n"
    "v_mul_f32 v32, v32, v70\n"
    "v_mul_f32 v33, v33, v70\n"
    "v_mul_f32 v34, v34, v70\n"
    "v_mul_f32 v35, v35, v70\n"
    "v_accvgpr_write_b32 a60, v32\n"
    "v_accvgpr_write_b32 a61, v33\n"
    "v_accvgpr_write_b32 a62, v34\n"
    "v_accvgpr_write_b32 a63, v35\n"
    "v_accvgpr_read_b32 v32, a68\n"
    "v_accvgpr_read_b32 v33, a69\n"
    "v_accvgpr_read_b32 v34, a70\n"
    "v_accvgpr_read_b32 v35, a71\n"
    "v_mul_f32 v32, v32, v70\n"
    "v_mul_f32 v33, v33, v70\n"
    "v_mul_f32 v34, v34, v70\n"
    "v_mul_f32 v35, v35, v70\n"
    "v_accvgpr_write_b32 a68, v32\n"
    "v_accvgpr_write_b32 a69, v33\n"
    "v_accvgpr_write_b32 a70, v34\n"
    "v_accvgpr_write_b32 a71, v35\n"
    "v_accvgpr_read_b32 v32, a76\n"
    "v_accvgpr_read_b32 v33, a77\n"
    "v_accvgpr_read_b32 v34, a78\n"
    "v_accvgpr_read_b32 v35, a79\n"
    "v_mul_f32 v32, v32, v70\n"
    "v_mul_f32 v33, v33, v70\n"
    "v_mul_f32 v34, v34, v70\n"
    "v_mul_f32 v35, v35, v70\n"
    "v_accvgpr_write_b32 a76, v32\n"
    "v_accvgpr_write_b32 a77, v33\n"
    "v_accvgpr_write_b32 a78, v34\n"
    "v_accvgpr_write_b32 a79, v35\n"
    "v_accvgpr_read_b32 v32, a84\n"
    "v_accvgpr_read_b32 v33, a85\n"
    "v_accvgpr_read_b32 v34, a86\n"
    "v_accvgpr_read_b32 v35, a87\n"
    "v_mul_f32 v32, v32, v70\n"
    "v_mul_f32 v33, v33, v70\n"
    "v_mul_f32 v34, v34, v70\n"
    "v_mul_f32 v35, v35, v70\n"
    "v_accvgpr_write_b32 a84, v32\n"
    "v_accvgpr_write_b32 a85, v33\n"
    "v_accvgpr_write_b32 a86, v34\n"
    "v_accvgpr_write_b32 a87, v35\n"
    "v_accvgpr_read_b32 v32, a92\n"
    "v_accvgpr_read_b32 v33, a93\n"
    "v_accvgpr_read_b32 v34, a94\n"
    "v_accvgpr_read_b32 v35, a95\n"
    "v_mul_f32 v32, v32, v70\n"
    "v_mul_f32 v33, v33, v70\n"
    "v_mul_f32 v34, v34, v70\n"
    "v_mul_f32 v35, v35, v70\n"
    "v_accvgpr_write_b32 a92, v32\n"
    "v_accvgpr_write_b32 a93, v33\n"
    "v_accvgpr_write_b32 a94, v34\n"
    "v_accvgpr_write_b32 a95, v35\n"
    "v_accvgpr_read_b32 v32, a100\n"
    "v_accvgpr_read_b32 v33, a101\n"
    "v_accvgpr_read_b32 v34, a102\n"
    "v_accvgpr_read_b32 v35, a103\n"
    "v_mul_f32 v32, v32, v70\n"
    "v_mul_f32 v33, v33, v70\n"
    "v_mul_f32 v34, v34, v70\n"
    "v_mul_f32 v35, v35, v70\n"
    "v_accvgpr_write_b32 a100, v32\n"
    "v_accvgpr_write_b32 a101, v33\n"
    "v_accvgpr_write_b32 a102, v34\n"
    "v_accvgpr_write_b32 a103, v35\n"
    "v_accvgpr_read_b32 v32, a108\n"
    "v_accvgpr_read_b32 v33, a109\n"
    "v_accvgpr_read_b32 v34, a110\n"
    "v_accvgpr_read_b32 v35, a111\n"
    "v_mul_f32 v32, v32, v70\n"
    "v_mul_f32 v33, v33, v70\n"
    "v_mul_f32 v34, v34, v70\n"
    "v_mul_f32 v35, v35, v70\n"
    "v_accvgpr_write_b32 a108, v32\n"
    "v_accvgpr_write_b32 a109, v33\n"
    "v_accvgpr_write_b32 a110, v34\n"
    "v_accvgpr_write_b32 a111, v35\n"
    "v_accvgpr_read_b32 v32, a116\n"
    "v_accvgpr_read_b32 v33, a117\n"
    "v_accvgpr_read_b32 v34, a118\n"
    "v_accvgpr_read_b32 v35, a119\n"
    "v_mul_f32 v32, v32, v70\n"
    "v_mul_f32 v33, v33, v70\n"
    "v_mul_f32 v34, v34, v70\n"
    "v_mul_f32 v35, v35, v70\n"
    "v_accvgpr_write_b32 a116, v32\n"
    "v_accvgpr_write_b32 a117, v33\n"
    "v_accvgpr_write_b32 a118, v34\n"
    "v_accvgpr_write_b32 a119, v35\n"
    "v_accvgpr_read_b32 v32, a124\n"
    "v_accvgpr_read_b32 v33, a125\n"
    "v_accvgpr_read_b32 v34, a126\n"
    "v_accvgpr_read_b32 v35, a127\n"
    "v_mul_f32 v32, v32, v70\n"
    "v_mul_f32 v33, v33, v70\n"
    "v_mul_f32 v34, v34, v70\n"
    "v_mul_f32 v35, v35, v70\n"
    "v_accvgpr_write_b32 a124, v32\n"
    "v_accvgpr_write_b32 a125, v33\n"
    "v_accvgpr_write_b32 a126, v34\n"
    "v_accvgpr_write_b32 a127, v35\n"
    "v_max3_f32 v73, v16, v17, v18\n"
    "v_max3_f32 v73, v73, v19, v20\n"
    "v_max3_f32 v73, v73, v21, v22\n"
    "v_max_f32 v73, v73, v23\n"
    "v_mov_b32 v74, v73\n"
    "s_nop 1\n"
    "v_permlane32_swap_b32 v73, v74\n"
    "s_nop 1\n"
    "v_max_f32 v73, v73, v74\n"
    "v_mov_b32 v74, v73\n"
    "s_nop 1\n"
    "v_permlane16_swap_b32 v73, v74\n"
    "s_nop 1\n"
    "v_max_f32 v73, v73, v74\n"
    "v_max_f32 v73, v73, v72\n"
    "v_sub_f32 v74, v72, v73\n"
    "v_exp_f32 v74, v74\n"
    "v_mov_b32 v72, v73\n"
    "v_mul_f32 v82, v82, v74\n"
    "v_sub_f32 v16, v16, v72\n"
    "v_sub_f32 v17, v17, v72\n"
    "v_sub_f32 v18, v18, v72\n"
    "v_sub_f32 v19, v19, v72\n"
    "v_sub_f32 v20, v20, v72\n"
    "v_sub_f32 v21, v21, v72\n"
    "v_sub_f32 v22, v22, v72\n"
    "v_sub_f32 v23, v23, v72\n"
    "v_accvgpr_read_b32 v32, a128\n"
    "v_accvgpr_read_b32 v33, a129\n"
    "v_accvgpr_read_b32 v34, a130\n"
    "v_accvgpr_read_b32 v35, a131\n"
    "v_mul_f32 v32, v32, v74\n"
    "v_mul_f32 v33, v33, v74\n"
    "v_mul_f32 v34, v34, v74\n"
    "v_mul_f32 v35, v35, v74\n"
    "v_accvgpr_write_b32 a128, v32\n"
    "v_accvgpr_write_b32 a129, v33\n"
    "v_accvgpr_write_b32 a130, v34\n"
    "v_accvgpr_write_b32 a131, v35\n"
    "v_accvgpr_read_b32 v32, a136\n"
    "v_accvgpr_read_b32 v33, a137\n"
    "v_accvgpr_read_b32 v34, a138\n"
    "v_accvgpr_read_b32 v35, a139\n"
    "v_mul_f32 v32, v32, v74\n"
    "v_mul_f32 v33, v33, v74\n"
    "v_mul_f32 v34, v34, v74\n"
    "v_mul_f32 v35, v35, v74\n"
    "v_accvgpr_write_b32 a136, v32\n"
    "v_accvgpr_write_b32 a137, v33\n"
    "v_accvgpr_write_b32 a138, v34\n"
    "v_accvgpr_write_b32 a139, v35\n"
    "v_accvgpr_read_b32 v32, a144\n"
    "v_accvgpr_read_b32 v33, a145\n"
    "v_accvgpr_read_b32 v34, a146\n"
    "v_accvgpr_read_b32 v35, a147\n"
    "v_mul_f32 v32, v32, v74\n"
    "v_mul_f32 v33, v33, v74\n"
    "v_mul_f32 v34, v34, v74\n"
    "v_mul_f32 v35, v35, v74\n"
    "v_accvgpr_write_b32 a144, v32\n"
    "v_accvgpr_write_b32 a145, v33\n"
    "v_accvgpr_write_b32 a146, v34\n"
    "v_accvgpr_write_b32 a147, v35\n"
    "v_accvgpr_read_b32 v32, a152\n"
    "v_accvgpr_read_b32 v33, a153\n"
    "v_accvgpr_read_b32 v34, a154\n"
    "v_accvgpr_read_b32 v35, a155\n"
    "v_mul_f32 v32, v32, v74\n"
    "v_mul_f32 v33, v33, v74\n"
    "v_mul_f32 v34, v34, v74\n"
    "v_mul_f32 v35, v35, v74\n"
    "v_accvgpr_write_b32 a152, v32\n"
    "v_accvgpr_write_b32 a153, v33\n"
    "v_accvgpr_write_b32 a154, v34\n"
    "v_accvgpr_write_b32 a155, v35\n"
    "v_accvgpr_read_b32 v32, a160\n"
    "v_accvgpr_read_b32 v33, a161\n"
    "v_accvgpr_read_b32 v34, a162\n"
    "v_accvgpr_read_b32 v35, a163\n"
    "v_mul_f32 v32, v32, v74\n"
    "v_mul_f32 v33, v33, v74\n"
    "v_mul_f32 v34, v34, v74\n"
    "v_mul_f32 v35, v35, v74\n"
    "v_accvgpr_write_b32 a160, v32\n"
    "v_accvgpr_write_b32 a161, v33\n"
    "v_accvgpr_write_b32 a162, v34\n"
    "v_accvgpr_write_b32 a163, v35\n"
    "v_accvgpr_read_b32 v32, a168\n"
    "v_accvgpr_read_b32 v33, a169\n"
    "v_accvgpr_read_b32 v34, a170\n"
    "v_accvgpr_read_b32 v35, a171\n"
    "v_mul_f32 v32, v32, v74\n"
    "v_mul_f32 v33, v33, v74\n"
    "v_mul_f32 v34, v34, v74\n"
    "v_mul_f32 v35, v35, v74\n"
    "v_accvgpr_write_b32 a168, v32\n"
    "v_accvgpr_write_b32 a169, v33\n"
    "v_accvgpr_write_b32 a170, v34\n"
    "v_accvgpr_write_b32 a171, v35\n"
    "v_accvgpr_read_b32 v32, a176\n"
    "v_accvgpr_read_b32 v33, a177\n"
    "v_accvgpr_read_b32 v34, a178\n"
    "v_accvgpr_read_b32 v35, a179\n"
    "v_mul_f32 v32, v32, v74\n"
    "v_mul_f32 v33, v33, v74\n"
    "v_mul_f32 v34, v34, v74\n"
    "v_mul_f32 v35, v35, v74\n"
    "v_accvgpr_write_b32 a176, v32\n"
    "v_accvgpr_write_b32 a177, v33\n"
    "v_accvgpr_write_b32 a178, v34\n"
    "v_accvgpr_write_b32 a179, v35\n"
    "v_accvgpr_read_b32 v32, a184\n"
    "v_accvgpr_read_b32 v33, a185\n"
    "v_accvgpr_read_b32 v34, a186\n"
    "v_accvgpr_read_b32 v35, a187\n"
    "v_mul_f32 v32, v32, v74\n"
    "v_mul_f32 v33, v33, v74\n"
    "v_mul_f32 v34, v34, v74\n"
    "v_mul_f32 v35, v35, v74\n"
    "v_accvgpr_write_b32 a184, v32\n"
    "v_accvgpr_write_b32 a185, v33\n"
    "v_accvgpr_write_b32 a186, v34\n"
    "v_accvgpr_write_b32 a187, v35\n"
    "v_accvgpr_read_b32 v32, a192\n"
    "v_accvgpr_read_b32 v33, a193\n"
    "v_accvgpr_read_b32 v34, a194\n"
    "v_accvgpr_read_b32 v35, a195\n"
    "v_mul_f32 v32, v32, v74\n"
    "v_mul_f32 v33, v33, v74\n"
    "v_mul_f32 v34, v34, v74\n"
    "v_mul_f32 v35, v35, v74\n"
    "v_accvgpr_write_b32 a192, v32\n"
    "v_accvgpr_write_b32 a193, v33\n"
    "v_accvgpr_write_b32 a194, v34\n"
    "v_accvgpr_write_b32 a195, v35\n"
    "v_accvgpr_read_b32 v32, a200\n"
    "v_accvgpr_read_b32 v33, a201\n"
    "v_accvgpr_read_b32 v34, a202\n"
    "v_accvgpr_read_b32 v35, a203\n"
    "v_mul_f32 v32, v32, v74\n"
    "v_mul_f32 v33, v33, v74\n"
    "v_mul_f32 v34, v34, v74\n"
    "v_mul_f32 v35, v35, v74\n"
    "v_accvgpr_write_b32 a200, v32\n"
    "v_accvgpr_write_b32 a201, v33\n"
    "v_accvgpr_write_b32 a202, v34\n"
    "v_accvgpr_write_b32 a203, v35\n"
    "v_accvgpr_read_b32 v32, a208\n"
    "v_accvgpr_read_b32 v33, a209\n"
    "v_accvgpr_read_b32 v34, a210\n"
    "v_accvgpr_read_b32 v35, a211\n"
    "v_mul_f32 v32, v32, v74\n"
    "v_mul_f32 v33, v33, v74\n"
    "v_mul_f32 v34, v34, v74\n"
    "v_mul_f32 v35, v35, v74\n"
    "v_accvgpr_write_b32 a208, v32\n"
    "v_accvgpr_write_b32 a209, v33\n"
    "v_accvgpr_write_b32 a210, v34\n"
    "v_accvgpr_write_b32 a211, v35\n"
    "v_accvgpr_read_b32 v32, a216\n"
    "v_accvgpr_read_b32 v33, a217\n"
    "v_accvgpr_read_b32 v34, a218\n"
    "v_accvgpr_read_b32 v35, a219\n"
    "v_mul_f32 v32, v32, v74\n"
    "v_mul_f32 v33, v33, v74\n"
    "v_mul_f32 v34, v34, v74\n"
    "v_mul_f32 v35, v35, v74\n"
    "v_accvgpr_write_b32 a216, v32\n"
    "v_accvgpr_write_b32 a217, v33\n"
    "v_accvgpr_write_b32 a218, v34\n"
    "v_accvgpr_write_b32 a219, v35\n"
    "v_accvgpr_read_b32 v32, a224\n"
    "v_accvgpr_read_b32 v33, a225\n"
    "v_accvgpr_read_b32 v34, a226\n"
    "v_accvgpr_read_b32 v35, a227\n"
    "v_mul_f32 v32, v32, v74\n"
    "v_mul_f32 v33, v33, v74\n"
    "v_mul_f32 v34, v34, v74\n"
    "v_mul_f32 v35, v35, v74\n"
    "v_accvgpr_write_b32 a224, v32\n"
    "v_accvgpr_write_b32 a225, v33\n"
    "v_accvgpr_write_b32 a226, v34\n"
    "v_accvgpr_write_b32 a227, v35\n"
    "v_accvgpr_read_b32 v32, a232\n"
    "v_accvgpr_read_b32 v33, a233\n"
    "v_accvgpr_read_b32 v34, a234\n"
    "v_accvgpr_read_b32 v35, a235\n"
    "v_mul_f32 v32, v32, v74\n"
    "v_mul_f32 v33, v33, v74\n"
    "v_mul_f32 v34, v34, v74\n"
    "v_mul_f32 v35, v35, v74\n"
    "v_accvgpr_write_b32 a232, v32\n"
    "v_accvgpr_write_b32 a233, v33\n"
    "v_accvgpr_write_b32 a234, v34\n"
    "v_accvgpr_write_b32 a235, v35\n"
    "v_accvgpr_read_b32 v32, a240\n"
    "v_accvgpr_read_b32 v33, a241\n"
    "v_accvgpr_read_b32 v34, a242\n"
    "v_accvgpr_read_b32 v35, a243\n"
    "v_mul_f32 v32, v32, v74\n"
    "v_mul_f32 v33, v33, v74\n"
    "v_mul_f32 v34, v34, v74\n"
    "v_mul_f32 v35, v35, v74\n"
    "v_accvgpr_write_b32 a240, v32\n"
    "v_accvgpr_write_b32 a241, v33\n"
    "v_accvgpr_write_b32 a242, v34\n"
    "v_accvgpr_write_b32 a243, v35\n"
    "v_accvgpr_read_b32 v32, a248\n"
    "v_accvgpr_read_b32 v33, a249\n"
    "v_accvgpr_read_b32 v34, a250\n"
    "v_accvgpr_read_b32 v35, a251\n"
    "v_mul_f32 v32, v32, v74\n"
    "v_mul_f32 v33, v33, v74\n"
    "v_mul_f32 v34, v34, v74\n"
    "v_mul_f32 v35, v35, v74\n"
    "v_accvgpr_write_b32 a248, v32\n"
    "v_accvgpr_write_b32 a249, v33\n"
    "v_accvgpr_write_b32 a250, v34\n"
    "v_accvgpr_write_b32 a251, v35\n"
    "v_max3_f32 v77, v24, v25, v26\n"
    "v_max3_f32 v77, v77, v27, v28\n"
    "v_max3_f32 v77, v77, v29, v30\n"
    "v_max_f32 v77, v77, v31\n"
    "v_mov_b32 v78, v77\n"
    "s_nop 1\n"
    "v_permlane32_swap_b32 v77, v78\n"
    "s_nop 1\n"
    "v_max_f32 v77, v77, v78\n"
    "v_mov_b32 v78, v77\n"
    "s_nop 1\n"
    "v_permlane16_swap_b32 v77, v78\n"
    "s_nop 1\n"
    "v_max_f32 v77, v77, v78\n"
    "v_max_f32 v77, v77, v76\n"
    "v_sub_f32 v78, v76, v77\n"
    "v_exp_f32 v78, v78\n"
    "v_mov_b32 v76, v77\n"
    "v_mul_f32 v83, v83, v78\n"
    "v_sub_f32 v24, v24, v76\n"
    "v_sub_f32 v25, v25, v76\n"
    "v_sub_f32 v26, v26, v76\n"
    "v_sub_f32 v27, v27, v76\n"
    "v_sub_f32 v28, v28, v76\n"
    "v_sub_f32 v29, v29, v76\n"
    "v_sub_f32 v30, v30, v76\n"
    "v_sub_f32 v31, v31, v76\n"
    "v_accvgpr_read_b32 v32, a132\n"
    "v_accvgpr_read_b32 v33, a133\n"
    "v_accvgpr_read_b32 v34, a134\n"
    "v_accvgpr_read_b32 v35, a135\n"
    "v_mul_f32 v32, v32, v78\n"
    "v_mul_f32 v33, v33, v78\n"
    "v_mul_f32 v34, v34, v78\n"
    "v_mul_f32 v35, v35, v78\n"
    "v_accvgpr_write_b32 a132, v32\n"
    "v_accvgpr_write_b32 a133, v33\n"
    "v_accvgpr_write_b32 a134, v34\n"
    "v_accvgpr_write_b32 a135, v35\n"
    "v_accvgpr_read_b32 v32, a140\n"
    "v_accvgpr_read_b32 v33, a141\n"
    "v_accvgpr_read_b32 v34, a142\n"
    "v_accvgpr_read_b32 v35, a143\n"
    "v_mul_f32 v32, v32, v78\n"
    "v_mul_f32 v33, v33, v78\n"
    "v_mul_f32 v34, v34, v78\n"
    "v_mul_f32 v35, v35, v78\n"
    "v_accvgpr_write_b32 a140, v32\n"
    "v_accvgpr_write_b32 a141, v33\n"
    "v_accvgpr_write_b32 a142, v34\n"
    "v_accvgpr_write_b32 a143, v35\n"
    "v_accvgpr_read_b32 v32, a148\n"
    "v_accvgpr_read_b32 v33, a149\n"
    "v_accvgpr_read_b32 v34, a150\n"
    "v_accvgpr_read_b32 v35, a151\n"
    "v_mul_f32 v32, v32, v78\n"
    "v_mul_f32 v33, v33, v78\n"
    "v_mul_f32 v34, v34, v78\n"
    "v_mul_f32 v35, v35, v78\n"
    "v_accvgpr_write_b32 a148, v32\n"
    "v_accvgpr_write_b32 a149, v33\n"
    "v_accvgpr_write_b32 a150, v34\n"
    "v_accvgpr_write_b32 a151, v35\n"
    "v_accvgpr_read_b32 v32, a156\n"
    "v_accvgpr_read_b32 v33, a157\n"
    "v_accvgpr_read_b32 v34, a158\n"
    "v_accvgpr_read_b32 v35, a159\n"
    "v_mul_f32 v32, v32, v78\n"
    "v_mul_f32 v33, v33, v78\n"
    "v_mul_f32 v34, v34, v78\n"
    "v_mul_f32 v35, v35, v78\n"
    "v_accvgpr_write_b32 a156, v32\n"
    "v_accvgpr_write_b32 a157, v33\n"
    "v_accvgpr_write_b32 a158, v34\n"
    "v_accvgpr_write_b32 a159, v35\n"
    "v_accvgpr_read_b32 v32, a164\n"
    "v_accvgpr_read_b32 v33, a165\n"
    "v_accvgpr_read_b32 v34, a166\n"
    "v_accvgpr_read_b32 v35, a167\n"
    "v_mul_f32 v32, v32, v78\n"
    "v_mul_f32 v33, v33, v78\n"
    "v_mul_f32 v34, v34, v78\n"
    "v_mul_f32 v35, v35, v78\n"
    "v_accvgpr_write_b32 a164, v32\n"
    "v_accvgpr_write_b32 a165, v33\n"
    "v_accvgpr_write_b32 a166, v34\n"
    "v_accvgpr_write_b32 a167, v35\n"
    "v_accvgpr_read_b32 v32, a172\n"
    "v_accvgpr_read_b32 v33, a173\n"
    "v_accvgpr_read_b32 v34, a174\n"
    "v_accvgpr_read_b32 v35, a175\n"
    "v_mul_f32 v32, v32, v78\n"
    "v_mul_f32 v33, v33, v78\n"
    "v_mul_f32 v34, v34, v78\n"
    "v_mul_f32 v35, v35, v78\n"
    "v_accvgpr_write_b32 a172, v32\n"
    "v_accvgpr_write_b32 a173, v33\n"
    "v_accvgpr_write_b32 a174, v34\n"
    "v_accvgpr_write_b32 a175, v35\n"
    "v_accvgpr_read_b32 v32, a180\n"
    "v_accvgpr_read_b32 v33, a181\n"
    "v_accvgpr_read_b32 v34, a182\n"
    "v_accvgpr_read_b32 v35, a183\n"
    "v_mul_f32 v32, v32, v78\n"
    "v_mul_f32 v33, v33, v78\n"
    "v_mul_f32 v34, v34, v78\n"
    "v_mul_f32 v35, v35, v78\n"
    "v_accvgpr_write_b32 a180, v32\n"
    "v_accvgpr_write_b32 a181, v33\n"
    "v_accvgpr_write_b32 a182, v34\n"
    "v_accvgpr_write_b32 a183, v35\n"
    "v_accvgpr_read_b32 v32, a188\n"
    "v_accvgpr_read_b32 v33, a189\n"
    "v_accvgpr_read_b32 v34, a190\n"
    "v_accvgpr_read_b32 v35, a191\n"
    "v_mul_f32 v32, v32, v78\n"
    "v_mul_f32 v33, v33, v78\n"
    "v_mul_f32 v34, v34, v78\n"
    "v_mul_f32 v35, v35, v78\n"
    "v_accvgpr_write_b32 a188, v32\n"
    "v_accvgpr_write_b32 a189, v33\n"
    "v_accvgpr_write_b32 a190, v34\n"
    "v_accvgpr_write_b32 a191, v35\n"
    "v_accvgpr_read_b32 v32, a196\n"
    "v_accvgpr_read_b32 v33, a197\n"
    "v_accvgpr_read_b32 v34, a198\n"
    "v_accvgpr_read_b32 v35, a199\n"
    "v_mul_f32 v32, v32, v78\n"
    "v_mul_f32 v33, v33, v78\n"
    "v_mul_f32 v34, v34, v78\n"
    "v_mul_f32 v35, v35, v78\n"
    "v_accvgpr_write_b32 a196, v32\n"
    "v_accvgpr_write_b32 a197, v33\n"
    "v_accvgpr_write_b32 a198, v34\n"
    "v_accvgpr_write_b32 a199, v35\n"
    "v_accvgpr_read_b32 v32, a204\n"
    "v_accvgpr_read_b32 v33, a205\n"
    "v_accvgpr_read_b32 v34, a206\n"
    "v_accvgpr_read_b32 v35, a207\n"
    "v_mul_f32 v32, v32, v78\n"
    "v_mul_f32 v33, v33, v78\n"
    "v_mul_f32 v34, v34, v78\n"
    "v_mul_f32 v35, v35, v78\n"
    "v_accvgpr_write_b32 a204, v32\n"
    "v_accvgpr_write_b32 a205, v33\n"
    "v_accvgpr_write_b32 a206, v34\n"
    "v_accvgpr_write_b32 a207, v35\n"
    "v_accvgpr_read_b32 v32, a212\n"
    "v_accvgpr_read_b32 v33, a213\n"
    "v_accvgpr_read_b32 v34, a214\n"
    "v_accvgpr_read_b32 v35, a215\n"
    "v_mul_f32 v32, v32, v78\n"
    "v_mul_f32 v33, v33, v78\n"
    "v_mul_f32 v34, v34, v78\n"
    "v_mul_f32 v35, v35, v78\n"
    "v_accvgpr_write_b32 a212, v32\n"
    "v_accvgpr_write_b32 a213, v33\n"
    "v_accvgpr_write_b32 a214, v34\n"
    "v_accvgpr_write_b32 a215, v35\n"
    "v_accvgpr_read_b32 v32, a220\n"
    "v_accvgpr_read_b32 v33, a221\n"
    "v_accvgpr_read_b32 v34, a222\n"
    "v_accvgpr_read_b32 v35, a223\n"
    "v_mul_f32 v32, v32, v78\n"
    "v_mul_f32 v33, v33, v78\n"
    "v_mul_f32 v34, v34, v78\n"
    "v_mul_f32 v35, v35, v78\n"
    "v_accvgpr_write_b32 a220, v32\n"
    "v_accvgpr_write_b32 a221, v33\n"
    "v_accvgpr_write_b32 a222, v34\n"
    "v_accvgpr_write_b32 a223, v35\n"
    "v_accvgpr_read_b32 v32, a228\n"
    "v_accvgpr_read_b32 v33, a229\n"
    "v_accvgpr_read_b32 v34, a230\n"
    "v_accvgpr_read_b32 v35, a231\n"
    "v_mul_f32 v32, v32, v78\n"
    "v_mul_f32 v33, v33, v78\n"
    "v_mul_f32 v34, v34, v78\n"
    "v_mul_f32 v35, v35, v78\n"
    "v_accvgpr_write_b32 a228, v32\n"
    "v_accvgpr_write_b32 a229, v33\n"
    "v_accvgpr_write_b32 a230, v34\n"
    "v_accvgpr_write_b32 a231, v35\n"
    "v_accvgpr_read_b32 v32, a236\n"
    "v_accvgpr_read_b32 v33, a237\n"
    "v_accvgpr_read_b32 v34, a238\n"
    "v_accvgpr_read_b32 v35, a239\n"
    "v_mul_f32 v32, v32, v78\n"
    "v_mul_f32 v33, v33, v78\n"
    "v_mul_f32 v34, v34, v78\n"
    "v_mul_f32 v35, v35, v78\n"
    "v_accvgpr_write_b32 a236, v32\n"
    "v_accvgpr_write_b32 a237, v33\n"
    "v_accvgpr_write_b32 a238, v34\n"
    "v_accvgpr_write_b32 a239, v35\n"
    "v_accvgpr_read_b32 v32, a244\n"
    "v_accvgpr_read_b32 v33, a245\n"
    "v_accvgpr_read_b32 v34, a246\n"
    "v_accvgpr_read_b32 v35, a247\n"
    "v_mul_f32 v32, v32, v78\n"
    "v_mul_f32 v33, v33, v78\n"
    "v_mul_f32 v34, v34, v78\n"
    "v_mul_f32 v35, v35, v78\n"
    "v_accvgpr_write_b32 a244, v32\n"
    "v_accvgpr_write_b32 a245, v33\n"
    "v_accvgpr_write_b32 a246, v34\n"
    "v_accvgpr_write_b32 a247, v35\n"
    "v_accvgpr_read_b32 v32, a252\n"
    "v_accvgpr_read_b32 v33, a253\n"
    "v_accvgpr_read_b32 v34, a254\n"
    "v_accvgpr_read_b32 v35, a255\n"
    "v_mul_f32 v32, v32, v78\n"
    "v_mul_f32 v33, v33, v78\n"
    "v_mul_f32 v34, v34, v78\n"
    "v_mul_f32 v35, v35, v78\n"
    "v_accvgpr_write_b32 a252, v32\n"
    "v_accvgpr_write_b32 a253, v33\n"
    "v_accvgpr_write_b32 a254, v34\n"
    "v_accvgpr_write_b32 a255, v35\n"
    "v_exp_f32 v0, v0\n"
    "v_exp_f32 v1, v1\n"
    "s_nop 0\n"
    "v_add_f32 v80, v80, v0\n"
    "v_add_f32 v80, v80, v1\n"
    "v_cvt_pk_bf16_f32 v0, v0, v1\n"
    "v_exp_f32 v8, v8\n"
    "v_exp_f32 v9, v9\n"
    "s_nop 0\n"
    "v_add_f32 v81, v81, v8\n"
    "v_add_f32 v81, v81, v9\n"
    "v_cvt_pk_bf16_f32 v8, v8, v9\n"
    "v_exp_f32 v16, v16\n"
    "v_exp_f32 v17, v17\n"
    "s_nop 0\n"
    "v_add_f32 v82, v82, v16\n"
    "v_add_f32 v82, v82, v17\n"
    "v_cvt_pk_bf16_f32 v16, v16, v17\n"
    "v_exp_f32 v24, v24\n"
    "v_exp_f32 v25, v25\n"
    "s_nop 0\n"
    "v_add_f32 v83, v83, v24\n"
    "v_add_f32 v83, v83, v25\n"
    "v_cvt_pk_bf16_f32 v24, v24, v25\n"
    "v_exp_f32 v2, v2\n"
    "v_exp_f32 v3, v3\n"
    "s_nop 0\n"
    "v_add_f32 v80, v80, v2\n"
    "v_add_f32 v80, v80, v3\n"
    "v_cvt_pk_bf16_f32 v1, v2, v3\n"
    "v_exp_f32 v10, v10\n"
    "v_exp_f32 v11, v11\n"
    "s_nop 0\n"
    "v_add_f32 v81, v81, v10\n"
    "v_add_f32 v81, v81, v11\n"
    "v_cvt_pk_bf16_f32 v9, v10, v11\n"
    "v_exp_f32 v18, v18\n"
    "v_exp_f32 v19, v19\n"
    "s_nop 0\n"
    "v_add_f32 v82, v82, v18\n"
    "v_add_f32 v82, v82, v19\n"
    "v_cvt_pk_bf16_f32 v17, v18, v19\n"
    "v_exp_f32 v26, v26\n"
    "v_exp_f32 v27, v27\n"
    "s_nop 0\n"
    "v_add_f32 v83, v83, v26\n"
    "v_add_f32 v83, v83, v27\n"
    "v_cvt_pk_bf16_f32 v25, v26, v27\n"
    "v_exp_f32 v4, v4\n"
    "v_exp_f32 v5, v5\n"
    "s_nop 0\n"
    "v_add_f32 v80, v80, v4\n"
    "v_add_f32 v80, v80, v5\n"
    "v_cvt_pk_bf16_f32 v2, v4, v5\n"
    "v_exp_f32 v12, v12\n"
    "v_exp_f32 v13, v13\n"
    "s_nop 0\n"
    "v_add_f32 v81, v81, v12\n"
    "v_add_f32 v81, v81, v13\n"
    "v_cvt_pk_bf16_f32 v10, v12, v13\n"
    "v_exp_f32 v20, v20\n"
    "v_exp_f32 v21, v21\n"
    "s_nop 0\n"
    "v_add_f32 v82, v82, v20\n"
    "v_add_f32 v82, v82, v21\n"
    "v_cvt_pk_bf16_f32 v18, v20, v21\n"
    "v_exp_f32 v28, v28\n"
    "v_exp_f32 v29, v29\n"
    "s_nop 0\n"
    "v_add_f32 v83, v83, v28\n"
    "v_add_f32 v83, v83, v29\n"
    "v_cvt_pk_bf16_f32 v26, v28, v29\n"
    "v_exp_f32 v6, v6\n"
    "v_exp_f32 v7, v7\n"
    "s_nop 0\n"
    "v_add_f32 v80, v80, v6\n"
    "v_add_f32 v80, v80, v7\n"
    "v_cvt_pk_bf16_f32 v3, v6, v7\n"
    "v_exp_f32 v14, v14\n"
    "v_exp_f32 v15, v15\n"
    "s_nop 0\n"
    "v_add_f32 v81, v81, v14\n"
    "v_add_f32 v81, v81, v15\n"
    "v_cvt_pk_bf16_f32 v11, v14, v15\n"
    "v_exp_f32 v22, v22\n"
    "v_exp_f32 v23, v23\n"
    "s_nop 0\n"
    "v_add_f32 v82, v82, v22\n"
    "v_add_f32 v82, v82, v23\n"
    "v_cvt_pk_bf16_f32 v19, v22, v23\n"
    "v_exp_f32 v30, v30\n"
    "v_exp_f32 v31, v31\n"
    "s_nop 0\n"
    "v_add_f32 v83, v83, v30\n"
    "v_add_f32 v83, v83, v31\n"
    "v_cvt_pk_bf16_f32 v27, v30, v31\n"
    "s_nop 1\n"
    "ds_read_b64_tr_b16 v[84:85], v125 offset:0\n"
    "ds_read_b64_tr_b16 v[86:87], v125 offset:16896\n"
    "s_waitcnt lgkmcnt(0)\n"
    "v_mfma_f32_16x16x32_bf16 a[0:3], v[84:87], v[0:3], a[0:3]\n"
    "v_mfma_f32_16x16x32_bf16 a[4:7], v[84:87], v[8:11], a[4:7]\n"
    "v_mfma_f32_16x16x32_bf16 a[128:131], v[84:87], v[16:19], a[128:131]\n"
    "v_mfma_f32_16x16x32_bf16 a[132:135], v[84:87], v[24:27], a[132:135]\n"
    "ds_read_b64_tr_b16 v[88:89], v125 offset:32\n"
    "ds_read_b64_tr_b16 v[90:91], v125 offset:16928\n"
    "s_waitcnt lgkmcnt(0)\n"
    "v_mfma_f32_16x16x32_bf16 a[8:11], v[88:91], v[0:3], a[8:11]\n"
    "v_mfma_f32_16x16x32_bf16 a[12:15], v[88:91], v[8:11], a[12:15]\n"
    "v_mfma_f32_16x16x32_bf16 a[136:139], v[88:91], v[16:19], a[136:139]\n"
    "v_mfma_f32_16x16x32_bf16 a[140:143], v[88:91], v[24:27], a[140:143]\n"
    "ds_read_b64_tr_b16 v[92:93], v125 offset:64\n"
    "ds_read_b64_tr_b16 v[94:95], v125 offset:16960\n"
    "s_waitcnt lgkmcnt(0)\n"
    "v_mfma_f32_16x16x32_bf16 a[16:19], v[92:95], v[0:3], a[16:19]\n"
    "v_mfma_f32_16x16x32_bf16 a[20:23], v[92:95], v[8:11], a[20:23]\n"
    "v_mfma_f32_16x16x32_bf16 a[144:147], v[92:95], v[16:19], a[144:147]\n"
    "v_mfma_f32_16x16x32_bf16 a[148:151], v[92:95], v[24:27], a[148:151]\n"
    "ds_read_b64_tr_b16 v[96:97], v125 offset:96\n"
    "ds_read_b64_tr_b16 v[98:99], v125 offset:16992\n"
    "s_waitcnt lgkmcnt(0)\n"
    "v_mfma_f32_16x16x32_bf16 a[24:27], v[96:99], v[0:3], a[24:27]\n"
    "v_mfma_f32_16x16x32_bf16 a[28:31], v[96:99], v[8:11], a[28:31]\n"
    "v_mfma_f32_16x16x32_bf16 a[152:155], v[96:99], v[16:19], a[152:155]\n"
    "v_mfma_f32_16x16x32_bf16 a[156:159], v[96:99], v[24:27], a[156:159]\n"
    "ds_read_b64_tr_b16 v[100:101], v125 offset:128\n"
    "ds_read_b64_tr_b16 v[102:103], v125 offset:17024\n"
    "s_waitcnt lgkmcnt(0)\n"
    "v_mfma_f32_16x16x32_bf16 a[32:35], v[100:103], v[0:3], a[32:35]\n"
    "v_mfma_f32_16x16x32_bf16 a[36:39], v[100:103], v[8:11], a[36:39]\n"
    "v_mfma_f32_16x16x32_bf16 a[160:163], v[100:103], v[16:19], a[160:163]\n"
    "v_mfma_f32_16x16x32_bf16 a[164:167], v[100:103], v[24:27], a[164:167]\n"
    "ds_read_b64_tr_b16 v[104:105], v125 offset:160\n"
    "ds_read_b64_tr_b16 v[106:107], v125 offset:17056\n"
    "s_waitcnt lgkmcnt(0)\n"
    "v_mfma_f32_16x16x32_bf16 a[40:43], v[104:107], v[0:3], a[40:43]\n"
    "v_mfma_f32_16x16x32_bf16 a[44:47], v[104:107], v[8:11], a[44:47]\n"
    "v_mfma_f32_16x16x32_bf16 a[168:171], v[104:107], v[16:19], a[168:171]\n"
    "v_mfma_f32_16x16x32_bf16 a[172:175], v[104:107], v[24:27], a[172:175]\n"
    "ds_read_b64_tr_b16 v[108:109], v125 offset:192\n"
    "ds_read_b64_tr_b16 v[110:111], v125 offset:17088\n"
    "s_waitcnt lgkmcnt(0)\n"
    "v_mfma_f32_16x16x32_bf16 a[48:51], v[108:111], v[0:3], a[48:51]\n"
    "v_mfma_f32_16x16x32_bf16 a[52:55], v[108:111], v[8:11], a[52:55]\n"
    "v_mfma_f32_16x16x32_bf16 a[176:179], v[108:111], v[16:19], a[176:179]\n"
    "v_mfma_f32_16x16x32_bf16 a[180:183], v[108:111], v[24:27], a[180:183]\n"
    "ds_read_b64_tr_b16 v[112:113], v125 offset:224\n"
    "ds_read_b64_tr_b16 v[114:115], v125 offset:17120\n"
    "s_waitcnt lgkmcnt(0)\n"
    "v_mfma_f32_16x16x32_bf16 a[56:59], v[112:115], v[0:3], a[56:59]\n"
    "v_mfma_f32_16x16x32_bf16 a[60:63], v[112:115], v[8:11], a[60:63]\n"
    "v_mfma_f32_16x16x32_bf16 a[184:187], v[112:115], v[16:19], a[184:187]\n"
    "v_mfma_f32_16x16x32_bf16 a[188:191], v[112:115], v[24:27], a[188:191]\n"
    "ds_read_b64_tr_b16 v[84:85], v125 offset:256\n"
    "ds_read_b64_tr_b16 v[86:87], v125 offset:17152\n"
    "s_waitcnt lgkmcnt(0)\n"
    "v_mfma_f32_16x16x32_bf16 a[64:67], v[84:87], v[0:3], a[64:67]\n"
    "v_mfma_f32_16x16x32_bf16 a[68:71], v[84:87], v[8:11], a[68:71]\n"
    "v_mfma_f32_16x16x32_bf16 a[192:195], v[84:87], v[16:19], a[192:195]\n"
    "v_mfma_f32_16x16x32_bf16 a[196:199], v[84:87], v[24:27], a[196:199]\n"
    "ds_read_b64_tr_b16 v[88:89], v125 offset:288\n"
    "ds_read_b64_tr_b16 v[90:91], v125 offset:17184\n"
    "s_waitcnt lgkmcnt(0)\n"
    "v_mfma_f32_16x16x32_bf16 a[72:75], v[88:91], v[0:3], a[72:75]\n"
    "v_mfma_f32_16x16x32_bf16 a[76:79], v[88:91], v[8:11], a[76:79]\n"
    "v_mfma_f32_16x16x32_bf16 a[200:203], v[88:91], v[16:19], a[200:203]\n"
    "v_mfma_f32_16x16x32_bf16 a[204:207], v[88:91], v[24:27], a[204:207]\n"
    "ds_read_b64_tr_b16 v[92:93], v125 offset:320\n"
    "ds_read_b64_tr_b16 v[94:95], v125 offset:17216\n"
    "s_waitcnt lgkmcnt(0)\n"
    "v_mfma_f32_16x16x32_bf16 a[80:83], v[92:95], v[0:3], a[80:83]\n"
    "v_mfma_f32_16x16x32_bf16 a[84:87], v[92:95], v[8:11], a[84:87]\n"
    "v_mfma_f32_16x16x32_bf16 a[208:211], v[92:95], v[16:19], a[208:211]\n"
    "v_mfma_f32_16x16x32_bf16 a[212:215], v[92:95], v[24:27], a[212:215]\n"
    "ds_read_b64_tr_b16 v[96:97], v125 offset:352\n"
    "ds_read_b64_tr_b16 v[98:99], v125 offset:17248\n"
    "s_waitcnt lgkmcnt(0)\n"
    "v_mfma_f32_16x16x32_bf16 a[88:91], v[96:99], v[0:3], a[88:91]\n"
    "v_mfma_f32_16x16x32_bf16 a[92:95], v[96:99], v[8:11], a[92:95]\n"
    "v_mfma_f32_16x16x32_bf16 a[216:219], v[96:99], v[16:19], a[216:219]\n"
    "v_mfma_f32_16x16x32_bf16 a[220:223], v[96:99], v[24:27], a[220:223]\n"
    "ds_read_b64_tr_b16 v[100:101], v125 offset:384\n"
    "ds_read_b64_tr_b16 v[102:103], v125 offset:17280\n"
    "s_waitcnt lgkmcnt(0)\n"
    "v_mfma_f32_16x16x32_bf16 a[96:99], v[100:103], v[0:3], a[96:99]\n"
    "v_mfma_f32_16x16x32_bf16 a[100:103], v[100:103], v[8:11], a[100:103]\n"
    "v_mfma_f32_16x16x32_bf16 a[224:227], v[100:103], v[16:19], a[224:227]\n"
    "v_mfma_f32_16x16x32_bf16 a[228:231], v[100:103], v[24:27], a[228:231]\n"
    "ds_read_b64_tr_b16 v[104:105], v125 offset:416\n"
    "ds_read_b64_tr_b16 v[106:107], v125 offset:17312\n"
    "s_waitcnt lgkmcnt(0)\n"
    "v_mfma_f32_16x16x32_bf16 a[104:107], v[104:107], v[0:3], a[104:107]\n"
    "v_mfma_f32_16x16x32_bf16 a[108:111], v[104:107], v[8:11], a[108:111]\n"
    "v_mfma_f32_16x16x32_bf16 a[232:235], v[104:107], v[16:19], a[232:235]\n"
    "v_mfma_f32_16x16x32_bf16 a[236:239], v[104:107], v[24:27], a[236:239]\n"
    "ds_read_b64_tr_b16 v[108:109], v125 offset:448\n"
    "ds_read_b64_tr_b16 v[110:111], v125 offset:17344\n"
    "s_waitcnt lgkmcnt(0)\n"
    "v_mfma_f32_16x16x32_bf16 a[112:115], v[108:111], v[0:3], a[112:115]\n"
    "v_mfma_f32_16x16x32_bf16 a[116:119], v[108:111], v[8:11], a[116:119]\n"
    "v_mfma_f32_16x16x32_bf16 a[240:243], v[108:111], v[16:19], a[240:243]\n"
    "v_mfma_f32_16x16x32_bf16 a[244:247], v[108:111], v[24:27], a[244:247]\n"
    "ds_read_b64_tr_b16 v[112:113], v125 offset:480\n"
    "ds_read_b64_tr_b16 v[114:115], v125 offset:17376\n"
    "s_waitcnt lgkmcnt(0)\n"
    "v_mfma_f32_16x16x32_bf16 a[120:123], v[112:115], v[0:3], a[120:123]\n"
    "v_mfma_f32_16x16x32_bf16 a[124:127], v[112:115], v[8:11], a[124:127]\n"
    "v_mfma_f32_16x16x32_bf16 a[248:251], v[112:115], v[16:19], a[248:251]\n"
    "v_mfma_f32_16x16x32_bf16 a[252:255], v[112:115], v[24:27], a[252:255]\n"
    "s_sub_u32 s85, s85, 1\n"
    "s_cmp_lg_u32 s85, 0\n"
    "s_waitcnt lgkmcnt(0)\n"
    "s_cbranch_scc1 .Lsloop\n"
    "s_nop 15\n"
    "s_barrier\n"
    "v_mbcnt_lo_u32_b32 v127, -1, 0\n"
    "v_mbcnt_hi_u32_b32 v127, -1, v127\n"
    "v_and_b32 v127, 15, v127\n"
    "s_lshl_b32 s86, s69, 8\n"
    "s_add_u32 s86, s86, 135168\n"
    "v_lshl_add_u32 v120, v127, 2, s86\n"
    "ds_write_b32 v120, v64 offset:0\n"
    "ds_write_b32 v120, v68 offset:64\n"
    "ds_write_b32 v120, v72 offset:128\n"
    "ds_write_b32 v120, v76 offset:192\n"
    "s_waitcnt lgkmcnt(0)\n"
    "s_barrier\n"
    "s_xor_b32 s86, s69, 2\n"
    "s_lshl_b32 s86, s86, 8\n"
    "s_add_u32 s86, s86, 135168\n"
    "v_lshl_add_u32 v120, v127, 2, s86\n"
    "ds_read_b32 v65, v120 offset:128\n"
    "ds_read_b32 v69, v120 offset:192\n"
    "ds_read_b32 v73, v120 offset:0\n"
    "ds_read_b32 v77, v120 offset:64\n"
    "s_waitcnt lgkmcnt(3)\n"
    "v_max_f32 v65, v65, v64\n"
    "v_sub_f32 v66, v64, v65\n"
    "v_exp_f32 v66, v66\n"
    "s_nop 0\n"
    "v_mul_f32 v80, v80, v66\n"
    "v_accvgpr_read_b32 v32, a0\n"
    "v_accvgpr_read_b32 v33, a1\n"
    "v_accvgpr_read_b32 v34, a2\n"
    "v_accvgpr_read_b32 v35, a3\n"
    "v_mul_f32 v32, v32, v66\n"
    "v_mul_f32 v33, v33, v66\n"
    "v_mul_f32 v34, v34, v66\n"
    "v_mul_f32 v35, v35, v66\n"
    "v_accvgpr_write_b32 a0, v32\n"
    "v_accvgpr_write_b32 a1, v33\n"
    "v_accvgpr_write_b32 a2, v34\n"
    "v_accvgpr_write_b32 a3, v35\n"
    "v_accvgpr_read_b32 v32, a8\n"
    "v_accvgpr_read_b32 v33, a9\n"
    "v_accvgpr_read_b32 v34, a10\n"
    "v_accvgpr_read_b32 v35, a11\n"
    "v_mul_f32 v32, v32, v66\n"
    "v_mul_f32 v33, v33, v66\n"
    "v_mul_f32 v34, v34, v66\n"
    "v_mul_f32 v35, v35, v66\n"
    "v_accvgpr_write_b32 a8, v32\n"
    "v_accvgpr_write_b32 a9, v33\n"
    "v_accvgpr_write_b32 a10, v34\n"
    "v_accvgpr_write_b32 a11, v35\n"
    "v_accvgpr_read_b32 v32, a16\n"
    "v_accvgpr_read_b32 v33, a17\n"
    "v_accvgpr_read_b32 v34, a18\n"
    "v_accvgpr_read_b32 v35, a19\n"
    "v_mul_f32 v32, v32, v66\n"
    "v_mul_f32 v33, v33, v66\n"
    "v_mul_f32 v34, v34, v66\n"
    "v_mul_f32 v35, v35, v66\n"
    "v_accvgpr_write_b32 a16, v32\n"
    "v_accvgpr_write_b32 a17, v33\n"
    "v_accvgpr_write_b32 a18, v34\n"
    "v_accvgpr_write_b32 a19, v35\n"
    "v_accvgpr_read_b32 v32, a24\n"
    "v_accvgpr_read_b32 v33, a25\n"
    "v_accvgpr_read_b32 v34, a26\n"
    "v_accvgpr_read_b32 v35, a27\n"
    "v_mul_f32 v32, v32, v66\n"
    "v_mul_f32 v33, v33, v66\n"
    "v_mul_f32 v34, v34, v66\n"
    "v_mul_f32 v35, v35, v66\n"
    "v_accvgpr_write_b32 a24, v32\n"
    "v_accvgpr_write_b32 a25, v33\n"
    "v_accvgpr_write_b32 a26, v34\n"
    "v_accvgpr_write_b32 a27, v35\n"
    "v_accvgpr_read_b32 v32, a32\n"
    "v_accvgpr_read_b32 v33, a33\n"
    "v_accvgpr_read_b32 v34, a34\n"
    "v_accvgpr_read_b32 v35, a35\n"
    "v_mul_f32 v32, v32, v66\n"
    "v_mul_f32 v33, v33, v66\n"
    "v_mul_f32 v34, v34, v66\n"
    "v_mul_f32 v35, v35, v66\n"
    "v_accvgpr_write_b32 a32, v32\n"
    "v_accvgpr_write_b32 a33, v33\n"
    "v_accvgpr_write_b32 a34, v34\n"
    "v_accvgpr_write_b32 a35, v35\n"
    "v_accvgpr_read_b32 v32, a40\n"
    "v_accvgpr_read_b32 v33, a41\n"
    "v_accvgpr_read_b32 v34, a42\n"
    "v_accvgpr_read_b32 v35, a43\n"
    "v_mul_f32 v32, v32, v66\n"
    "v_mul_f32 v33, v33, v66\n"
    "v_mul_f32 v34, v34, v66\n"
    "v_mul_f32 v35, v35, v66\n"
    "v_accvgpr_write_b32 a40, v32\n"
    "v_accvgpr_write_b32 a41, v33\n"
    "v_accvgpr_write_b32 a42, v34\n"
    "v_accvgpr_write_b32 a43, v35\n"
    "v_accvgpr_read_b32 v32, a48\n"
    "v_accvgpr_read_b32 v33, a49\n"
    "v_accvgpr_read_b32 v34, a50\n"
    "v_accvgpr_read_b32 v35, a51\n"
    "v_mul_f32 v32, v32, v66\n"
    "v_mul_f32 v33, v33, v66\n"
    "v_mul_f32 v34, v34, v66\n"
    "v_mul_f32 v35, v35, v66\n"
    "v_accvgpr_write_b32 a48, v32\n"
    "v_accvgpr_write_b32 a49, v33\n"
    "v_accvgpr_write_b32 a50, v34\n"
    "v_accvgpr_write_b32 a51, v35\n"
    "v_accvgpr_read_b32 v32, a56\n"
    "v_accvgpr_read_b32 v33, a57\n"
    "v_accvgpr_read_b32 v34, a58\n"
    "v_accvgpr_read_b32 v35, a59\n"
    "v_mul_f32 v32, v32, v66\n"
    "v_mul_f32 v33, v33, v66\n"
    "v_mul_f32 v34, v34, v66\n"
    "v_mul_f32 v35, v35, v66\n"
    "v_accvgpr_write_b32 a56, v32\n"
    "v_accvgpr_write_b32 a57, v33\n"
    "v_accvgpr_write_b32 a58, v34\n"
    "v_accvgpr_write_b32 a59, v35\n"
    "v_accvgpr_read_b32 v32, a64\n"
    "v_accvgpr_read_b32 v33, a65\n"
    "v_accvgpr_read_b32 v34, a66\n"
    "v_accvgpr_read_b32 v35, a67\n"
    "v_mul_f32 v32, v32, v66\n"
    "v_mul_f32 v33, v33, v66\n"
    "v_mul_f32 v34, v34, v66\n"
    "v_mul_f32 v35, v35, v66\n"
    "v_accvgpr_write_b32 a64, v32\n"
    "v_accvgpr_write_b32 a65, v33\n"
    "v_accvgpr_write_b32 a66, v34\n"
    "v_accvgpr_write_b32 a67, v35\n"
    "v_accvgpr_read_b32 v32, a72\n"
    "v_accvgpr_read_b32 v33, a73\n"
    "v_accvgpr_read_b32 v34, a74\n"
    "v_accvgpr_read_b32 v35, a75\n"
    "v_mul_f32 v32, v32, v66\n"
    "v_mul_f32 v33, v33, v66\n"
    "v_mul_f32 v34, v34, v66\n"
    "v_mul_f32 v35, v35, v66\n"
    "v_accvgpr_write_b32 a72, v32\n"
    "v_accvgpr_write_b32 a73, v33\n"
    "v_accvgpr_write_b32 a74, v34\n"
    "v_accvgpr_write_b32 a75, v35\n"
    "v_accvgpr_read_b32 v32, a80\n"
    "v_accvgpr_read_b32 v33, a81\n"
    "v_accvgpr_read_b32 v34, a82\n"
    "v_accvgpr_read_b32 v35, a83\n"
    "v_mul_f32 v32, v32, v66\n"
    "v_mul_f32 v33, v33, v66\n"
    "v_mul_f32 v34, v34, v66\n"
    "v_mul_f32 v35, v35, v66\n"
    "v_accvgpr_write_b32 a80, v32\n"
    "v_accvgpr_write_b32 a81, v33\n"
    "v_accvgpr_write_b32 a82, v34\n"
    "v_accvgpr_write_b32 a83, v35\n"
    "v_accvgpr_read_b32 v32, a88\n"
    "v_accvgpr_read_b32 v33, a89\n"
    "v_accvgpr_read_b32 v34, a90\n"
    "v_accvgpr_read_b32 v35, a91\n"
    "v_mul_f32 v32, v32, v66\n"
    "v_mul_f32 v33, v33, v66\n"
    "v_mul_f32 v34, v34, v66\n"
    "v_mul_f32 v35, v35, v66\n"
    "v_accvgpr_write_b32 a88, v32\n"
    "v_accvgpr_write_b32 a89, v33\n"
    "v_accvgpr_write_b32 a90, v34\n"
    "v_accvgpr_write_b32 a91, v35\n"
    "v_accvgpr_read_b32 v32, a96\n"
    "v_accvgpr_read_b32 v33, a97\n"
    "v_accvgpr_read_b32 v34, a98\n"
    "v_accvgpr_read_b32 v35, a99\n"
    "v_mul_f32 v32, v32, v66\n"
    "v_mul_f32 v33, v33, v66\n"
    "v_mul_f32 v34, v34, v66\n"
    "v_mul_f32 v35, v35, v66\n"
    "v_accvgpr_write_b32 a96, v32\n"
    "v_accvgpr_write_b32 a97, v33\n"
    "v_accvgpr_write_b32 a98, v34\n"
    "v_accvgpr_write_b32 a99, v35\n"
    "v_accvgpr_read_b32 v32, a104\n"
    "v_accvgpr_read_b32 v33, a105\n"
    "v_accvgpr_read_b32 v34, a106\n"
    "v_accvgpr_read_b32 v35, a107\n"
    "v_mul_f32 v32, v32, v66\n"
    "v_mul_f32 v33, v33, v66\n"
    "v_mul_f32 v34, v34, v66\n"
    "v_mul_f32 v35, v35, v66\n"
    "v_accvgpr_write_b32 a104, v32\n"
    "v_accvgpr_write_b32 a105, v33\n"
    "v_accvgpr_write_b32 a106, v34\n"
    "v_accvgpr_write_b32 a107, v35\n"
    "v_accvgpr_read_b32 v32, a112\n"
    "v_accvgpr_read_b32 v33, a113\n"
    "v_accvgpr_read_b32 v34, a114\n"
    "v_accvgpr_read_b32 v35, a115\n"
    "v_mul_f32 v32, v32, v66\n"
    "v_mul_f32 v33, v33, v66\n"
    "v_mul_f32 v34, v34, v66\n"
    "v_mul_f32 v35, v35, v66\n"
    "v_accvgpr_write_b32 a112, v32\n"
    "v_accvgpr_write_b32 a113, v33\n"
    "v_accvgpr_write_b32 a114, v34\n"
    "v_accvgpr_write_b32 a115, v35\n"
    "v_accvgpr_read_b32 v32, a120\n"
    "v_accvgpr_read_b32 v33, a121\n"
    "v_accvgpr_read_b32 v34, a122\n"
    "v_accvgpr_read_b32 v35, a123\n"
    "v_mul_f32 v32, v32, v66\n"
    "v_mul_f32 v33, v33, v66\n"
    "v_mul_f32 v34, v34, v66\n"
    "v_mul_f32 v35, v35, v66\n"
    "v_accvgpr_write_b32 a120, v32\n"
    "v_accvgpr_write_b32 a121, v33\n"
    "v_accvgpr_write_b32 a122, v34\n"
    "v_accvgpr_write_b32 a123, v35\n"
    "s_waitcnt lgkmcnt(2)\n"
    "v_max_f32 v69, v69, v68\n"
    "v_sub_f32 v70, v68, v69\n"
    "v_exp_f32 v70, v70\n"
    "s_nop 0\n"
    "v_mul_f32 v81, v81, v70\n"
    "v_accvgpr_read_b32 v32, a4\n"
    "v_accvgpr_read_b32 v33, a5\n"
    "v_accvgpr_read_b32 v34, a6\n"
    "v_accvgpr_read_b32 v35, a7\n"
    "v_mul_f32 v32, v32, v70\n"
    "v_mul_f32 v33, v33, v70\n"
    "v_mul_f32 v34, v34, v70\n"
    "v_mul_f32 v35, v35, v70\n"
    "v_accvgpr_write_b32 a4, v32\n"
    "v_accvgpr_write_b32 a5, v33\n"
    "v_accvgpr_write_b32 a6, v34\n"
    "v_accvgpr_write_b32 a7, v35\n"
    "v_accvgpr_read_b32 v32, a12\n"
    "v_accvgpr_read_b32 v33, a13\n"
    "v_accvgpr_read_b32 v34, a14\n"
    "v_accvgpr_read_b32 v35, a15\n"
    "v_mul_f32 v32, v32, v70\n"
    "v_mul_f32 v33, v33, v70\n"
    "v_mul_f32 v34, v34, v70\n"
    "v_mul_f32 v35, v35, v70\n"
    "v_accvgpr_write_b32 a12, v32\n"
    "v_accvgpr_write_b32 a13, v33\n"
    "v_accvgpr_write_b32 a14, v34\n"
    "v_accvgpr_write_b32 a15, v35\n"
    "v_accvgpr_read_b32 v32, a20\n"
    "v_accvgpr_read_b32 v33, a21\n"
    "v_accvgpr_read_b32 v34, a22\n"
    "v_accvgpr_read_b32 v35, a23\n"
    "v_mul_f32 v32, v32, v70\n"
    "v_mul_f32 v33, v33, v70\n"
    "v_mul_f32 v34, v34, v70\n"
    "v_mul_f32 v35, v35, v70\n"
    "v_accvgpr_write_b32 a20, v32\n"
    "v_accvgpr_write_b32 a21, v33\n"
    "v_accvgpr_write_b32 a22, v34\n"
    "v_accvgpr_write_b32 a23, v35\n"
    "v_accvgpr_read_b32 v32, a28\n"
    "v_accvgpr_read_b32 v33, a29\n"
    "v_accvgpr_read_b32 v34, a30\n"
    "v_accvgpr_read_b32 v35, a31\n"
    "v_mul_f32 v32, v32, v70\n"
    "v_mul_f32 v33, v33, v70\n"
    "v_mul_f32 v34, v34, v70\n"
    "v_mul_f32 v35, v35, v70\n"
    "v_accvgpr_write_b32 a28, v32\n"
    "v_accvgpr_write_b32 a29, v33\n"
    "v_accvgpr_write_b32 a30, v34\n"
    "v_accvgpr_write_b32 a31, v35\n"
    "v_accvgpr_read_b32 v32, a36\n"
    "v_accvgpr_read_b32 v33, a37\n"
    "v_accvgpr_read_b32 v34, a38\n"
    "v_accvgpr_read_b32 v35, a39\n"
    "v_mul_f32 v32, v32, v70\n"
    "v_mul_f32 v33, v33, v70\n"
    "v_mul_f32 v34, v34, v70\n"
    "v_mul_f32 v35, v35, v70\n"
    "v_accvgpr_write_b32 a36, v32\n"
    "v_accvgpr_write_b32 a37, v33\n"
    "v_accvgpr_write_b32 a38, v34\n"
    "v_accvgpr_write_b32 a39, v35\n"
    "v_accvgpr_read_b32 v32, a44\n"
    "v_accvgpr_read_b32 v33, a45\n"
    "v_accvgpr_read_b32 v34, a46\n"
    "v_accvgpr_read_b32 v35, a47\n"
    "v_mul_f32 v32, v32, v70\n"
    "v_mul_f32 v33, v33, v70\n"
    "v_mul_f32 v34, v34, v70\n"
    "v_mul_f32 v35, v35, v70\n"
    "v_accvgpr_write_b32 a44, v32\n"
    "v_accvgpr_write_b32 a45, v33\n"
    "v_accvgpr_write_b32 a46, v34\n"
    "v_accvgpr_write_b32 a47, v35\n"
    "v_accvgpr_read_b32 v32, a52\n"
    "v_accvgpr_read_b32 v33, a53\n"
    "v_accvgpr_read_b32 v34, a54\n"
    "v_accvgpr_read_b32 v35, a55\n"
    "v_mul_f32 v32, v32, v70\n"
    "v_mul_f32 v33, v33, v70\n"
    "v_mul_f32 v34, v34, v70\n"
    "v_mul_f32 v35, v35, v70\n"
    "v_accvgpr_write_b32 a52, v32\n"
    "v_accvgpr_write_b32 a53, v33\n"
    "v_accvgpr_write_b32 a54, v34\n"
    "v_accvgpr_write_b32 a55, v35\n"
    "v_accvgpr_read_b32 v32, a60\n"
    "v_accvgpr_read_b32 v33, a61\n"
    "v_accvgpr_read_b32 v34, a62\n"
    "v_accvgpr_read_b32 v35, a63\n"
    "v_mul_f32 v32, v32, v70\n"
    "v_mul_f32 v33, v33, v70\n"
    "v_mul_f32 v34, v34, v70\n"
    "v_mul_f32 v35, v35, v70\n"
    "v_accvgpr_write_b32 a60, v32\n"
    "v_accvgpr_write_b32 a61, v33\n"
    "v_accvgpr_write_b32 a62, v34\n"
    "v_accvgpr_write_b32 a63, v35\n"
    "v_accvgpr_read_b32 v32, a68\n"
    "v_accvgpr_read_b32 v33, a69\n"
    "v_accvgpr_read_b32 v34, a70\n"
    "v_accvgpr_read_b32 v35, a71\n"
    "v_mul_f32 v32, v32, v70\n"
    "v_mul_f32 v33, v33, v70\n"
    "v_mul_f32 v34, v34, v70\n"
    "v_mul_f32 v35, v35, v70\n"
    "v_accvgpr_write_b32 a68, v32\n"
    "v_accvgpr_write_b32 a69, v33\n"
    "v_accvgpr_write_b32 a70, v34\n"
    "v_accvgpr_write_b32 a71, v35\n"
    "v_accvgpr_read_b32 v32, a76\n"
    "v_accvgpr_read_b32 v33, a77\n"
    "v_accvgpr_read_b32 v34, a78\n"
    "v_accvgpr_read_b32 v35, a79\n"
    "v_mul_f32 v32, v32, v70\n"
    "v_mul_f32 v33, v33, v70\n"
    "v_mul_f32 v34, v34, v70\n"
    "v_mul_f32 v35, v35, v70\n"
    "v_accvgpr_write_b32 a76, v32\n"
    "v_accvgpr_write_b32 a77, v33\n"
    "v_accvgpr_write_b32 a78, v34\n"
    "v_accvgpr_write_b32 a79, v35\n"
    "v_accvgpr_read_b32 v32, a84\n"
    "v_accvgpr_read_b32 v33, a85\n"
    "v_accvgpr_read_b32 v34, a86\n"
    "v_accvgpr_read_b32 v35, a87\n"
    "v_mul_f32 v32, v32, v70\n"
    "v_mul_f32 v33, v33, v70\n"
    "v_mul_f32 v34, v34, v70\n"
    "v_mul_f32 v35, v35, v70\n"
    "v_accvgpr_write_b32 a84, v32\n"
    "v_accvgpr_write_b32 a85, v33\n"
    "v_accvgpr_write_b32 a86, v34\n"
    "v_accvgpr_write_b32 a87, v35\n"
    "v_accvgpr_read_b32 v32, a92\n"
    "v_accvgpr_read_b32 v33, a93\n"
    "v_accvgpr_read_b32 v34, a94\n"
    "v_accvgpr_read_b32 v35, a95\n"
    "v_mul_f32 v32, v32, v70\n"
    "v_mul_f32 v33, v33, v70\n"
    "v_mul_f32 v34, v34, v70\n"
    "v_mul_f32 v35, v35, v70\n"
    "v_accvgpr_write_b32 a92, v32\n"
    "v_accvgpr_write_b32 a93, v33\n"
    "v_accvgpr_write_b32 a94, v34\n"
    "v_accvgpr_write_b32 a95, v35\n"
    "v_accvgpr_read_b32 v32, a100\n"
    "v_accvgpr_read_b32 v33, a101\n"
    "v_accvgpr_read_b32 v34, a102\n"
    "v_accvgpr_read_b32 v35, a103\n"
    "v_mul_f32 v32, v32, v70\n"
    "v_mul_f32 v33, v33, v70\n"
    "v_mul_f32 v34, v34, v70\n"
    "v_mul_f32 v35, v35, v70\n"
    "v_accvgpr_write_b32 a100, v32\n"
    "v_accvgpr_write_b32 a101, v33\n"
    "v_accvgpr_write_b32 a102, v34\n"
    "v_accvgpr_write_b32 a103, v35\n"
    "v_accvgpr_read_b32 v32, a108\n"
    "v_accvgpr_read_b32 v33, a109\n"
    "v_accvgpr_read_b32 v34, a110\n"
    "v_accvgpr_read_b32 v35, a111\n"
    "v_mul_f32 v32, v32, v70\n"
    "v_mul_f32 v33, v33, v70\n"
    "v_mul_f32 v34, v34, v70\n"
    "v_mul_f32 v35, v35, v70\n"
    "v_accvgpr_write_b32 a108, v32\n"
    "v_accvgpr_write_b32 a109, v33\n"
    "v_accvgpr_write_b32 a110, v34\n"
    "v_accvgpr_write_b32 a111, v35\n"
    "v_accvgpr_read_b32 v32, a116\n"
    "v_accvgpr_read_b32 v33, a117\n"
    "v_accvgpr_read_b32 v34, a118\n"
    "v_accvgpr_read_b32 v35, a119\n"
    "v_mul_f32 v32, v32, v70\n"
    "v_mul_f32 v33, v33, v70\n"
    "v_mul_f32 v34, v34, v70\n"
    "v_mul_f32 v35, v35, v70\n"
    "v_accvgpr_write_b32 a116, v32\n"
    "v_accvgpr_write_b32 a117, v33\n"
    "v_accvgpr_write_b32 a118, v34\n"
    "v_accvgpr_write_b32 a119, v35\n"
    "v_accvgpr_read_b32 v32, a124\n"
    "v_accvgpr_read_b32 v33, a125\n"
    "v_accvgpr_read_b32 v34, a126\n"
    "v_accvgpr_read_b32 v35, a127\n"
    "v_mul_f32 v32, v32, v70\n"
    "v_mul_f32 v33, v33, v70\n"
    "v_mul_f32 v34, v34, v70\n"
    "v_mul_f32 v35, v35, v70\n"
    "v_accvgpr_write_b32 a124, v32\n"
    "v_accvgpr_write_b32 a125, v33\n"
    "v_accvgpr_write_b32 a126, v34\n"
    "v_accvgpr_write_b32 a127, v35\n"
    "s_waitcnt lgkmcnt(1)\n"
    "v_max_f32 v73, v73, v72\n"
    "v_sub_f32 v74, v72, v73\n"
    "v_exp_f32 v74, v74\n"
    "s_nop 0\n"
    "v_mul_f32 v82, v82, v74\n"
    "v_accvgpr_read_b32 v32, a128\n"
    "v_accvgpr_read_b32 v33, a129\n"
    "v_accvgpr_read_b32 v34, a130\n"
    "v_accvgpr_read_b32 v35, a131\n"
    "v_mul_f32 v32, v32, v74\n"
    "v_mul_f32 v33, v33, v74\n"
    "v_mul_f32 v34, v34, v74\n"
    "v_mul_f32 v35, v35, v74\n"
    "v_accvgpr_write_b32 a128, v32\n"
    "v_accvgpr_write_b32 a129, v33\n"
    "v_accvgpr_write_b32 a130, v34\n"
    "v_accvgpr_write_b32 a131, v35\n"
    "v_accvgpr_read_b32 v32, a136\n"
    "v_accvgpr_read_b32 v33, a137\n"
    "v_accvgpr_read_b32 v34, a138\n"
    "v_accvgpr_read_b32 v35, a139\n"
    "v_mul_f32 v32, v32, v74\n"
    "v_mul_f32 v33, v33, v74\n"
    "v_mul_f32 v34, v34, v74\n"
    "v_mul_f32 v35, v35, v74\n"
    "v_accvgpr_write_b32 a136, v32\n"
    "v_accvgpr_write_b32 a137, v33\n"
    "v_accvgpr_write_b32 a138, v34\n"
    "v_accvgpr_write_b32 a139, v35\n"
    "v_accvgpr_read_b32 v32, a144\n"
    "v_accvgpr_read_b32 v33, a145\n"
    "v_accvgpr_read_b32 v34, a146\n"
    "v_accvgpr_read_b32 v35, a147\n"
    "v_mul_f32 v32, v32, v74\n"
    "v_mul_f32 v33, v33, v74\n"
    "v_mul_f32 v34, v34, v74\n"
    "v_mul_f32 v35, v35, v74\n"
    "v_accvgpr_write_b32 a144, v32\n"
    "v_accvgpr_write_b32 a145, v33\n"
    "v_accvgpr_write_b32 a146, v34\n"
    "v_accvgpr_write_b32 a147, v35\n"
    "v_accvgpr_read_b32 v32, a152\n"
    "v_accvgpr_read_b32 v33, a153\n"
    "v_accvgpr_read_b32 v34, a154\n"
    "v_accvgpr_read_b32 v35, a155\n"
    "v_mul_f32 v32, v32, v74\n"
    "v_mul_f32 v33, v33, v74\n"
    "v_mul_f32 v34, v34, v74\n"
    "v_mul_f32 v35, v35, v74\n"
    "v_accvgpr_write_b32 a152, v32\n"
    "v_accvgpr_write_b32 a153, v33\n"
    "v_accvgpr_write_b32 a154, v34\n"
    "v_accvgpr_write_b32 a155, v35\n"
    "v_accvgpr_read_b32 v32, a160\n"
    "v_accvgpr_read_b32 v33, a161\n"
    "v_accvgpr_read_b32 v34, a162\n"
    "v_accvgpr_read_b32 v35, a163\n"
    "v_mul_f32 v32, v32, v74\n"
    "v_mul_f32 v33, v33, v74\n"
    "v_mul_f32 v34, v34, v74\n"
    "v_mul_f32 v35, v35, v74\n"
    "v_accvgpr_write_b32 a160, v32\n"
    "v_accvgpr_write_b32 a161, v33\n"
    "v_accvgpr_write_b32 a162, v34\n"
    "v_accvgpr_write_b32 a163, v35\n"
    "v_accvgpr_read_b32 v32, a168\n"
    "v_accvgpr_read_b32 v33, a169\n"
    "v_accvgpr_read_b32 v34, a170\n"
    "v_accvgpr_read_b32 v35, a171\n"
    "v_mul_f32 v32, v32, v74\n"
    "v_mul_f32 v33, v33, v74\n"
    "v_mul_f32 v34, v34, v74\n"
    "v_mul_f32 v35, v35, v74\n"
    "v_accvgpr_write_b32 a168, v32\n"
    "v_accvgpr_write_b32 a169, v33\n"
    "v_accvgpr_write_b32 a170, v34\n"
    "v_accvgpr_write_b32 a171, v35\n"
    "v_accvgpr_read_b32 v32, a176\n"
    "v_accvgpr_read_b32 v33, a177\n"
    "v_accvgpr_read_b32 v34, a178\n"
    "v_accvgpr_read_b32 v35, a179\n"
    "v_mul_f32 v32, v32, v74\n"
    "v_mul_f32 v33, v33, v74\n"
    "v_mul_f32 v34, v34, v74\n"
    "v_mul_f32 v35, v35, v74\n"
    "v_accvgpr_write_b32 a176, v32\n"
    "v_accvgpr_write_b32 a177, v33\n"
    "v_accvgpr_write_b32 a178, v34\n"
    "v_accvgpr_write_b32 a179, v35\n"
    "v_accvgpr_read_b32 v32, a184\n"
    "v_accvgpr_read_b32 v33, a185\n"
    "v_accvgpr_read_b32 v34, a186\n"
    "v_accvgpr_read_b32 v35, a187\n"
    "v_mul_f32 v32, v32, v74\n"
    "v_mul_f32 v33, v33, v74\n"
    "v_mul_f32 v34, v34, v74\n"
    "v_mul_f32 v35, v35, v74\n"
    "v_accvgpr_write_b32 a184, v32\n"
    "v_accvgpr_write_b32 a185, v33\n"
    "v_accvgpr_write_b32 a186, v34\n"
    "v_accvgpr_write_b32 a187, v35\n"
    "v_accvgpr_read_b32 v32, a192\n"
    "v_accvgpr_read_b32 v33, a193\n"
    "v_accvgpr_read_b32 v34, a194\n"
    "v_accvgpr_read_b32 v35, a195\n"
    "v_mul_f32 v32, v32, v74\n"
    "v_mul_f32 v33, v33, v74\n"
    "v_mul_f32 v34, v34, v74\n"
    "v_mul_f32 v35, v35, v74\n"
    "v_accvgpr_write_b32 a192, v32\n"
    "v_accvgpr_write_b32 a193, v33\n"
    "v_accvgpr_write_b32 a194, v34\n"
    "v_accvgpr_write_b32 a195, v35\n"
    "v_accvgpr_read_b32 v32, a200\n"
    "v_accvgpr_read_b32 v33, a201\n"
    "v_accvgpr_read_b32 v34, a202\n"
    "v_accvgpr_read_b32 v35, a203\n"
    "v_mul_f32 v32, v32, v74\n"
    "v_mul_f32 v33, v33, v74\n"
    "v_mul_f32 v34, v34, v74\n"
    "v_mul_f32 v35, v35, v74\n"
    "v_accvgpr_write_b32 a200, v32\n"
    "v_accvgpr_write_b32 a201, v33\n"
    "v_accvgpr_write_b32 a202, v34\n"
    "v_accvgpr_write_b32 a203, v35\n"
    "v_accvgpr_read_b32 v32, a208\n"
    "v_accvgpr_read_b32 v33, a209\n"
    "v_accvgpr_read_b32 v34, a210\n"
    "v_accvgpr_read_b32 v35, a211\n"
    "v_mul_f32 v32, v32, v74\n"
    "v_mul_f32 v33, v33, v74\n"
    "v_mul_f32 v34, v34, v74\n"
    "v_mul_f32 v35, v35, v74\n"
    "v_accvgpr_write_b32 a208, v32\n"
    "v_accvgpr_write_b32 a209, v33\n"
    "v_accvgpr_write_b32 a210, v34\n"
    "v_accvgpr_write_b32 a211, v35\n"
    "v_accvgpr_read_b32 v32, a216\n"
    "v_accvgpr_read_b32 v33, a217\n"
    "v_accvgpr_read_b32 v34, a218\n"
    "v_accvgpr_read_b32 v35, a219\n"
    "v_mul_f32 v32, v32, v74\n"
    "v_mul_f32 v33, v33, v74\n"
    "v_mul_f32 v34, v34, v74\n"
    "v_mul_f32 v35, v35, v74\n"
    "v_accvgpr_write_b32 a216, v32\n"
    "v_accvgpr_write_b32 a217, v33\n"
    "v_accvgpr_write_b32 a218, v34\n"
    "v_accvgpr_write_b32 a219, v35\n"
    "v_accvgpr_read_b32 v32, a224\n"
    "v_accvgpr_read_b32 v33, a225\n"
    "v_accvgpr_read_b32 v34, a226\n"
    "v_accvgpr_read_b32 v35, a227\n"
    "v_mul_f32 v32, v32, v74\n"
    "v_mul_f32 v33, v33, v74\n"
    "v_mul_f32 v34, v34, v74\n"
    "v_mul_f32 v35, v35, v74\n"
    "v_accvgpr_write_b32 a224, v32\n"
    "v_accvgpr_write_b32 a225, v33\n"
    "v_accvgpr_write_b32 a226, v34\n"
    "v_accvgpr_write_b32 a227, v35\n"
    "v_accvgpr_read_b32 v32, a232\n"
    "v_accvgpr_read_b32 v33, a233\n"
    "v_accvgpr_read_b32 v34, a234\n"
    "v_accvgpr_read_b32 v35, a235\n"
    "v_mul_f32 v32, v32, v74\n"
    "v_mul_f32 v33, v33, v74\n"
    "v_mul_f32 v34, v34, v74\n"
    "v_mul_f32 v35, v35, v74\n"
    "v_accvgpr_write_b32 a232, v32\n"
    "v_accvgpr_write_b32 a233, v33\n"
    "v_accvgpr_write_b32 a234, v34\n"
    "v_accvgpr_write_b32 a235, v35\n"
    "v_accvgpr_read_b32 v32, a240\n"
    "v_accvgpr_read_b32 v33, a241\n"
    "v_accvgpr_read_b32 v34, a242\n"
    "v_accvgpr_read_b32 v35, a243\n"
    "v_mul_f32 v32, v32, v74\n"
    "v_mul_f32 v33, v33, v74\n"
    "v_mul_f32 v34, v34, v74\n"
    "v_mul_f32 v35, v35, v74\n"
    "v_accvgpr_write_b32 a240, v32\n"
    "v_accvgpr_write_b32 a241, v33\n"
    "v_accvgpr_write_b32 a242, v34\n"
    "v_accvgpr_write_b32 a243, v35\n"
    "v_accvgpr_read_b32 v32, a248\n"
    "v_accvgpr_read_b32 v33, a249\n"
    "v_accvgpr_read_b32 v34, a250\n"
    "v_accvgpr_read_b32 v35, a251\n"
    "v_mul_f32 v32, v32, v74\n"
    "v_mul_f32 v33, v33, v74\n"
    "v_mul_f32 v34, v34, v74\n"
    "v_mul_f32 v35, v35, v74\n"
    "v_accvgpr_write_b32 a248, v32\n"
    "v_accvgpr_write_b32 a249, v33\n"
    "v_accvgpr_write_b32 a250, v34\n"
    "v_accvgpr_write_b32 a251, v35\n"
    "s_waitcnt lgkmcnt(0)\n"
    "v_max_f32 v77, v77, v76\n"
    "v_sub_f32 v78, v76, v77\n"
    "v_exp_f32 v78, v78\n"
    "s_nop 0\n"
    "v_mul_f32 v83, v83, v78\n"
    "v_accvgpr_read_b32 v32, a132\n"
    "v_accvgpr_read_b32 v33, a133\n"
    "v_accvgpr_read_b32 v34, a134\n"
    "v_accvgpr_read_b32 v35, a135\n"
    "v_mul_f32 v32, v32, v78\n"
    "v_mul_f32 v33, v33, v78\n"
    "v_mul_f32 v34, v34, v78\n"
    "v_mul_f32 v35, v35, v78\n"
    "v_accvgpr_write_b32 a132, v32\n"
    "v_accvgpr_write_b32 a133, v33\n"
    "v_accvgpr_write_b32 a134, v34\n"
    "v_accvgpr_write_b32 a135, v35\n"
    "v_accvgpr_read_b32 v32, a140\n"
    "v_accvgpr_read_b32 v33, a141\n"
    "v_accvgpr_read_b32 v34, a142\n"
    "v_accvgpr_read_b32 v35, a143\n"
    "v_mul_f32 v32, v32, v78\n"
    "v_mul_f32 v33, v33, v78\n"
    "v_mul_f32 v34, v34, v78\n"
    "v_mul_f32 v35, v35, v78\n"
    "v_accvgpr_write_b32 a140, v32\n"
    "v_accvgpr_write_b32 a141, v33\n"
    "v_accvgpr_write_b32 a142, v34\n"
    "v_accvgpr_write_b32 a143, v35\n"
    "v_accvgpr_read_b32 v32, a148\n"
    "v_accvgpr_read_b32 v33, a149\n"
    "v_accvgpr_read_b32 v34, a150\n"
    "v_accvgpr_read_b32 v35, a151\n"
    "v_mul_f32 v32, v32, v78\n"
    "v_mul_f32 v33, v33, v78\n"
    "v_mul_f32 v34, v34, v78\n"
    "v_mul_f32 v35, v35, v78\n"
    "v_accvgpr_write_b32 a148, v32\n"
    "v_accvgpr_write_b32 a149, v33\n"
    "v_accvgpr_write_b32 a150, v34\n"
    "v_accvgpr_write_b32 a151, v35\n"
    "v_accvgpr_read_b32 v32, a156\n"
    "v_accvgpr_read_b32 v33, a157\n"
    "v_accvgpr_read_b32 v34, a158\n"
    "v_accvgpr_read_b32 v35, a159\n"
    "v_mul_f32 v32, v32, v78\n"
    "v_mul_f32 v33, v33, v78\n"
    "v_mul_f32 v34, v34, v78\n"
    "v_mul_f32 v35, v35, v78\n"
    "v_accvgpr_write_b32 a156, v32\n"
    "v_accvgpr_write_b32 a157, v33\n"
    "v_accvgpr_write_b32 a158, v34\n"
    "v_accvgpr_write_b32 a159, v35\n"
    "v_accvgpr_read_b32 v32, a164\n"
    "v_accvgpr_read_b32 v33, a165\n"
    "v_accvgpr_read_b32 v34, a166\n"
    "v_accvgpr_read_b32 v35, a167\n"
    "v_mul_f32 v32, v32, v78\n"
    "v_mul_f32 v33, v33, v78\n"
    "v_mul_f32 v34, v34, v78\n"
    "v_mul_f32 v35, v35, v78\n"
    "v_accvgpr_write_b32 a164, v32\n"
    "v_accvgpr_write_b32 a165, v33\n"
    "v_accvgpr_write_b32 a166, v34\n"
    "v_accvgpr_write_b32 a167, v35\n"
    "v_accvgpr_read_b32 v32, a172\n"
    "v_accvgpr_read_b32 v33, a173\n"
    "v_accvgpr_read_b32 v34, a174\n"
    "v_accvgpr_read_b32 v35, a175\n"
    "v_mul_f32 v32, v32, v78\n"
    "v_mul_f32 v33, v33, v78\n"
    "v_mul_f32 v34, v34, v78\n"
    "v_mul_f32 v35, v35, v78\n"
    "v_accvgpr_write_b32 a172, v32\n"
    "v_accvgpr_write_b32 a173, v33\n"
    "v_accvgpr_write_b32 a174, v34\n"
    "v_accvgpr_write_b32 a175, v35\n"
    "v_accvgpr_read_b32 v32, a180\n"
    "v_accvgpr_read_b32 v33, a181\n"
    "v_accvgpr_read_b32 v34, a182\n"
    "v_accvgpr_read_b32 v35, a183\n"
    "v_mul_f32 v32, v32, v78\n"
    "v_mul_f32 v33, v33, v78\n"
    "v_mul_f32 v34, v34, v78\n"
    "v_mul_f32 v35, v35, v78\n"
    "v_accvgpr_write_b32 a180, v32\n"
    "v_accvgpr_write_b32 a181, v33\n"
    "v_accvgpr_write_b32 a182, v34\n"
    "v_accvgpr_write_b32 a183, v35\n"
    "v_accvgpr_read_b32 v32, a188\n"
    "v_accvgpr_read_b32 v33, a189\n"
    "v_accvgpr_read_b32 v34, a190\n"
    "v_accvgpr_read_b32 v35, a191\n"
    "v_mul_f32 v32, v32, v78\n"
    "v_mul_f32 v33, v33, v78\n"
    "v_mul_f32 v34, v34, v78\n"
    "v_mul_f32 v35, v35, v78\n"
    "v_accvgpr_write_b32 a188, v32\n"
    "v_accvgpr_write_b32 a189, v33\n"
    "v_accvgpr_write_b32 a190, v34\n"
    "v_accvgpr_write_b32 a191, v35\n"
    "v_accvgpr_read_b32 v32, a196\n"
    "v_accvgpr_read_b32 v33, a197\n"
    "v_accvgpr_read_b32 v34, a198\n"
    "v_accvgpr_read_b32 v35, a199\n"
    "v_mul_f32 v32, v32, v78\n"
    "v_mul_f32 v33, v33, v78\n"
    "v_mul_f32 v34, v34, v78\n"
    "v_mul_f32 v35, v35, v78\n"
    "v_accvgpr_write_b32 a196, v32\n"
    "v_accvgpr_write_b32 a197, v33\n"
    "v_accvgpr_write_b32 a198, v34\n"
    "v_accvgpr_write_b32 a199, v35\n"
    "v_accvgpr_read_b32 v32, a204\n"
    "v_accvgpr_read_b32 v33, a205\n"
    "v_accvgpr_read_b32 v34, a206\n"
    "v_accvgpr_read_b32 v35, a207\n"
    "v_mul_f32 v32, v32, v78\n"
    "v_mul_f32 v33, v33, v78\n"
    "v_mul_f32 v34, v34, v78\n"
    "v_mul_f32 v35, v35, v78\n"
    "v_accvgpr_write_b32 a204, v32\n"
    "v_accvgpr_write_b32 a205, v33\n"
    "v_accvgpr_write_b32 a206, v34\n"
    "v_accvgpr_write_b32 a207, v35\n"
    "v_accvgpr_read_b32 v32, a212\n"
    "v_accvgpr_read_b32 v33, a213\n"
    "v_accvgpr_read_b32 v34, a214\n"
    "v_accvgpr_read_b32 v35, a215\n"
    "v_mul_f32 v32, v32, v78\n"
    "v_mul_f32 v33, v33, v78\n"
    "v_mul_f32 v34, v34, v78\n"
    "v_mul_f32 v35, v35, v78\n"
    "v_accvgpr_write_b32 a212, v32\n"
    "v_accvgpr_write_b32 a213, v33\n"
    "v_accvgpr_write_b32 a214, v34\n"
    "v_accvgpr_write_b32 a215, v35\n"
    "v_accvgpr_read_b32 v32, a220\n"
    "v_accvgpr_read_b32 v33, a221\n"
    "v_accvgpr_read_b32 v34, a222\n"
    "v_accvgpr_read_b32 v35, a223\n"
    "v_mul_f32 v32, v32, v78\n"
    "v_mul_f32 v33, v33, v78\n"
    "v_mul_f32 v34, v34, v78\n"
    "v_mul_f32 v35, v35, v78\n"
    "v_accvgpr_write_b32 a220, v32\n"
    "v_accvgpr_write_b32 a221, v33\n"
    "v_accvgpr_write_b32 a222, v34\n"
    "v_accvgpr_write_b32 a223, v35\n"
    "v_accvgpr_read_b32 v32, a228\n"
    "v_accvgpr_read_b32 v33, a229\n"
    "v_accvgpr_read_b32 v34, a230\n"
    "v_accvgpr_read_b32 v35, a231\n"
    "v_mul_f32 v32, v32, v78\n"
    "v_mul_f32 v33, v33, v78\n"
    "v_mul_f32 v34, v34, v78\n"
    "v_mul_f32 v35, v35, v78\n"
    "v_accvgpr_write_b32 a228, v32\n"
    "v_accvgpr_write_b32 a229, v33\n"
    "v_accvgpr_write_b32 a230, v34\n"
    "v_accvgpr_write_b32 a231, v35\n"
    "v_accvgpr_read_b32 v32, a236\n"
    "v_accvgpr_read_b32 v33, a237\n"
    "v_accvgpr_read_b32 v34, a238\n"
    "v_accvgpr_read_b32 v35, a239\n"
    "v_mul_f32 v32, v32, v78\n"
    "v_mul_f32 v33, v33, v78\n"
    "v_mul_f32 v34, v34, v78\n"
    "v_mul_f32 v35, v35, v78\n"
    "v_accvgpr_write_b32 a236, v32\n"
    "v_accvgpr_write_b32 a237, v33\n"
    "v_accvgpr_write_b32 a238, v34\n"
    "v_accvgpr_write_b32 a239, v35\n"
    "v_accvgpr_read_b32 v32, a244\n"
    "v_accvgpr_read_b32 v33, a245\n"
    "v_accvgpr_read_b32 v34, a246\n"
    "v_accvgpr_read_b32 v35, a247\n"
    "v_mul_f32 v32, v32, v78\n"
    "v_mul_f32 v33, v33, v78\n"
    "v_mul_f32 v34, v34, v78\n"
    "v_mul_f32 v35, v35, v78\n"
    "v_accvgpr_write_b32 a244, v32\n"
    "v_accvgpr_write_b32 a245, v33\n"
    "v_accvgpr_write_b32 a246, v34\n"
    "v_accvgpr_write_b32 a247, v35\n"
    "v_accvgpr_read_b32 v32, a252\n"
    "v_accvgpr_read_b32 v33, a253\n"
    "v_accvgpr_read_b32 v34, a254\n"
    "v_accvgpr_read_b32 v35, a255\n"
    "v_mul_f32 v32, v32, v78\n"
    "v_mul_f32 v33, v33, v78\n"
    "v_mul_f32 v34, v34, v78\n"
    "v_mul_f32 v35, v35, v78\n"
    "v_accvgpr_write_b32 a252, v32\n"
    "v_accvgpr_write_b32 a253, v33\n"
    "v_accvgpr_write_b32 a254, v34\n"
    "v_accvgpr_write_b32 a255, v35\n"
    "s_barrier\n"
    "v_mbcnt_lo_u32_b32 v127, -1, 0\n"
    "v_mbcnt_hi_u32_b32 v127, -1, v127\n"
    "v_lshrrev_b32 v120, 3, v127\n"
    "v_mul_u32_u24 v64, 896, v120\n"
    "v_bfe_u32 v120, v127, 4, 2\n"
    "v_lshlrev_b32 v120, 1, v120\n"
    "v_and_b32 v121, 7, v127\n"
    "v_xor_b32 v120, v120, v121\n"
    "v_lshl_add_u32 v64, v120, 4, v64\n"
    "s_mul_i32 s86, s69, 7168\n"
    "s_add_u32 s86, s86, 3168\n"
    "v_add_u32 v64, s86, v64\n"
    "v_add_u32 v65, 0xfffffc80, v64\n"
    "v_add_u32 v66, 0xfffff900, v64\n"
    "v_add_u32 v67, 0xfffff580, v64\n"
    "v_add_u32 v68, 0x200, v64\n"
    "v_add_u32 v69, 0xfffffe80, v64\n"
    "v_add_u32 v70, 0xfffffb00, v64\n"
    "s_mul_i32 s100, s69, 7168\n"
    "v_mbcnt_lo_u32_b32 v76, -1, 0\n"
    "v_mbcnt_hi_u32_b32 v76, -1, v76\n"
    "v_and_b32 v76, 7, v76\n"
    "v_lshlrev_b32 v76, 2, v76\n"
    "s_lshl_b32 s86, s71, 8\n"
    "v_add_u32 v76, s86, v76\n"
    "global_load_dword v78, v76, s[36:37] offset:128 sc1\n"
    "s_add_u32 m0, s100, 0\n"
    "s_mov_b32 s75, 0\n"
    "buffer_load_dwordx4 v64, s[60:63], s75 offen offset:0 lds\n"
    "buffer_load_dwordx4 v65, s[60:63], s75 offen offset:1024 lds\n"
    "buffer_load_dwordx4 v66, s[60:63], s75 offen offset:2048 lds\n"
    "buffer_load_dwordx4 v67, s[60:63], s75 offen offset:3072 lds\n"
    "s_add_u32 m0, s100, 4096\n"
    "s_nop 0\n"
    "buffer_load_dwordx4 v68, s[60:63], s75 offen offset:0 lds\n"
    "buffer_load_dwordx4 v69, s[60:63], s75 offen offset:1024 lds\n"
    "buffer_load_dwordx4 v70, s[60:63], s75 offen offset:2048 lds\n"
    "s_add_u32 m0, s100, 28672\n"
    "s_mov_b32 s76, 28672\n"
    "buffer_load_dwordx4 v64, s[60:63], s76 offen offset:0 lds\n"
    "buffer_load_dwordx4 v65, s[60:63], s76 offen offset:1024 lds\n"
    "buffer_load_dwordx4 v66, s[60:63], s76 offen offset:2048 lds\n"
    "buffer_load_dwordx4 v67, s[60:63], s76 offen offset:3072 lds\n"
    "s_add_u32 m0, s100, 32768\n"
    "s_nop 0\n"
    "buffer_load_dwordx4 v68, s[60:63], s76 offen offset:0 lds\n"
    "buffer_load_dwordx4 v69, s[60:63], s76 offen offset:1024 lds\n"
    "buffer_load_dwordx4 v70, s[60:63], s76 offen offset:2048 lds\n"
    "v_mov_b32 v120, v80\n"
    "s_nop 1\n"
    "v_mov_b32 v121, v81\n"
    "s_nop 1\n"
    "v_mov_b32 v122, v82\n"
    "s_nop 1\n"
    "v_mov_b32 v123, v83\n"
    "s_nop 1\n"
    "v_permlane32_swap_b32 v80, v120\n"
    "s_nop 1\n"
    "v_permlane32_swap_b32 v81, v121\n"
    "s_nop 1\n"
    "v_permlane32_swap_b32 v82, v122\n"
    "s_nop 1\n"
    "v_permlane32_swap_b32 v83, v123\n"
    "s_nop 1\n"
    "v_add_f32 v80, v80, v120\n"
    "s_nop 1\n"
    "v_add_f32 v81, v81, v121\n"
    "s_nop 1\n"
    "v_add_f32 v82, v82, v122\n"
    "s_nop 1\n"
    "v_add_f32 v83, v83, v123\n"
    "s_nop 1\n"
    "v_mov_b32 v120, v80\n"
    "s_nop 1\n"
    "v_mov_b32 v121, v81\n"
    "s_nop 1\n"
    "v_mov_b32 v122, v82\n"
    "s_nop 1\n"
    "v_mov_b32 v123, v83\n"
    "s_nop 1\n"
    "v_permlane16_swap_b32 v80, v120\n"
    "s_nop 1\n"
    "v_permlane16_swap_b32 v81, v121\n"
    "s_nop 1\n"
    "v_permlane16_swap_b32 v82, v122\n"
    "s_nop 1\n"
    "v_permlane16_swap_b32 v83, v123\n"
    "s_nop 1\n"
    "v_add_f32 v80, v80, v120\n"
    "s_nop 1\n"
    "v_add_f32 v81, v81, v121\n"
    "s_nop 1\n"
    "v_add_f32 v82, v82, v122\n"
    "s_nop 1\n"
    "v_add_f32 v83, v83, v123\n"
    "s_nop 1\n"
    "v_mbcnt_lo_u32_b32 v71, -1, 0\n"
    "s_nop 1\n"
    "v_mbcnt_hi_u32_b32 v71, -1, v71\n"
    "s_nop 1\n"
    "v_and_b32 v71, 15, v71\n"
    "s_nop 1\n"
    "s_lshl_b32 s86, s69, 8\n"
    "s_nop 1\n"
    "s_add_u32 s86, s86, 136192\n"
    "s_nop 1\n"
    "v_lshl_add_u32 v71, v71, 2, s86\n"
    "s_nop 1\n"
    "ds_write_b32 v71, v80 offset:0\n"
    "s_nop 1\n"
    "ds_write_b32 v71, v81 offset:64\n"
    "s_nop 1\n"
    "ds_write_b32 v71, v82 offset:128\n"
    "s_nop 1\n"
    "ds_write_b32 v71, v83 offset:192\n"
    "s_nop 1\n"
    "v_mov_b32 v122, 0x5f800000\n"
    "s_nop 1\n"
    "v_cmp_nlt_f32 vcc, v80, v122\n"
    "s_nop 1\n"
    "s_mov_b64 s[86:87], vcc\n"
    "s_nop 1\n"
    "v_cmp_nlt_f32 vcc, v81, v122\n"
    "s_nop 1\n"
    "s_or_b64 s[86:87], s[86:87], vcc\n"
    "s_nop 1\n"
    "v_cmp_nlt_f32 vcc, v82, v122\n"
    "s_nop 1\n"
    "s_or_b64 s[86:87], s[86:87], vcc\n"
    "s_nop 1\n"
    "v_cmp_nlt_f32 vcc, v83, v122\n"
    "s_nop 1\n"
    "s_or_b64 s[86:87], s[86:87], vcc\n"
    "s_nop 1\n"
    "s_cmp_lg_u64 s[86:87], 0\n"
    "s_nop 1\n"
    "s_cselect_b32 s86, 1, 0\n"
    "s_nop 1\n"
    "v_mov_b32 v122, s86\n"
    "s_nop 1\n"
    "s_lshl_b32 s87, s69, 2\n"
    "s_nop 1\n"
    "s_add_u32 s87, s87, 137216\n"
    "s_nop 1\n"
    "v_mov_b32 v123, s87\n"
    "s_nop 1\n"
    "ds_write_b32 v123, v122\n"
    "s_nop 1\n"
    "v_mbcnt_lo_u32_b32 v77, -1, 0\n"
    "v_mbcnt_hi_u32_b32 v77, -1, v77\n"
    "s_lshl_b32 s88, s69, 13\n"
    "s_add_u32 s88, s88, 57344\n"
    "v_lshl_add_u32 v74, v77, 4, s88\n"
    "s_xor_b32 s89, s69, 2\n"
    "s_lshl_b32 s89, s89, 13\n"
    "s_add_u32 s89, s89, 57344\n"
    "v_lshl_add_u32 v75, v77, 4, s89\n"
    "ds_write_b128 v74, a[128:131] offset:0\n"
    "ds_write_b128 v74, a[132:135] offset:1024\n"
    "ds_write_b128 v74, a[136:139] offset:2048\n"
    "ds_write_b128 v74, a[140:143] offset:3072\n"
    "ds_write_b128 v74, a[144:147] offset:4096\n"
    "ds_write_b128 v74, a[148:151] offset:5120\n"
    "ds_write_b128 v74, a[152:155] offset:6144\n"
    "ds_write_b128 v74, a[156:159] offset:7168\n"
    "s_mov_b32 s91, 1\n"
    "s_waitcnt vmcnt(0)\n"
    "s_branch .Lejoin\n"
    ".Lspiny:\n"
    "s_mov_b32 s85, 4194304\n"
    ".Lspiny1:\n"
    "s_sleep 1\n"
    "global_load_dword v78, v76, s[36:37] offset:128 sc1\n"
    "s_waitcnt vmcnt(0)\n"
    "v_cmp_eq_u32 vcc, v78, v42\n"
    "s_cmp_eq_u64 vcc, -1\n"
    "s_cbranch_scc1 .Lrety\n"
    "s_sub_u32 s85, s85, 1\n"
    "s_cbranch_scc0 .Lspiny1\n"
    "s_branch .Lrety\n"
    ".Lend:\n"
    :: "s"(ka), "s"(wg), "s"(wv)
    : "v0", "v1", "v2", "v3", "v4", "v5", "v6", "v7", "v8", "v9", "v10", "v11", "v12", "v13", "v14", "v15", "v16", "v17", "v18", "v19", "v20", "v21", "v22", "v23", "v24", "v25", "v26", "v27", "v28", "v29", "v30", "v31", "v32", "v33", "v34", "v35", "v36", "v37", "v38", "v39", "v40", "v41", "v42", "v43", "v44", "v45", "v46", "v47", "v48", "v49", "v50", "v51", "v52", "v53", "v54", "v55", "v56", "v57", "v58", "v59", "v60", "v61", "v62", "v63", "v64", "v65", "v66", "v67", "v68", "v69", "v70", "v71", "v72", "v73", "v74", "v75", "v76", "v77", "v78", "v79", "v80", "v81", "v82", "v83", "v84", "v85", "v86", "v87", "v88", "v89", "v90", "v91", "v92", "v93", "v94", "v95", "v96", "v97", "v98", "v99", "v100", "v101", "v102", "v103", "v104", "v105", "v106", "v107", "v108", "v109", "v110", "v111", "v112", "v113", "v114", "v115", "v116", "v117", "v118", "v119", "v120", "v121", "v122", "v123", "v124", "v125", "v126", "v127", "v128", "v129", "v130", "v131", "v132", "v133", "v134", "v135", "v136", "v137", "v138", "v139", "v140", "v141", "v142", "v143", "v144", "v145", "v146", "v147", "v148", "v149", "v150", "v151", "v152", "v153", "v154", "v155", "v156", "v157", "v158", "v159", "v160", "v161", "v162", "v163", "v164", "v165", "v166", "v167", "v168", "v169", "v170", "v171", "v172", "v173", "v174", "v175", "v176", "v177", "v178", "v179", "v180", "v181", "v182", "v183", "v184", "v185", "v186", "v187", "v188", "v189", "v190", "v191", "v192", "v193", "v194", "v195", "v196", "v197", "v198", "v199", "v200", "v201", "v202", "v203", "v204", "v205", "v206", "v207", "v208", "v209", "v210", "v211", "v212", "v213", "v214", "v215", "v216", "v217", "v218", "v219", "v220", "v221", "v222", "v223", "v224", "v225", "v226", "v227", "v228", "v229", "v230", "v231", "v232", "v233", "v234", "v235", "v236", "v237", "v238", "v239", "v240", "v241", "v242", "v243", "v244", "v245", "v246", "v247", "v248", "v249", "v250", "v251", "v252", "v253", "v254", "v255", "a0", "a1", "a2", "a3", "a4", "a5", "a6", "a7", "a8", "a9", "a10", "a11", "a12", "a13", "a14", "a15", "a16", "a17", "a18", "a19", "a20", "a21", "a22", "a23", "a24", "a25", "a26", "a27", "a28", "a29", "a30", "a31", "a32", "a33", "a34", "a35", "a36", "a37", "a38", "a39", "a40", "a41", "a42", "a43", "a44", "a45", "a46", "a47", "a48", "a49", "a50", "a51", "a52", "a53", "a54", "a55", "a56", "a57", "a58", "a59", "a60", "a61", "a62", "a63", "a64", "a65", "a66", "a67", "a68", "a69", "a70", "a71", "a72", "a73", "a74", "a75", "a76", "a77", "a78", "a79", "a80", "a81", "a82", "a83", "a84", "a85", "a86", "a87", "a88", "a89", "a90", "a91", "a92", "a93", "a94", "a95", "a96", "a97", "a98", "a99", "a100", "a101", "a102", "a103", "a104", "a105", "a106", "a107", "a108", "a109", "a110", "a111", "a112", "a113", "a114", "a115", "a116", "a117", "a118", "a119", "a120", "a121", "a122", "a123", "a124", "a125", "a126", "a127", "a128", "a129", "a130", "a131", "a132", "a133", "a134", "a135", "a136", "a137", "a138", "a139", "a140", "a141", "a142", "a143", "a144", "a145", "a146", "a147", "a148", "a149", "a150", "a151", "a152", "a153", "a154", "a155", "a156", "a157", "a158", "a159", "a160", "a161", "a162", "a163", "a164", "a165", "a166", "a167", "a168", "a169", "a170", "a171", "a172", "a173", "a174", "a175", "a176", "a177", "a178", "a179", "a180", "a181", "a182", "a183", "a184", "a185", "a186", "a187", "a188", "a189", "a190", "a191", "a192", "a193", "a194", "a195", "a196", "a197", "a198", "a199", "a200", "a201", "a202", "a203", "a204", "a205", "a206", "a207", "a208", "a209", "a210", "a211", "a212", "a213", "a214", "a215", "a216", "a217", "a218", "a219", "a220", "a221", "a222", "a223", "a224", "a225", "a226", "a227", "a228", "a229", "a230", "a231", "a232", "a233", "a234", "a235", "a236", "a237", "a238", "a239", "a240", "a241", "a242", "a243", "a244", "a245", "a246", "a247", "a248", "a249", "a250", "a251", "a252", "a253", "a254", "a255", "s16", "s17", "s18", "s19", "s20", "s21", "s22", "s23", "s24", "s25", "s26", "s27", "s28", "s29", "s30", "s31", "s32", "s33", "s34", "s35", "s36", "s37", "s38", "s39", "s40", "s41", "s42", "s43", "s44", "s45", "s46", "s47", "s48", "s49", "s50", "s51", "s52", "s53", "s54", "s55", "s56", "s57", "s58", "s59", "s60", "s61", "s62", "s63", "s64", "s65", "s66", "s67", "s68", "s69", "s70", "s71", "s72", "s73", "s74", "s75", "s76", "s77", "s78", "s79", "s80", "s81", "s82", "s83", "s84", "s85", "s86", "s87", "s88", "s89", "s90", "s91", "s92", "s93", "s94", "s95", "s96", "s97", "s98", "s99", "s100", "s101", "vcc", "scc", "m0", "memory");
}

extern "C" void run_dbg(void* const* d_in, void* d_out, void* d_ws, void* dbg, hipStream_t stream) {
  hipLaunchKernelGGL(fused_main_kernel, dim3(256), dim3(256), 0, stream, d_in[0], d_in[1], d_in[2], d_in[3], d_in[4], d_in[6], d_in[7], d_in[8], d_in[9], d_out, d_ws, dbg);
}
extern "C" void kernel_launch(void* const* d_in, const int* in_sizes, int n_in, void* d_out, int out_size, void* d_ws, size_t ws_size, hipStream_t stream) {
  run_dbg(d_in, d_out, d_ws, d_ws, stream);
}
```
